```python
import torch

def _softmax(x, axis=-1):
    return torch.softmax(x, dim=axis)
def _gelu(x):
    return torch.nn.functional.gelu(x, approximate="tanh")
def _silu(x):
    return torch.nn.functional.silu(x)
def _sigmoid(x):
    return torch.sigmoid(x)
def _layernorm(x, axis=-1, eps=1e-5):
    m = x.mean(dim=axis, keepdim=True)
    v = x.var(dim=axis, keepdim=True, unbiased=False)
    return (x - m) / torch.sqrt(v + eps)
def _scatter_add(tgt, idx, src, axis=0):
    return tgt.clone().index_add_(axis, idx, src)
def _segment_sum(src, seg, n):
    out = torch.zeros((n,) + src.shape[1:], dtype=src.dtype)
    return out.index_add_(0, seg, src)
def _pad(x, pw):
    flat = tuple(v for pair in reversed(pw) for v in pair)
    return torch.nn.functional.pad(x, flat)

kernel_name = 'gated_mix__ssm_conv_s4d__dense_activation_squared_relu__prompt'

def setup_inputs():

    _w = (lambda a: (lambda t: t.to(torch.bfloat16) if t.is_floating_point() else t)(torch.as_tensor(a)))
    def _A_setup():
        Bsz, L, D, N = 1, 2048, 512, 128
        return {
            "u": _w((lambda *s: torch.randn(*s, dtype=torch.bfloat16) * 0.05)(Bsz, L, D)),
            "log_A_real": _w((lambda *s: torch.randn(*s, dtype=torch.bfloat16) * 6.4)(D, N) * 0.5),
            "A_imag": _w((lambda *s: torch.randn(*s, dtype=torch.bfloat16) * 0.1)(D, N) * 3.14),
            "C_real": _w((lambda *s: torch.randn(*s, dtype=torch.bfloat16) * 0.4)(D, N) / N ** 0.5),
            "C_imag": _w((lambda *s: torch.randn(*s, dtype=torch.bfloat16) * 0.4)(D, N) / N ** 0.5),
            "log_dt": _w((lambda *s: torch.randn(*s, dtype=torch.bfloat16) * 0.1)(D) * 0.5 - 4.0),
            "D_skip": _w((lambda *s: torch.randn(*s, dtype=torch.bfloat16) * 0.1)(D)),
        }
    _A_in = _A_setup()

    def _B_setup():
        B, S, D, F = 1, 2048, 512, 1792
        return {
            "x": _w((lambda *s: torch.randn(*s, dtype=torch.bfloat16) * 0.05)(B, S, D)),
            "w1": _w((lambda *s: torch.randn(*s, dtype=torch.bfloat16) * 0.05)(D, F)),
            "b1": _w((lambda *s: torch.randn(*s, dtype=torch.bfloat16) * 0.05)(F)),
            "w2": _w((lambda *s: torch.randn(*s, dtype=torch.bfloat16) * 0.05)(F, D)),
            "b2": _w((lambda *s: torch.randn(*s, dtype=torch.bfloat16) * 0.05)(D)),
        }
    _B_in = _B_setup()

    return {
        "A_u": _A_in["u"],
        "A_log_A_real": _A_in["log_A_real"],
        "A_A_imag": _A_in["A_imag"],
        "A_C_real": _A_in["C_real"],
        "A_C_imag": _A_in["C_imag"],
        "A_log_dt": _A_in["log_dt"],
        "A_D_skip": _A_in["D_skip"],
        "B_x": _B_in["x"],
        "B_w1": _B_in["w1"],
        "B_b1": _B_in["b1"],
        "B_w2": _B_in["w2"],
        "B_b2": _B_in["b2"],
        "gate_w": _w((lambda *s: torch.randn(*s, dtype=torch.bfloat16) * 0.05)(512) * 16.0),
    }

def reference(A_u, A_log_A_real, A_A_imag, A_C_real, A_C_imag, A_log_dt, A_D_skip, B_x, B_w1, B_b1, B_w2, B_b2, gate_w):
    def _A_ref(u, log_A_real, A_imag, C_real, C_imag, log_dt, D_skip):
        L = u.shape[1]
        dt = torch.exp(log_dt)
        A = -torch.exp(log_A_real) + 1j * A_imag
        C = (C_real + 1j * C_imag) * dt[:, None]
        pos = (lambda x, dt: x.to(dt))(torch.arange(L), log_dt.dtype)
        P = torch.exp((A * dt[:, None])[:, :, None] * pos[None, None, :])
        K = 2.0 * torch.einsum("dn,dnl->dl", C, P).real
        L2 = 2 * L
        u_t = (lambda x, *d: x.permute(*d))(u, 0, 2, 1)
        Kf = (lambda x, *a, **k: torch.fft.rfft(x.float(), *a, **k))(K, L2)
        Uf = (lambda x, *a, **k: torch.fft.rfft(x.float(), *a, **k))(u_t, L2)
        y = (lambda x, *a, **k: torch.fft.irfft(x, *a, **k).to(torch.bfloat16))(Uf * Kf[None, :, :], L2)[:, :, :L]
        y = (lambda x, *d: x.permute(*d))(y, 0, 2, 1)
        return y + u * D_skip[None, None, :]

    def _B_ref(x, w1, b1, w2, b2):
        h = torch.einsum("bsd,df->bsf", x, w1) + b1
        zero = torch.zeros_like(h)
        r = torch.where(h > zero, h, zero)
        h = r * r
        out = torch.einsum("bsf,fd->bsd", h, w2) + b2
        return out

    x = A_u
    g = _sigmoid((x * gate_w).sum(-1, keepdims=True))
    a = _A_ref(x, A_log_A_real, A_A_imag, A_C_real, A_C_imag, A_log_dt, A_D_skip)
    b = _B_ref(x, B_w1, B_b1, B_w2, B_b2)
    return g * a + (1 - g) * b
```

```cpp
#include <hip/hip_runtime.h>
#include <hip/hip_bf16.h>
typedef unsigned short u16;
__device__ __forceinline__ float bf2f(u16 v){ return __uint_as_float(((unsigned)v)<<16); }
__device__ __forceinline__ u16 f2bf(float f){ unsigned u=__float_as_uint(f); return (u16)((u + 0x7FFF + ((u>>16)&1))>>16); }
__device__ __forceinline__ float rbf(float f){ return bf2f(f2bf(f)); }
constexpr int L=2048, D=512, N=128, F=1792;

__global__ void kgen(const u16* lar,const u16* ai,const u16* cr,const u16* ci,const u16* ldt,float* K){
  int d=blockIdx.y; int l=blockIdx.x*256+threadIdx.x;
  float dt=rbf(expf(bf2f(ldt[d])));
  float p=rbf((float)l);
  float s=0.f;
  for(int n=0;n<N;n++){
    float ar=-rbf(expf(bf2f(lar[d*N+n]))); float aim=bf2f(ai[d*N+n]);
    float zr=ar*dt, zi=aim*dt;
    float e=expf(zr*p); float c=cosf(zi*p), sn=sinf(zi*p);
    float Cr=bf2f(cr[d*N+n])*dt, Ci=bf2f(ci[d*N+n])*dt;
    s+=Cr*e*c-Ci*e*sn;
  }
  K[d*L+l]=2.f*s;
}
__global__ void conv(const u16* u,const float* K,const u16* dskip,float* a){
  int d=blockIdx.y; int l=blockIdx.x*256+threadIdx.x;
  __shared__ float su[L]; __shared__ float sk[L];
  for(int i=threadIdx.x;i<L;i+=256){ su[i]=bf2f(u[i*D+d]); sk[i]=K[d*L+i]; }
  __syncthreads();
  float s=0.f;
  for(int j=0;j<=l;j++) s+=sk[j]*su[l-j];
  float y=rbf(s);
  a[l*D+d]=rbf(y+rbf(su[l]*bf2f(dskip[d])));
}
__global__ void gemm1(const u16* x,const u16* w1,const u16* b1,u16* h){
  int f=blockIdx.x*256+threadIdx.x; int r=blockIdx.y;
  __shared__ float sx[D];
  for(int i=threadIdx.x;i<D;i+=256) sx[i]=bf2f(x[r*D+i]);
  __syncthreads();
  float s=0.f;
  for(int k=0;k<D;k++) s+=sx[k]*bf2f(w1[k*F+f]);
  float v=rbf(rbf(s)+bf2f(b1[f]));
  v=v>0.f?v:0.f;
  h[r*F+f]=f2bf(v*v);
}
__global__ void gemm2(const u16* x,const u16* h,const u16* w2,const u16* b2,const u16* gw,const float* a,u16* out){
  int r=blockIdx.x; int d=threadIdx.x;
  __shared__ float sh[F]; __shared__ float red[512];
  for(int i=threadIdx.x;i<F;i+=512) sh[i]=bf2f(h[r*F+i]);
  red[d]=rbf(bf2f(x[r*D+d])*bf2f(gw[d]));
  __syncthreads();
  for(int s=256;s>0;s>>=1){ if(d<s) red[d]+=red[d+s]; __syncthreads(); }
  float z=rbf(red[0]); float g=rbf(1.f/(1.f+expf(-z)));
  float s=0.f;
  for(int k=0;k<F;k++) s+=sh[k]*bf2f(w2[k*D+d]);
  float b=rbf(rbf(s)+bf2f(b2[d]));
  float o=rbf(rbf(g*a[r*D+d])+rbf(rbf(1.f-g)*b));
  out[r*D+d]=f2bf(o);
}
extern "C" void kernel_launch(void* const* d_in, const int* in_sizes, int n_in,
                              void* d_out, int out_size, void* d_ws, size_t ws_size,
                              hipStream_t stream) {
  const u16* u=(const u16*)d_in[0];
  float* K=(float*)d_ws; float* a=K+D*L; u16* h=(u16*)(a+D*L);
  kgen<<<dim3(L/256,D),256,0,stream>>>((const u16*)d_in[1],(const u16*)d_in[2],(const u16*)d_in[3],(const u16*)d_in[4],(const u16*)d_in[5],K);
  conv<<<dim3(L/256,D),256,0,stream>>>(u,K,(const u16*)d_in[6],a);
  gemm1<<<dim3(F/256,L),256,0,stream>>>(u,(const u16*)d_in[8],(const u16*)d_in[9],h);
  gemm2<<<L,512,0,stream>>>(u,h,(const u16*)d_in[10],(const u16*)d_in[11],(const u16*)d_in[12],a,(u16*)d_out);
}
```

```cpp
#include <hip/hip_runtime.h>
#pragma clang diagnostic ignored "-Wint-to-pointer-cast"

typedef unsigned short u16;
typedef unsigned u32;
typedef unsigned long long u64;
typedef __bf16 h16x8 __attribute__((ext_vector_type(8)));
typedef __bf16 h16x2 __attribute__((ext_vector_type(2)));
typedef short s16x4 __attribute__((ext_vector_type(4)));
typedef short s16x8 __attribute__((ext_vector_type(8)));
typedef short s16x2 __attribute__((ext_vector_type(2)));
typedef float f32x2 __attribute__((ext_vector_type(2)));
typedef float f32x4 __attribute__((ext_vector_type(4)));
typedef float f32x16 __attribute__((ext_vector_type(16)));
typedef u32 u32x2 __attribute__((ext_vector_type(2)));
typedef u32 u32x4 __attribute__((ext_vector_type(4)));
#define LAS __attribute__((address_space(3)))
#define CAS __attribute__((address_space(4)))
#define DI __device__ __forceinline__

constexpr int L = 2048, D = 512, F = 1792;
constexpr u32 WS_FLAG = 4096;
constexpr u32 WS_G = 65536;
constexpr u32 WS_UT = 1u << 20;
constexpr u32 WS_AT = 5u << 20;
constexpr u32 WS_H = 9u << 20;
constexpr u32 WS_END = 17u << 20;
constexpr u32 WS_STAMP = 32u << 20;
constexpr u32 SLOT = 32768;
constexpr u32 TAB = 3 * SLOT;
constexpr u32 KD = TAB + 256 * 224;
constexpr u32 KD_CH = 32 * 36 * 2;
constexpr u32 KD_ZERO = 34 * 2;
constexpr u32 ZS = KD + 2 * KD_CH;
constexpr u32 RED = TAB;
constexpr u32 TC = RED + 16384;
constexpr u32 TC_STR = 4160;
constexpr u32 US = TC + 4 * TC_STR;
constexpr u32 US_STR = (256 + 2048) * 2;
constexpr u32 AS = US + 2 * US_STR;
constexpr u32 LDS_BYTES = 163840;
static_assert(AS + 64 * 144 <= KD && ZS + 2048 <= LDS_BYTES, "lds map");
constexpr u32 SPIN_CAP = 1u << 16;

DI float bf2f(u32 v) { return __uint_as_float(v << 16); }
DI u32 pk2(float lo, float hi) { f32x2 a = {lo, hi}; return __builtin_bit_cast(u32, __builtin_convertvector(a, h16x2)); }
DI float rbf(float x) { return bf2f(pk2(x, x) & 0xffffu); }
DI float dot2(u32 a, u32 b, float c) { return __builtin_amdgcn_fdot2_f32_bf16(__builtin_bit_cast(h16x2, a), __builtin_bit_cast(h16x2, b), c, false); }
DI u32 rfl(u32 x) { return __builtin_amdgcn_readfirstlane(x); }
DI f32x2 cmul(f32x2 a, f32x2 b) { f32x2 r; r.x = a.x * b.x - a.y * b.y; r.y = a.x * b.y + a.y * b.x; return r; }
#define MF32(a, b, c) __builtin_amdgcn_mfma_f32_32x32x16_bf16(__builtin_bit_cast(h16x8, a), __builtin_bit_cast(h16x8, b), c, 0, 0, 0)
#define MF16(a, b, c) __builtin_amdgcn_mfma_f32_16x16x32_bf16(__builtin_bit_cast(h16x8, a), __builtin_bit_cast(h16x8, b), c, 0, 0, 0)
#define DMA(voff, srd, soff, dst) \
  asm volatile("s_mov_b32 m0, %0\n\ts_nop 0\n\tbuffer_load_dwordx4 %1, %2, %3 offen lds" ::"s"(dst), "v"(voff), "s"(srd), "s"(soff) : "memory")
#define VMCNT(n) asm volatile("s_waitcnt vmcnt(%0)" ::"i"(n) : "memory")
#define LGKM0() asm volatile("s_waitcnt lgkmcnt(0)" ::: "memory")
#define BAR() asm volatile("s_waitcnt lgkmcnt(0)\n\ts_barrier" ::: "memory")
#define SB() __builtin_amdgcn_sched_barrier(0)
#define ST(i)

DI u32x4 lds128(u32 a) { return *(const LAS u32x4*)a; }
DI u32x4 ldstr(u32 a0, u32 a1) {
  s16x4 lo = __builtin_amdgcn_ds_read_tr16_b64_v4i16((LAS s16x4*)a0);
  s16x4 hi = __builtin_amdgcn_ds_read_tr16_b64_v4i16((LAS s16x4*)a1);
  return __builtin_bit_cast(u32x4, __builtin_shufflevector(lo, hi, 0, 1, 2, 3, 4, 5, 6, 7));
}

struct Args {
  const u16 *x, *lar, *aim, *cre, *cim, *ldt, *dsk, *w1, *b1, *w2, *b2, *gw;
  u16* out;
  char* ws;
};

__global__ void __launch_bounds__(256, 1) fused_mix(Args A) {
  __shared__ __attribute__((aligned(16))) char smem[LDS_BYTES];
  const u32 sb = (u32)(size_t)(LAS char*)smem;
  ST(0);
  const u32 tid = threadIdx.x, lane = tid & 63, w = rfl(tid >> 6);
  const u32 b = blockIdx.x, xcd = b & 7, bi = b >> 3;
  const u32 hf = xcd & 1, p = 4 * (xcd >> 1) + (bi >> 3), jj = bi & 7;
  const bool spare = jj == 7;
  const u32 cb1 = 7 * hf + jj, rb = 2 * p + hf, cb2 = jj, pair = b;
  const u32 rot = 2 * (bi >> 3) + jj;
  const u32 wm = w >> 1, wn = w & 1;
  const u32 hh = lane >> 5, blk = (lane >> 4) & 1, q4 = (lane & 15) >> 2, p4 = lane & 3, g4 = lane >> 4;

  const __amdgpu_buffer_rsrc_t sX = __builtin_amdgcn_make_buffer_rsrc((void*)A.x, 0, L * D * 2, 0x00020000);
  const __amdgpu_buffer_rsrc_t sW1 = __builtin_amdgcn_make_buffer_rsrc((void*)A.w1, 0, D * F * 2, 0x00020000);
  const __amdgpu_buffer_rsrc_t sW2 = __builtin_amdgcn_make_buffer_rsrc((void*)A.w2, 0, D * F * 2, 0x00020000);
  const __amdgpu_buffer_rsrc_t sWS = __builtin_amdgcn_make_buffer_rsrc((void*)A.ws, 0, WS_END, 0x00020000);
  const __amdgpu_buffer_rsrc_t sOut = __builtin_amdgcn_make_buffer_rsrc((void*)A.out, 0, L * D * 2, 0x00020000);

  u32 r_lar, r_aim, r_cre, r_cim, r_b1a, r_b1b;
  {
    const u32 o = (pair * 256 + tid) * 2;
    asm volatile("global_load_ushort %0, %1, %2" : "=v"(r_lar) : "v"(o), "s"(A.lar));
    asm volatile("global_load_ushort %0, %1, %2" : "=v"(r_aim) : "v"(o), "s"(A.aim));
    asm volatile("global_load_ushort %0, %1, %2" : "=v"(r_cre) : "v"(o), "s"(A.cre));
    asm volatile("global_load_ushort %0, %1, %2" : "=v"(r_cim) : "v"(o), "s"(A.cim));
    const u32 o1 = ((spare ? 0 : cb1) * 128 + wn * 64 + (lane & 31)) * 2;
    asm volatile("global_load_ushort %0, %1, %2" : "=v"(r_b1a) : "v"(o1), "s"(A.b1));
    asm volatile("global_load_ushort %0, %1, %2 offset:64" : "=v"(r_b1b) : "v"(o1), "s"(A.b1));
  }
  const u32 tag = *(const CAS u32*)A.ws + 1u;
  const u32 t_ldt = *(const CAS u32*)(A.ldt + 2 * pair), t_dsk = *(const CAS u32*)(A.dsk + 2 * pair);

  const u32 vx0 = (lane >> 3) * 1024 + (((lane & 7) ^ g4) << 4), vx1 = vx0 ^ 64;
  const u32 vw0 = g4 * (F * 2) + (((lane & 15) ^ (g4 << 2)) << 4);
  const u32 v2 = (lane >> 3) * 1024 + (((lane & 7) ^ (blk << 2)) << 4);
  auto stage1 = [&](u32 s, u32 slot) {
    const u32 ks = (s + rot) & 7;
    const u32 sx = (128 * p + 32 * w) * 1024 + ks * 128, dx = sb + slot + w * 4096;
    const u32 sw = (ks * 64 + 16 * w) * (F * 2) + cb1 * 256, dw = dx + 16384;
#pragma unroll
    for (int q = 0; q < 4; q++) {
      DMA((q & 1) ? vx1 : vx0, sX, sx + q * 8192, dx + q * 1024);
      DMA(vw0 ^ (q << 4), sW1, sw + q * 4 * (F * 2), dw + q * 1024);
    }
  };
  auto stage2 = [&](u32 c, u32 slot) {
    const u32 so = (28 * w + 4 * c) * 16 * 1024 + cb2 * 128, dd = sb + slot + w * 8192;
#pragma unroll
    for (int q = 0; q < 8; q++) DMA(v2, sW2, so + q * 8192, dd + q * 1024);
  };

  if (!spare) {
    stage1(0, 0); stage1(1, SLOT); stage1(2, 2 * SLOT);
  } else {
    const u32 vb = g4 * 1024 + (((lane & 15) ^ hh) << 4);
#pragma unroll
    for (int q = 0; q < 16; q++) DMA(vb ^ (((2 * q) & 15) << 4), sX, (64 * rb + 4 * q) * 1024 + w * 256, sb + w * 16384 + q * 1024);
    u32x4 gwv[8];
#pragma unroll
    for (int m = 0; m < 8; m++) gwv[m] = *(const u32x4*)(A.gw + 128 * w + 16 * m + 8 * hh);
    VMCNT(0);
    const u32 i = lane & 31;
    const u32 ra = sb + w * 16384 + i * 512 + ((hh ^ (i & 15)) << 4);
    const u32 vo = hh * 4 * 16384 + i * 16;
    float za = 0.f, zb = 0.f;
#pragma unroll
    for (int m = 0; m < 8; m++) {
      const u32x4 ea = lds128(ra ^ (m << 5)), eb = lds128((ra ^ (m << 5)) + 256);
#pragma unroll
      for (int e = 0; e < 4; e++) {
        u32x4 gr = {ea[e], tag, eb[e], tag};
        __builtin_amdgcn_raw_buffer_store_b128(gr, sWS, vo + WS_UT + (64 * w + 8 * m + e) * 16384 + rb * 512, 0, 16);
        za = dot2(ea[e], gwv[m][e], za);
        zb = dot2(eb[e], gwv[m][e], zb);
      }
    }
    *(LAS f32x2*)(sb + ZS + (2 * w + hh) * 256 + i * 8) = f32x2{za, zb};
    BAR();
    if (w == 0) {
      float z = 0.f;
#pragma unroll
      for (int k = 0; k < 8; k++) z += *(const LAS float*)(sb + ZS + k * 256 + lane * 4);
      z = rbf(z);
      const float g = rbf(1.f / (1.f + __expf(-z)));
      u32x2 gr = {pk2(g, 1.f - g), tag};
      __builtin_amdgcn_raw_buffer_store_b64(gr, sWS, lane * 8, WS_G + rb * 512, 16);
    }
    stage2(0, 2 * SLOT); stage2(1, 0); stage2(2, SLOT);
  }
  ST(1);

  u32 ga[9];
#pragma unroll
  for (int e = 0; e < 9; e++) {
    const int tap = 2040 - 8 * (int)tid + e - 1;
    const u32 pos = (u32)rbf((float)(tap < 0 ? 0 : tap));
    const u32 hi = pos >> 8, lo = pos & 7;
    const u32 ex = min(3u, 32u - (u32)__clz(hi));
    const u32 rho = 4 * ex + ((8 * hi + lo) >> ex);
    ga[e] = sb + KD + (tap < 0 ? KD_ZERO : (rho * 36 + ((pos & 255) >> 3)) * 2);
    asm volatile("" : "+v"(ga[e]));
  }
  if (tid < 2) *(LAS u32*)(sb + KD + tid * KD_CH + KD_ZERO) = 0;

  asm volatile("s_waitcnt vmcnt(24)" : "+v"(r_lar), "+v"(r_aim), "+v"(r_cre), "+v"(r_cim), "+v"(r_b1a), "+v"(r_b1b));
  ST(2);
  {
    const float dt = rbf(__expf(bf2f(wm ? t_ldt >> 16 : t_ldt & 0xffffu)));
    const float er = -(rbf(__expf(bf2f(r_lar))) * dt) * 1.44269504088896f;
    const float wi = (bf2f(r_aim) * dt) * 0.159154943091895f;
    auto seed = [&](float t) {
      const float m = __builtin_amdgcn_exp2f(er * t), a = __builtin_amdgcn_fractf(wi * t);
      return f32x2{m * __builtin_amdgcn_cosf(a), m * __builtin_amdgcn_sinf(a)};
    };
    const u32 ta = sb + TAB + tid * 224;
    auto put8 = [&](const f32x2* v, u32 off) {
      u32x4 re = {pk2(v[0].x, v[1].x), pk2(v[2].x, v[3].x), pk2(v[4].x, v[5].x), pk2(v[6].x, v[7].x)};
      u32x4 im = {pk2(v[0].y, v[1].y), pk2(v[2].y, v[3].y), pk2(v[4].y, v[5].y), pk2(v[6].y, v[7].y)};
      *(LAS u32x4*)(ta + off) = re;
      *(LAS u32x4*)(ta + 112 + off) = im;
    };
    f32x2 v[8];
    const f32x2 w8 = seed(8.f), w64 = seed(64.f);
    v[0] = f32x2{1.f, 0.f}; v[1] = w8; v[2] = cmul(w8, w8); v[3] = cmul(v[2], w8);
    v[4] = cmul(v[2], v[2]); v[5] = cmul(v[4], w8); v[6] = cmul(v[4], v[2]); v[7] = cmul(v[4], v[3]);
    put8(v, 0);
#pragma unroll
    for (int r = 1; r < 4; r++) {
#pragma unroll
      for (int k = 0; k < 8; k++) v[k] = cmul(v[k], w64);
      put8(v, 16 * r);
    }
    f32x2 v1 = seed(1.f), W = seed(256.f);
    v1.y = -v1.y; W.y = -W.y;
    f32x2 u[8];
    u[0] = f32x2{2.f * (bf2f(r_cre) * dt), -2.f * (bf2f(r_cim) * dt)};
    const f32x2 v2c = cmul(v1, v1);
    u[1] = cmul(u[0], v1);
#pragma unroll
    for (int k = 2; k < 8; k++) u[k] = cmul(u[k - 2], v2c);
    put8(u, 64);
    v[0] = cmul(u[0], W); v[1] = cmul(u[2], W); v[2] = cmul(u[4], W); v[3] = cmul(u[6], W);
    v[4] = cmul(v[0], W); v[5] = cmul(v[2], W); v[6] = cmul(v[4], W); v[7] = cmul(v[5], W);
    put8(v, 80);
    u[0] = cmul(v[6], W); u[1] = cmul(u[0], W); u[2] = cmul(u[1], W); u[3] = cmul(u[2], W); u[4] = cmul(u[3], W);
    u[5] = u[6] = u[7] = f32x2{0.f, 0.f};
    put8(u, 96);
  }
  ST(3);

  if (!spare) {
    f32x16 acc[2][2];
    {
      const f32x16 z = {};
      const u32x4 one = {hh ? 0u : 0x3f80u, 0u, 0u, 0u};
      const u32x4 ba = {r_b1a, 0u, 0u, 0u}, bb = {r_b1b, 0u, 0u, 0u};
      acc[0][0] = MF32(ba, one, z); acc[0][1] = MF32(ba, one, z);
      acc[1][0] = MF32(bb, one, z); acc[1][1] = MF32(bb, one, z);
    }
    const u32 xa = sb + (64 * wm + (lane & 31)) * 128 + ((hh ^ ((lane >> 1) & 7)) << 4);
    const u32 wa = sb + 16384 + 256 * (8 * hh + q4) + 8 * (p4 & 1) + (((8 * wn + 2 * blk + (p4 >> 1)) ^ ((q4 << 2) | (2 * hh))) << 4);
    u32x4 fx[2][2], fw[2][2];
    auto rd = [&](int buf, u32 slot, int ks) {
      fx[buf][0] = lds128((xa ^ (ks << 5)) + slot);
      fx[buf][1] = lds128((xa ^ (ks << 5)) + slot + 4096);
      fw[buf][0] = ldstr(wa + slot + ks * 4096, (wa ^ 16) + slot + ks * 4096 + 1024);
      fw[buf][1] = ldstr((wa ^ 64) + slot + ks * 4096, (wa ^ 80) + slot + ks * 4096 + 1024);
    };
    auto mm = [&](int buf) {
      acc[0][0] = MF32(fw[buf][0], fx[buf][0], acc[0][0]);
      acc[0][1] = MF32(fw[buf][0], fx[buf][1], acc[0][1]);
      acc[1][0] = MF32(fw[buf][1], fx[buf][0], acc[1][0]);
      acc[1][1] = MF32(fw[buf][1], fx[buf][1], acc[1][1]);
    };
    VMCNT(16); BAR();
    ST(4);
    u32 slot = 0;
    rd(0, slot, 0);
#pragma unroll 1
    for (u32 s = 0; s < 8; s++) {
      rd(1, slot, 1); SB(); mm(0); SB();
      rd(0, slot, 2); SB(); mm(1); SB();
      rd(1, slot, 3); SB(); mm(0); SB();
      VMCNT(8); BAR();
      const u32 nslot = slot == 2 * SLOT ? 0 : slot + SLOT;
      rd(0, nslot, 0);
      SB();
      if (s < 5) stage1(s + 3, slot); else stage2(s - 5, slot);
      SB(); mm(1); SB();
      slot = nslot;
    }
    ST(5);
#pragma unroll
    for (int u = 0; u < 2; u++)
#pragma unroll
      for (int t = 0; t < 2; t++)
#pragma unroll
        for (int s = 0; s < 2; s++) {
          u32x4 o;
#pragma unroll
          for (int e = 0; e < 4; e++) {
            const float a0 = fmaxf(acc[u][t][8 * s + 2 * e], 0.f), a1 = fmaxf(acc[u][t][8 * s + 2 * e + 1], 0.f);
            o[e] = pk2(a0 * a0, a1 * a1);
          }
          __builtin_amdgcn_raw_buffer_store_b128(o, sWS, lane * 16 + WS_H + ((4 * p + 2 * wm + t) * 112 + cb1 * 8 + wn * 4 + 2 * u + s) * 1024, 0, 16);
        }
  } else {
    VMCNT(0); BAR();
  }
  SB();
  ST(6);
  u32x4 ut[4];
  auto load_ut = [&]() {
#pragma unroll
    for (int j = 0; j < 4; j++)
      asm volatile("buffer_load_dwordx4 %0, %1, %2, %3 offen sc1" : "=v"(ut[j]) : "v"(lane * 16), "s"(sWS), "s"(WS_UT + pair * 16384 + (4 * w + j) * 1024) : "memory");
  };
  load_ut();
  u32x4 w2r[16][2];
  const u32 w2a = sb + w * 8192 + 128 * (4 * hh + q4) + 8 * (p4 & 1) + (((4 * wn + 2 * blk + (p4 >> 1)) ^ ((q4 >> 1) << 2)) << 4);
  auto w2frag = [&](int st, int v) {
    const u32 a = (w2a ^ (v << 6)) + ((2 + (st >> 2)) % 3) * SLOT + (st & 3) * 2048;
    return ldstr(a, a + 1024);
  };
  auto xfer = [&](int c) {
#pragma unroll
    for (int j = 0; j < 4; j++) { w2r[4 * c + j][0] = w2frag(4 * c + j, 0); w2r[4 * c + j][1] = w2frag(4 * c + j, 1); }
    LGKM0();
    stage2(c + 3, ((2 + c) % 3) * SLOT);
  };
  VMCNT(28); xfer(0);
  {
    f32x4 ka[2] = {{(lane == 0 && wn == 0) ? bf2f(wm ? t_dsk >> 16 : t_dsk & 0xffffu) : 0.f, 0.f, 0.f, 0.f}, {0.f, 0.f, 0.f, 0.f}};
    const u32 t0 = sb + TAB + (wm * 128 + 4 * g4 + q4) * 224 + 8 * p4;
#pragma unroll
    for (int ks = 0; ks < 8; ks++) {
      const u32 a = t0 + ks * 16 * 224;
      const u32x4 fv = ldstr(a + 32 * wn, a + 32 * wn + 112);
      const u32x4 f0 = ldstr(a + 64, a + 64 + 112), f1 = ldstr(a + 96, a + 96 + 112);
      ka[0] = MF16(f0, fv, ka[0]);
      ka[1] = MF16(f1, fv, ka[1]);
    }
    const u32 ko = sb + KD + wm * KD_CH + (4 * g4 * 36 + 16 * wn + (lane & 15)) * 2;
#pragma unroll
    for (int rt = 0; rt < 2; rt++)
#pragma unroll
      for (int r = 0; r < 4; r += 2) {
        const u32 pr = pk2(ka[rt][r], ka[rt][r + 1]);
        *(LAS u16*)(ko + (16 * rt + r) * 72) = (u16)pr;
        *(LAS u16*)(ko + (16 * rt + r + 1) * 72) = (u16)(pr >> 16);
      }
  }
  VMCNT(28); xfer(1);
  VMCNT(20);
  BAR();
  if (tid == 0) asm volatile("buffer_store_dword %0, %1, %2, %3 offen sc1" ::"v"(tag), "v"(0u), "s"(sWS), "s"(WS_FLAG + p * 128 + (8 * hf + jj) * 4) : "memory");
  ST(7);
#pragma unroll
  for (int c = 0; c < 2; c++) {
    u32 e[9];
#pragma unroll
    for (int k = 0; k < 9; k++) e[k] = *(const LAS u16*)(ga[k] + c * KD_CH);
    u32x4 r1 = {e[8] | (e[7] << 16), e[6] | (e[5] << 16), e[4] | (e[3] << 16), e[2] | (e[1] << 16)};
    u32x4 r0 = {e[7] | (e[6] << 16), e[5] | (e[4] << 16), e[3] | (e[2] << 16), e[1] | (e[0] << 16)};
    *(LAS u32x4*)(sb + TC + (2 * c) * TC_STR + tid * 16) = r0;
    *(LAS u32x4*)(sb + TC + (2 * c + 1) * TC_STR + tid * 16) = r1;
  }
  if (tid < 8) *(LAS u32x4*)(sb + TC + (tid >> 1) * TC_STR + 4096 + (tid & 1) * 16) = u32x4{0, 0, 0, 0};
  *(LAS u32*)(sb + US + (tid >> 7) * US_STR + (tid & 127) * 4) = 0;
  asm volatile("s_waitcnt vmcnt(16)" : "+v"(ut[0]), "+v"(ut[1]), "+v"(ut[2]), "+v"(ut[3])::"memory");
  _Pragma("unroll 1") for (u32 spin = 0; spin < SPIN_CAP; spin++) {
    bool ok = true;
#pragma unroll
    for (int j = 0; j < 4; j++) ok &= ut[j][1] == tag && ut[j][3] == tag;
    if (__all(ok)) break;
    load_ut();
    asm volatile("s_waitcnt vmcnt(0)" : "+v"(ut[0]), "+v"(ut[1]), "+v"(ut[2]), "+v"(ut[3])::"memory");
  }
#pragma unroll
  for (int j = 0; j < 4; j++) {
    const u32 a = sb + US + 512 + ((4 * w + j) * 64 + lane) * 4;
    *(LAS u32*)a = __builtin_amdgcn_perm(ut[j][2], ut[j][0], 0x05040100);
    *(LAS u32*)(a + US_STR) = __builtin_amdgcn_perm(ut[j][2], ut[j][0], 0x07060302);
  }
  xfer(2);
  BAR();
  ST(8);

  u32x4 hfr[28][2];
  f32x4 ca[8];
#pragma unroll
  for (int c = 0; c < 8; c++) ca[c] = f32x4{0.f, 0.f, 0.f, 0.f};
  const u32 ua = sb + US + wm * US_STR + (256 - 16 - 128 * wn - 96 + 16 * (lane & 15) + 8 * g4) * 2;
  const u32 tb = sb + TC + (2 * wm + (lane & 1)) * TC_STR + (2046 - 16 - 128 * wn - 96 - 1792 + 8 * g4 - (lane & 14)) * 2;
  auto phase = [&](int f) {
    u32x4 fu[8], ft[8];
#pragma unroll
    for (int e = 0; e < 8; e++) {
      fu[e] = lds128(ua + (96 - 32 * f + 256 * e) * 2);
      const LAS u32* t = (const LAS u32*)(tb + (96 - 32 * f + 1792 - 256 * e) * 2);
      ft[e] = u32x4{t[0], t[1], t[2], t[3]};
    }
#pragma unroll
    for (int i = 0; i < 8; i++)
#pragma unroll
      for (int e = 0; e + i < 8; e++) ca[i + e] = MF16(fu[e], ft[i], ca[i + e]);
  };
  phase(0);
  VMCNT(16); xfer(3);
  u32 fl;
  const u32 fo = (lane & 15) * 4;
  asm volatile("buffer_load_dword %0, %1, %2, %3 offen sc1" : "=v"(fl) : "v"(fo), "s"(sWS), "s"(WS_FLAG + p * 128) : "memory");
  phase(1);
  phase(2);
  asm volatile("s_waitcnt vmcnt(0)" : "+v"(fl)::"memory");
  ST(9);
  _Pragma("unroll 1") for (u32 spin = 0; spin < SPIN_CAP; spin++) {
    if (__all(fl == tag)) break;
    asm volatile("buffer_load_dword %0, %1, %2, %3 offen sc1\n\ts_waitcnt vmcnt(0)" : "=&v"(fl) : "v"(fo), "s"(sWS), "s"(WS_FLAG + p * 128) : "memory");
  }
  ST(10);
  SB();
#pragma unroll
  for (int st = 0; st < 28; st++)
#pragma unroll
    for (int t = 0; t < 2; t++)
      hfr[st][t] = __builtin_amdgcn_raw_buffer_load_b128(sWS, lane * 16, WS_H + ((2 * rb + (t ^ wm)) * 112 + 28 * w + st) * 1024, 16);
  SB();
  phase(3);
  {
    f32x4 keep[4];
    const u32 ro = sb + RED + w * 4096 + lane * 16;
    if (wn) {
#pragma unroll
      for (int c = 0; c < 4; c++) { *(LAS f32x4*)(ro + c * 1024) = ca[c]; keep[c] = ca[c + 4]; }
      asm volatile("" ::: "memory");
    } else {
#pragma unroll
      for (int c = 0; c < 4; c++) { *(LAS f32x4*)(ro + c * 1024) = ca[c + 4]; keep[c] = ca[c]; }
    }
    BAR();
#pragma unroll
    for (int c = 0; c < 4; c++) {
      const f32x4 y = keep[c] + *(const LAS f32x4*)((ro ^ 4096) + c * 1024);
      u32x4 gr = {pk2(y[0], y[1]), tag, pk2(y[2], y[3]), tag};
      __builtin_amdgcn_raw_buffer_store_b128(gr, sWS, lane * 16 + WS_AT + (2 * pair + wm) * 8192 + (4 * wn + c) * 1024, 0, 16);
    }
  }
  SB();
  ST(11);

  f32x16 oc[2][2] = {};
  u32x4 at[4];
  u32x2 gg, b2v[4];
  auto load_at = [&]() {
#pragma unroll
    for (int j = 0; j < 4; j++)
      at[j] = __builtin_amdgcn_raw_buffer_load_b128(sWS, g4 * 8192 + (lane & 15) * 16, WS_AT + (64 * cb2 + 16 * w + 4 * j) * 8192 + rb * 256, 16);
    gg = __builtin_amdgcn_raw_buffer_load_b64(sWS, (lane & 31) * 8, WS_G + (64 * rb + 32 * wm) * 8, 16);
  };
#pragma unroll
  for (int st = 0; st < 28; st++) {
    const u32x4 a0 = st < 16 ? w2r[st < 16 ? st : 0][0] : w2frag(st, 0);
    const u32x4 a1 = st < 16 ? w2r[st < 16 ? st : 0][1] : w2frag(st, 1);
    oc[0][0] = MF32(a0, hfr[st][0], oc[0][0]);
    oc[1][0] = MF32(a1, hfr[st][0], oc[1][0]);
    oc[0][1] = MF32(a0, hfr[st][1], oc[0][1]);
    oc[1][1] = MF32(a1, hfr[st][1], oc[1][1]);
    if (st == 19) {
      SB();
      load_at();
#pragma unroll
      for (int q = 0; q < 4; q++) b2v[q] = *(const u32x2*)(A.b2 + 64 * cb2 + 32 * wn + 8 * q + 4 * hh);
      SB();
    }
  }
  ST(12);
  LGKM0();
#pragma unroll
  for (int j = 1; j < 4; j++)
#pragma unroll
    for (int q = 0; q < 4; q++) {
      const f32x16& s = oc[j & 1][j >> 1];
      *(LAS f32x4*)(sb + (j - 1) * SLOT + w * 8192 + q * 1024 + lane * 16) = f32x4{s[4 * q], s[4 * q + 1], s[4 * q + 2], s[4 * q + 3]};
    }
  _Pragma("unroll 1") for (u32 spin = 0; spin < SPIN_CAP; spin++) {
    bool ok = gg[1] == tag;
#pragma unroll
    for (int j = 0; j < 4; j++) ok &= at[j][1] == tag && at[j][3] == tag;
    if (__all(ok)) break;
    asm volatile("" ::: "memory");
    load_at();
  }
#pragma unroll
  for (int j = 0; j < 4; j++) {
    const u32 a = sb + AS + (lane & 15) * 144 + (16 * w + 4 * j + g4) * 2;
    *(LAS u16*)(a) = (u16)at[j][0];
    *(LAS u16*)(a + 16 * 144) = (u16)(at[j][0] >> 16);
    *(LAS u16*)(a + 32 * 144) = (u16)at[j][2];
    *(LAS u16*)(a + 48 * 144) = (u16)(at[j][2] >> 16);
  }
  BAR();
  ST(13);
  {
    const float g = bf2f(gg[0] & 0xffffu), gm = __uint_as_float(gg[0] & 0xffff0000u);
    const u32 aa = sb + AS + (32 * wm + (lane & 31)) * 144 + (32 * wn + 4 * hh) * 2;
#pragma unroll
    for (int q = 0; q < 4; q++) {
      f32x4 s = {oc[0][0][4 * q], oc[0][0][4 * q + 1], oc[0][0][4 * q + 2], oc[0][0][4 * q + 3]};
#pragma unroll
      for (int j = 1; j < 4; j++) s += *(const LAS f32x4*)(sb + (j - 1) * SLOT + (w ^ j) * 8192 + q * 1024 + lane * 16);
      const u32x2 av = *(const LAS u32x2*)(aa + 16 * q);
      const float o0 = g * bf2f(av[0] & 0xffffu) + gm * (s[0] + bf2f(b2v[q][0] & 0xffffu));
      const float o1 = g * __uint_as_float(av[0] & 0xffff0000u) + gm * (s[1] + __uint_as_float(b2v[q][0] & 0xffff0000u));
      const float o2 = g * bf2f(av[1] & 0xffffu) + gm * (s[2] + bf2f(b2v[q][1] & 0xffffu));
      const float o3 = g * __uint_as_float(av[1] & 0xffff0000u) + gm * (s[3] + __uint_as_float(b2v[q][1] & 0xffff0000u));
      *(LAS u32x2*)(aa + 16 * q) = u32x2{pk2(o0, o1), pk2(o2, o3)};
    }
    LGKM0();
#pragma unroll
    for (int j = 0; j < 2; j++) {
      const u32 r = 32 * wm + 16 * j + (lane >> 2);
      const u32x4 o = lds128(sb + AS + r * 144 + 64 * wn + (lane & 3) * 16);
      __builtin_amdgcn_raw_buffer_store_b128(o, sOut, r * 1024 + (lane & 3) * 16 + rb * 65536 + cb2 * 128 + wn * 64, 0, 0);
    }
  }
  if (b == 0 && w == 3) {
    _Pragma("unroll 1") for (u32 spin = 0; spin < SPIN_CAP; spin++) {
      asm volatile("" ::: "memory");
      const u32x4 f4 = __builtin_amdgcn_raw_buffer_load_b128(sWS, (lane >> 2) * 128 + (lane & 3) * 16, WS_FLAG, 16);
      if (__all(f4[0] == tag && f4[1] == tag && f4[2] == tag && f4[3] == tag)) break;
    }
    if (lane == 0) __builtin_amdgcn_raw_buffer_store_b32(tag, sWS, 0, 0, 16);
  }
}

extern "C" void kernel_launch(void* const* d_in, const int* in_sizes, int n_in, void* d_out, int out_size,
                              void* d_ws, size_t ws_size, hipStream_t stream) {
  Args a;
  a.x = (const u16*)d_in[0]; a.lar = (const u16*)d_in[1]; a.aim = (const u16*)d_in[2]; a.cre = (const u16*)d_in[3];
  a.cim = (const u16*)d_in[4]; a.ldt = (const u16*)d_in[5]; a.dsk = (const u16*)d_in[6]; a.w1 = (const u16*)d_in[8];
  a.b1 = (const u16*)d_in[9]; a.w2 = (const u16*)d_in[10]; a.b2 = (const u16*)d_in[11]; a.gw = (const u16*)d_in[12];
  a.out = (u16*)d_out; a.ws = (char*)d_ws;
  fused_mix<<<256, 256, 0, stream>>>(a);
}
```

```cpp
#define STAMP 1
#include <hip/hip_runtime.h>
#pragma clang diagnostic ignored "-Wint-to-pointer-cast"

typedef unsigned short u16;
typedef unsigned u32;
typedef unsigned long long u64;
typedef __bf16 h16x8 __attribute__((ext_vector_type(8)));
typedef __bf16 h16x2 __attribute__((ext_vector_type(2)));
typedef short s16x4 __attribute__((ext_vector_type(4)));
typedef short s16x8 __attribute__((ext_vector_type(8)));
typedef short s16x2 __attribute__((ext_vector_type(2)));
typedef float f32x2 __attribute__((ext_vector_type(2)));
typedef float f32x4 __attribute__((ext_vector_type(4)));
typedef float f32x16 __attribute__((ext_vector_type(16)));
typedef u32 u32x2 __attribute__((ext_vector_type(2)));
typedef u32 u32x4 __attribute__((ext_vector_type(4)));
#define LAS __attribute__((address_space(3)))
#define CAS __attribute__((address_space(4)))
#define DI __device__ __forceinline__

constexpr int L = 2048, D = 512, F = 1792;
constexpr u32 WS_FLAG = 4096;
constexpr u32 WS_G = 65536;
constexpr u32 WS_UT = 1u << 20;
constexpr u32 WS_AT = 5u << 20;
constexpr u32 WS_H = 9u << 20;
constexpr u32 WS_END = 17u << 20;
constexpr u32 WS_STAMP = 32u << 20;
constexpr u32 SLOT = 32768;
constexpr u32 TAB = 3 * SLOT;
constexpr u32 KD = TAB + 256 * 224;
constexpr u32 KD_CH = 32 * 36 * 2;
constexpr u32 KD_ZERO = 34 * 2;
constexpr u32 ZS = KD + 2 * KD_CH;
constexpr u32 RED = TAB;
constexpr u32 TC = RED + 16384;
constexpr u32 TC_STR = 4160;
constexpr u32 US = TC + 4 * TC_STR;
constexpr u32 US_STR = (256 + 2048) * 2;
constexpr u32 AS = US + 2 * US_STR;
constexpr u32 LDS_BYTES = 163840;
static_assert(AS + 64 * 144 <= KD && ZS + 2048 <= LDS_BYTES, "lds map");
constexpr u32 SPIN_CAP = 1u << 16;

DI float bf2f(u32 v) { return __uint_as_float(v << 16); }
DI u32 pk2(float lo, float hi) { f32x2 a = {lo, hi}; return __builtin_bit_cast(u32, __builtin_convertvector(a, h16x2)); }
DI float rbf(float x) { return bf2f(pk2(x, x) & 0xffffu); }
DI float dot2(u32 a, u32 b, float c) { return __builtin_amdgcn_fdot2_f32_bf16(__builtin_bit_cast(h16x2, a), __builtin_bit_cast(h16x2, b), c, false); }
DI u32 rfl(u32 x) { return __builtin_amdgcn_readfirstlane(x); }
DI f32x2 cmul(f32x2 a, f32x2 b) { f32x2 r; r.x = a.x * b.x - a.y * b.y; r.y = a.x * b.y + a.y * b.x; return r; }
#define MF32(a, b, c) __builtin_amdgcn_mfma_f32_32x32x16_bf16(__builtin_bit_cast(h16x8, a), __builtin_bit_cast(h16x8, b), c, 0, 0, 0)
#define MF16(a, b, c) __builtin_amdgcn_mfma_f32_16x16x32_bf16(__builtin_bit_cast(h16x8, a), __builtin_bit_cast(h16x8, b), c, 0, 0, 0)
#define DMA(voff, srd, soff, dst) \
  asm volatile("s_mov_b32 m0, %0\n\ts_nop 0\n\tbuffer_load_dwordx4 %1, %2, %3 offen lds" ::"s"(dst), "v"(voff), "s"(srd), "s"(soff) : "memory")
#define VMCNT(n) asm volatile("s_waitcnt vmcnt(%0)" ::"i"(n) : "memory")
#define LGKM0() asm volatile("s_waitcnt lgkmcnt(0)" ::: "memory")
#define BAR() asm volatile("s_waitcnt lgkmcnt(0)\n\ts_barrier" ::: "memory")
#define SB() __builtin_amdgcn_sched_barrier(0)
#define ST(i) do { SB(); asm volatile("s_memrealtime %0\n\ts_waitcnt lgkmcnt(0)" : "=s"(stamp[i])::"memory"); SB(); } while (0)

DI u32x4 lds128(u32 a) { return *(const LAS u32x4*)a; }
DI u32x4 ldstr(u32 a0, u32 a1) {
  s16x4 lo = __builtin_amdgcn_ds_read_tr16_b64_v4i16((LAS s16x4*)a0);
  s16x4 hi = __builtin_amdgcn_ds_read_tr16_b64_v4i16((LAS s16x4*)a1);
  return __builtin_bit_cast(u32x4, __builtin_shufflevector(lo, hi, 0, 1, 2, 3, 4, 5, 6, 7));
}

struct Args {
  const u16 *x, *lar, *aim, *cre, *cim, *ldt, *dsk, *w1, *b1, *w2, *b2, *gw;
  u16* out;
  char* ws;
};

__global__ void __launch_bounds__(256, 1) fused_mix(Args A) {
  __shared__ __attribute__((aligned(16))) char smem[LDS_BYTES];
  const u32 sb = (u32)(size_t)(LAS char*)smem;
  u64 stamp[16];
  for (int i = 0; i < 16; i++) stamp[i] = 0;
  ST(0);
  u64 cyc0; asm volatile("s_memtime %0\n\ts_waitcnt lgkmcnt(0)" : "=s"(cyc0)::"memory");
  const u32 tid = threadIdx.x, lane = tid & 63, w = rfl(tid >> 6);
  const u32 b = blockIdx.x, xcd = b & 7, bi = b >> 3;
  const u32 hf = xcd & 1, p = 4 * (xcd >> 1) + (bi >> 3), jj = bi & 7;
  const bool spare = jj == 7;
  const u32 cb1 = 7 * hf + jj, rb = 2 * p + hf, cb2 = jj, pair = b;
  const u32 rot = 2 * (bi >> 3) + jj;
  const u32 wm = w >> 1, wn = w & 1;
  const u32 hh = lane >> 5, blk = (lane >> 4) & 1, q4 = (lane & 15) >> 2, p4 = lane & 3, g4 = lane >> 4;

  const __amdgpu_buffer_rsrc_t sX = __builtin_amdgcn_make_buffer_rsrc((void*)A.x, 0, L * D * 2, 0x00020000);
  const __amdgpu_buffer_rsrc_t sW1 = __builtin_amdgcn_make_buffer_rsrc((void*)A.w1, 0, D * F * 2, 0x00020000);
  const __amdgpu_buffer_rsrc_t sW2 = __builtin_amdgcn_make_buffer_rsrc((void*)A.w2, 0, D * F * 2, 0x00020000);
  const __amdgpu_buffer_rsrc_t sWS = __builtin_amdgcn_make_buffer_rsrc((void*)A.ws, 0, WS_END, 0x00020000);
  const __amdgpu_buffer_rsrc_t sOut = __builtin_amdgcn_make_buffer_rsrc((void*)A.out, 0, L * D * 2, 0x00020000);

  u32 r_lar, r_aim, r_cre, r_cim, r_b1a, r_b1b;
  {
    const u32 o = (pair * 256 + tid) * 2;
    asm volatile("global_load_ushort %0, %1, %2" : "=v"(r_lar) : "v"(o), "s"(A.lar));
    asm volatile("global_load_ushort %0, %1, %2" : "=v"(r_aim) : "v"(o), "s"(A.aim));
    asm volatile("global_load_ushort %0, %1, %2" : "=v"(r_cre) : "v"(o), "s"(A.cre));
    asm volatile("global_load_ushort %0, %1, %2" : "=v"(r_cim) : "v"(o), "s"(A.cim));
    const u32 o1 = ((spare ? 0 : cb1) * 128 + wn * 64 + (lane & 31)) * 2;
    asm volatile("global_load_ushort %0, %1, %2" : "=v"(r_b1a) : "v"(o1), "s"(A.b1));
    asm volatile("global_load_ushort %0, %1, %2 offset:64" : "=v"(r_b1b) : "v"(o1), "s"(A.b1));
  }
  const u32 tag = *(const CAS u32*)A.ws + 1u;
  const u32 t_ldt = *(const CAS u32*)(A.ldt + 2 * pair), t_dsk = *(const CAS u32*)(A.dsk + 2 * pair);

  const u32 vx0 = (lane >> 3) * 1024 + (((lane & 7) ^ g4) << 4), vx1 = vx0 ^ 64;
  const u32 vw0 = g4 * (F * 2) + (((lane & 15) ^ (g4 << 2)) << 4);
  const u32 v2 = (lane >> 3) * 1024 + (((lane & 7) ^ (blk << 2)) << 4);
  auto stage1 = [&](u32 s, u32 slot) {
    const u32 ks = (s + rot) & 7;
    const u32 sx = (128 * p + 32 * w) * 1024 + ks * 128, dx = sb + slot + w * 4096;
    const u32 sw = (ks * 64 + 16 * w) * (F * 2) + cb1 * 256, dw = dx + 16384;
#pragma unroll
    for (int q = 0; q < 4; q++) {
      DMA((q & 1) ? vx1 : vx0, sX, sx + q * 8192, dx + q * 1024);
      DMA(vw0 ^ (q << 4), sW1, sw + q * 4 * (F * 2), dw + q * 1024);
    }
  };
  auto stage2 = [&](u32 c, u32 slot) {
    const u32 so = (28 * w + 4 * c) * 16 * 1024 + cb2 * 128, dd = sb + slot + w * 8192;
#pragma unroll
    for (int q = 0; q < 8; q++) DMA(v2, sW2, so + q * 8192, dd + q * 1024);
  };

  if (!spare) {
    stage1(0, 0); stage1(1, SLOT); stage1(2, 2 * SLOT);
  } else {
    const u32 vb = g4 * 1024 + (((lane & 15) ^ hh) << 4);
#pragma unroll
    for (int q = 0; q < 16; q++) DMA(vb ^ (((2 * q) & 15) << 4), sX, (64 * rb + 4 * q) * 1024 + w * 256, sb + w * 16384 + q * 1024);
    u32x4 gwv[8];
#pragma unroll
    for (int m = 0; m < 8; m++) gwv[m] = *(const u32x4*)(A.gw + 128 * w + 16 * m + 8 * hh);
    VMCNT(0);
    const u32 i = lane & 31;
    const u32 ra = sb + w * 16384 + i * 512 + ((hh ^ (i & 15)) << 4);
    const u32 vo = hh * 4 * 16384 + i * 16;
    float za = 0.f, zb = 0.f;
#pragma unroll
    for (int m = 0; m < 8; m++) {
      const u32x4 ea = lds128(ra ^ (m << 5)), eb = lds128((ra ^ (m << 5)) + 256);
#pragma unroll
      for (int e = 0; e < 4; e++) {
        u32x4 gr = {ea[e], tag, eb[e], tag};
        __builtin_amdgcn_raw_buffer_store_b128(gr, sWS, vo + WS_UT + (64 * w + 8 * m + e) * 16384 + rb * 512, 0, 16);
        za = dot2(ea[e], gwv[m][e], za);
        zb = dot2(eb[e], gwv[m][e], zb);
      }
    }
    *(LAS f32x2*)(sb + ZS + (2 * w + hh) * 256 + i * 8) = f32x2{za, zb};
    BAR();
    if (w == 0) {
      float z = 0.f;
#pragma unroll
      for (int k = 0; k < 8; k++) z += *(const LAS float*)(sb + ZS + k * 256 + lane * 4);
      z = rbf(z);
      const float g = rbf(1.f / (1.f + __expf(-z)));
      u32x2 gr = {pk2(g, 1.f - g), tag};
      __builtin_amdgcn_raw_buffer_store_b64(gr, sWS, lane * 8, WS_G + rb * 512, 16);
    }
    stage2(0, 2 * SLOT); stage2(1, 0); stage2(2, SLOT);
  }
  ST(1);

  u32 ga[9];
#pragma unroll
  for (int e = 0; e < 9; e++) {
    const int tap = 2040 - 8 * (int)tid + e - 1;
    const u32 pos = (u32)rbf((float)(tap < 0 ? 0 : tap));
    const u32 hi = pos >> 8, lo = pos & 7;
    const u32 ex = min(3u, 32u - (u32)__clz(hi));
    const u32 rho = 4 * ex + ((8 * hi + lo) >> ex);
    ga[e] = sb + KD + (tap < 0 ? KD_ZERO : (rho * 36 + ((pos & 255) >> 3)) * 2);
    asm volatile("" : "+v"(ga[e]));
  }
  if (tid < 2) *(LAS u32*)(sb + KD + tid * KD_CH + KD_ZERO) = 0;

  asm volatile("s_waitcnt vmcnt(24)" : "+v"(r_lar), "+v"(r_aim), "+v"(r_cre), "+v"(r_cim), "+v"(r_b1a), "+v"(r_b1b));
  ST(2);
  {
    const float dt = rbf(__expf(bf2f(wm ? t_ldt >> 16 : t_ldt & 0xffffu)));
    const float er = -(rbf(__expf(bf2f(r_lar))) * dt) * 1.44269504088896f;
    const float wi = (bf2f(r_aim) * dt) * 0.159154943091895f;
    auto seed = [&](float t) {
      const float m = __builtin_amdgcn_exp2f(er * t), a = __builtin_amdgcn_fractf(wi * t);
      return f32x2{m * __builtin_amdgcn_cosf(a), m * __builtin_amdgcn_sinf(a)};
    };
    const u32 ta = sb + TAB + tid * 224;
    auto put8 = [&](const f32x2* v, u32 off) {
      u32x4 re = {pk2(v[0].x, v[1].x), pk2(v[2].x, v[3].x), pk2(v[4].x, v[5].x), pk2(v[6].x, v[7].x)};
      u32x4 im = {pk2(v[0].y, v[1].y), pk2(v[2].y, v[3].y), pk2(v[4].y, v[5].y), pk2(v[6].y, v[7].y)};
      *(LAS u32x4*)(ta + off) = re;
      *(LAS u32x4*)(ta + 112 + off) = im;
    };
    f32x2 v[8];
    const f32x2 w8 = seed(8.f), w64 = seed(64.f);
    v[0] = f32x2{1.f, 0.f}; v[1] = w8; v[2] = cmul(w8, w8); v[3] = cmul(v[2], w8);
    v[4] = cmul(v[2], v[2]); v[5] = cmul(v[4], w8); v[6] = cmul(v[4], v[2]); v[7] = cmul(v[4], v[3]);
    put8(v, 0);
#pragma unroll
    for (int r = 1; r < 4; r++) {
#pragma unroll
      for (int k = 0; k < 8; k++) v[k] = cmul(v[k], w64);
      put8(v, 16 * r);
    }
    f32x2 v1 = seed(1.f), W = seed(256.f);
    v1.y = -v1.y; W.y = -W.y;
    f32x2 u[8];
    u[0] = f32x2{2.f * (bf2f(r_cre) * dt), -2.f * (bf2f(r_cim) * dt)};
    const f32x2 v2c = cmul(v1, v1);
    u[1] = cmul(u[0], v1);
#pragma unroll
    for (int k = 2; k < 8; k++) u[k] = cmul(u[k - 2], v2c);
    put8(u, 64);
    v[0] = cmul(u[0], W); v[1] = cmul(u[2], W); v[2] = cmul(u[4], W); v[3] = cmul(u[6], W);
    v[4] = cmul(v[0], W); v[5] = cmul(v[2], W); v[6] = cmul(v[4], W); v[7] = cmul(v[5], W);
    put8(v, 80);
    u[0] = cmul(v[6], W); u[1] = cmul(u[0], W); u[2] = cmul(u[1], W); u[3] = cmul(u[2], W); u[4] = cmul(u[3], W);
    u[5] = u[6] = u[7] = f32x2{0.f, 0.f};
    put8(u, 96);
  }
  ST(3);

  if (!spare) {
    f32x16 acc[2][2];
    {
      const f32x16 z = {};
      const u32x4 one = {hh ? 0u : 0x3f80u, 0u, 0u, 0u};
      const u32x4 ba = {r_b1a, 0u, 0u, 0u}, bb = {r_b1b, 0u, 0u, 0u};
      acc[0][0] = MF32(ba, one, z); acc[0][1] = MF32(ba, one, z);
      acc[1][0] = MF32(bb, one, z); acc[1][1] = MF32(bb, one, z);
    }
    const u32 xa = sb + (64 * wm + (lane & 31)) * 128 + ((hh ^ ((lane >> 1) & 7)) << 4);
    const u32 wa = sb + 16384 + 256 * (8 * hh + q4) + 8 * (p4 & 1) + (((8 * wn + 2 * blk + (p4 >> 1)) ^ ((q4 << 2) | (2 * hh))) << 4);
    u32x4 fx[2][2], fw[2][2];
    auto rd = [&](int buf, u32 slot, int ks) {
      fx[buf][0] = lds128((xa ^ (ks << 5)) + slot);
      fx[buf][1] = lds128((xa ^ (ks << 5)) + slot + 4096);
      fw[buf][0] = ldstr(wa + slot + ks * 4096, (wa ^ 16) + slot + ks * 4096 + 1024);
      fw[buf][1] = ldstr((wa ^ 64) + slot + ks * 4096, (wa ^ 80) + slot + ks * 4096 + 1024);
    };
    auto mm = [&](int buf) {
      acc[0][0] = MF32(fw[buf][0], fx[buf][0], acc[0][0]);
      acc[0][1] = MF32(fw[buf][0], fx[buf][1], acc[0][1]);
      acc[1][0] = MF32(fw[buf][1], fx[buf][0], acc[1][0]);
      acc[1][1] = MF32(fw[buf][1], fx[buf][1], acc[1][1]);
    };
    VMCNT(16); BAR();
    ST(4);
    u32 slot = 0;
    rd(0, slot, 0);
#pragma unroll 1
    for (u32 s = 0; s < 8; s++) {
      rd(1, slot, 1); SB(); mm(0); SB();
      rd(0, slot, 2); SB(); mm(1); SB();
      rd(1, slot, 3); SB(); mm(0); SB();
      VMCNT(8); BAR();
      const u32 nslot = slot == 2 * SLOT ? 0 : slot + SLOT;
      rd(0, nslot, 0);
      SB();
      if (s < 5) stage1(s + 3, slot); else stage2(s - 5, slot);
      SB(); mm(1); SB();
      slot = nslot;
    }
    ST(5);
#pragma unroll
    for (int u = 0; u < 2; u++)
#pragma unroll
      for (int t = 0; t < 2; t++)
#pragma unroll
        for (int s = 0; s < 2; s++) {
          u32x4 o;
#pragma unroll
          for (int e = 0; e < 4; e++) {
            const float a0 = fmaxf(acc[u][t][8 * s + 2 * e], 0.f), a1 = fmaxf(acc[u][t][8 * s + 2 * e + 1], 0.f);
            o[e] = pk2(a0 * a0, a1 * a1);
          }
          __builtin_amdgcn_raw_buffer_store_b128(o, sWS, lane * 16 + WS_H + ((4 * p + 2 * wm + t) * 112 + cb1 * 8 + wn * 4 + 2 * u + s) * 1024, 0, 16);
        }
  } else {
    VMCNT(0); BAR();
  }
  SB();
  ST(6);
  u32x4 ut[4];
  auto load_ut = [&]() {
#pragma unroll
    for (int j = 0; j < 4; j++)
      asm volatile("buffer_load_dwordx4 %0, %1, %2, %3 offen sc1" : "=v"(ut[j]) : "v"(lane * 16), "s"(sWS), "s"(WS_UT + pair * 16384 + (4 * w + j) * 1024) : "memory");
  };
  load_ut();
  u32x4 w2r[16][2];
  const u32 w2a = sb + w * 8192 + 128 * (4 * hh + q4) + 8 * (p4 & 1) + (((4 * wn + 2 * blk + (p4 >> 1)) ^ ((q4 >> 1) << 2)) << 4);
  auto w2frag = [&](int st, int v) {
    const u32 a = (w2a ^ (v << 6)) + ((2 + (st >> 2)) % 3) * SLOT + (st & 3) * 2048;
    return ldstr(a, a + 1024);
  };
  auto xfer = [&](int c) {
#pragma unroll
    for (int j = 0; j < 4; j++) { w2r[4 * c + j][0] = w2frag(4 * c + j, 0); w2r[4 * c + j][1] = w2frag(4 * c + j, 1); }
    LGKM0();
    stage2(c + 3, ((2 + c) % 3) * SLOT);
  };
  VMCNT(28); xfer(0);
  {
    f32x4 ka[2] = {{(lane == 0 && wn == 0) ? bf2f(wm ? t_dsk >> 16 : t_dsk & 0xffffu) : 0.f, 0.f, 0.f, 0.f}, {0.f, 0.f, 0.f, 0.f}};
    const u32 t0 = sb + TAB + (wm * 128 + 4 * g4 + q4) * 224 + 8 * p4;
#pragma unroll
    for (int ks = 0; ks < 8; ks++) {
      const u32 a = t0 + ks * 16 * 224;
      const u32x4 fv = ldstr(a + 32 * wn, a + 32 * wn + 112);
      const u32x4 f0 = ldstr(a + 64, a + 64 + 112), f1 = ldstr(a + 96, a + 96 + 112);
      ka[0] = MF16(f0, fv, ka[0]);
      ka[1] = MF16(f1, fv, ka[1]);
    }
    const u32 ko = sb + KD + wm * KD_CH + (4 * g4 * 36 + 16 * wn + (lane & 15)) * 2;
#pragma unroll
    for (int rt = 0; rt < 2; rt++)
#pragma unroll
      for (int r = 0; r < 4; r += 2) {
        const u32 pr = pk2(ka[rt][r], ka[rt][r + 1]);
        *(LAS u16*)(ko + (16 * rt + r) * 72) = (u16)pr;
        *(LAS u16*)(ko + (16 * rt + r + 1) * 72) = (u16)(pr >> 16);
      }
  }
  VMCNT(28); xfer(1);
  VMCNT(20);
  BAR();
  if (tid == 0) asm volatile("buffer_store_dword %0, %1, %2, %3 offen sc1" ::"v"(tag), "v"(0u), "s"(sWS), "s"(WS_FLAG + p * 128 + (8 * hf + jj) * 4) : "memory");
  ST(7);
#pragma unroll
  for (int c = 0; c < 2; c++) {
    u32 e[9];
#pragma unroll
    for (int k = 0; k < 9; k++) e[k] = *(const LAS u16*)(ga[k] + c * KD_CH);
    u32x4 r1 = {e[8] | (e[7] << 16), e[6] | (e[5] << 16), e[4] | (e[3] << 16), e[2] | (e[1] << 16)};
    u32x4 r0 = {e[7] | (e[6] << 16), e[5] | (e[4] << 16), e[3] | (e[2] << 16), e[1] | (e[0] << 16)};
    *(LAS u32x4*)(sb + TC + (2 * c) * TC_STR + tid * 16) = r0;
    *(LAS u32x4*)(sb + TC + (2 * c + 1) * TC_STR + tid * 16) = r1;
  }
  if (tid < 8) *(LAS u32x4*)(sb + TC + (tid >> 1) * TC_STR + 4096 + (tid & 1) * 16) = u32x4{0, 0, 0, 0};
  *(LAS u32*)(sb + US + (tid >> 7) * US_STR + (tid & 127) * 4) = 0;
  asm volatile("s_waitcnt vmcnt(16)" : "+v"(ut[0]), "+v"(ut[1]), "+v"(ut[2]), "+v"(ut[3])::"memory");
  _Pragma("unroll 1") for (u32 spin = 0; spin < SPIN_CAP; spin++) {
    bool ok = true;
#pragma unroll
    for (int j = 0; j < 4; j++) ok &= ut[j][1] == tag && ut[j][3] == tag;
    if (__all(ok)) break;
    load_ut();
    asm volatile("s_waitcnt vmcnt(0)" : "+v"(ut[0]), "+v"(ut[1]), "+v"(ut[2]), "+v"(ut[3])::"memory");
  }
#pragma unroll
  for (int j = 0; j < 4; j++) {
    const u32 a = sb + US + 512 + ((4 * w + j) * 64 + lane) * 4;
    *(LAS u32*)a = __builtin_amdgcn_perm(ut[j][2], ut[j][0], 0x05040100);
    *(LAS u32*)(a + US_STR) = __builtin_amdgcn_perm(ut[j][2], ut[j][0], 0x07060302);
  }
  xfer(2);
  BAR();
  ST(8);

  u32x4 hfr[28][2];
  f32x4 ca[8];
#pragma unroll
  for (int c = 0; c < 8; c++) ca[c] = f32x4{0.f, 0.f, 0.f, 0.f};
  const u32 ua = sb + US + wm * US_STR + (256 - 16 - 128 * wn - 96 + 16 * (lane & 15) + 8 * g4) * 2;
  const u32 tb = sb + TC + (2 * wm + (lane & 1)) * TC_STR + (2046 - 16 - 128 * wn - 96 - 1792 + 8 * g4 - (lane & 14)) * 2;
  auto phase = [&](int f) {
    u32x4 fu[8], ft[8];
#pragma unroll
    for (int e = 0; e < 8; e++) {
      fu[e] = lds128(ua + (96 - 32 * f + 256 * e) * 2);
      const LAS u32* t = (const LAS u32*)(tb + (96 - 32 * f + 1792 - 256 * e) * 2);
      ft[e] = u32x4{t[0], t[1], t[2], t[3]};
    }
#pragma unroll
    for (int i = 0; i < 8; i++)
#pragma unroll
      for (int e = 0; e + i < 8; e++) ca[i + e] = MF16(fu[e], ft[i], ca[i + e]);
  };
  phase(0);
  VMCNT(16); xfer(3);
  u32 fl;
  const u32 fo = (lane & 15) * 4;
  asm volatile("buffer_load_dword %0, %1, %2, %3 offen sc1" : "=v"(fl) : "v"(fo), "s"(sWS), "s"(WS_FLAG + p * 128) : "memory");
  phase(1);
  phase(2);
  asm volatile("s_waitcnt vmcnt(0)" : "+v"(fl)::"memory");
  ST(9);
  _Pragma("unroll 1") for (u32 spin = 0; spin < SPIN_CAP; spin++) {
    if (__all(fl == tag)) break;
    asm volatile("buffer_load_dword %0, %1, %2, %3 offen sc1\n\ts_waitcnt vmcnt(0)" : "=&v"(fl) : "v"(fo), "s"(sWS), "s"(WS_FLAG + p * 128) : "memory");
  }
  ST(10);
  SB();
#pragma unroll
  for (int st = 0; st < 28; st++)
#pragma unroll
    for (int t = 0; t < 2; t++)
      hfr[st][t] = __builtin_amdgcn_raw_buffer_load_b128(sWS, lane * 16, WS_H + ((2 * rb + (t ^ wm)) * 112 + 28 * w + st) * 1024, 16);
  SB();
  phase(3);
  {
    f32x4 keep[4];
    const u32 ro = sb + RED + w * 4096 + lane * 16;
    if (wn) {
#pragma unroll
      for (int c = 0; c < 4; c++) { *(LAS f32x4*)(ro + c * 1024) = ca[c]; keep[c] = ca[c + 4]; }
      asm volatile("" ::: "memory");
    } else {
#pragma unroll
      for (int c = 0; c < 4; c++) { *(LAS f32x4*)(ro + c * 1024) = ca[c + 4]; keep[c] = ca[c]; }
    }
    BAR();
#pragma unroll
    for (int c = 0; c < 4; c++) {
      const f32x4 y = keep[c] + *(const LAS f32x4*)((ro ^ 4096) + c * 1024);
      u32x4 gr = {pk2(y[0], y[1]), tag, pk2(y[2], y[3]), tag};
      __builtin_amdgcn_raw_buffer_store_b128(gr, sWS, lane * 16 + WS_AT + (2 * pair + wm) * 8192 + (4 * wn + c) * 1024, 0, 16);
    }
  }
  SB();
  ST(11);

  f32x16 oc[2][2] = {};
  u32x4 at[4];
  u32x2 gg, b2v[4];
  auto load_at = [&]() {
#pragma unroll
    for (int j = 0; j < 4; j++)
      at[j] = __builtin_amdgcn_raw_buffer_load_b128(sWS, g4 * 8192 + (lane & 15) * 16, WS_AT + (64 * cb2 + 16 * w + 4 * j) * 8192 + rb * 256, 16);
    gg = __builtin_amdgcn_raw_buffer_load_b64(sWS, (lane & 31) * 8, WS_G + (64 * rb + 32 * wm) * 8, 16);
  };
#pragma unroll
  for (int st = 0; st < 28; st++) {
    const u32x4 a0 = st < 16 ? w2r[st < 16 ? st : 0][0] : w2frag(st, 0);
    const u32x4 a1 = st < 16 ? w2r[st < 16 ? st : 0][1] : w2frag(st, 1);
    oc[0][0] = MF32(a0, hfr[st][0], oc[0][0]);
    oc[1][0] = MF32(a1, hfr[st][0], oc[1][0]);
    oc[0][1] = MF32(a0, hfr[st][1], oc[0][1]);
    oc[1][1] = MF32(a1, hfr[st][1], oc[1][1]);
    if (st == 19) {
      SB();
      load_at();
#pragma unroll
      for (int q = 0; q < 4; q++) b2v[q] = *(const u32x2*)(A.b2 + 64 * cb2 + 32 * wn + 8 * q + 4 * hh);
      SB();
    }
  }
  ST(12);
  LGKM0();
#pragma unroll
  for (int j = 1; j < 4; j++)
#pragma unroll
    for (int q = 0; q < 4; q++) {
      const f32x16& s = oc[j & 1][j >> 1];
      *(LAS f32x4*)(sb + (j - 1) * SLOT + w * 8192 + q * 1024 + lane * 16) = f32x4{s[4 * q], s[4 * q + 1], s[4 * q + 2], s[4 * q + 3]};
    }
  _Pragma("unroll 1") for (u32 spin = 0; spin < SPIN_CAP; spin++) {
    bool ok = gg[1] == tag;
#pragma unroll
    for (int j = 0; j < 4; j++) ok &= at[j][1] == tag && at[j][3] == tag;
    if (__all(ok)) break;
    asm volatile("" ::: "memory");
    load_at();
  }
#pragma unroll
  for (int j = 0; j < 4; j++) {
    const u32 a = sb + AS + (lane & 15) * 144 + (16 * w + 4 * j + g4) * 2;
    *(LAS u16*)(a) = (u16)at[j][0];
    *(LAS u16*)(a + 16 * 144) = (u16)(at[j][0] >> 16);
    *(LAS u16*)(a + 32 * 144) = (u16)at[j][2];
    *(LAS u16*)(a + 48 * 144) = (u16)(at[j][2] >> 16);
  }
  BAR();
  ST(13);
  {
    const float g = bf2f(gg[0] & 0xffffu), gm = __uint_as_float(gg[0] & 0xffff0000u);
    const u32 aa = sb + AS + (32 * wm + (lane & 31)) * 144 + (32 * wn + 4 * hh) * 2;
#pragma unroll
    for (int q = 0; q < 4; q++) {
      f32x4 s = {oc[0][0][4 * q], oc[0][0][4 * q + 1], oc[0][0][4 * q + 2], oc[0][0][4 * q + 3]};
#pragma unroll
      for (int j = 1; j < 4; j++) s += *(const LAS f32x4*)(sb + (j - 1) * SLOT + (w ^ j) * 8192 + q * 1024 + lane * 16);
      const u32x2 av = *(const LAS u32x2*)(aa + 16 * q);
      const float o0 = g * bf2f(av[0] & 0xffffu) + gm * (s[0] + bf2f(b2v[q][0] & 0xffffu));
      const float o1 = g * __uint_as_float(av[0] & 0xffff0000u) + gm * (s[1] + __uint_as_float(b2v[q][0] & 0xffff0000u));
      const float o2 = g * bf2f(av[1] & 0xffffu) + gm * (s[2] + bf2f(b2v[q][1] & 0xffffu));
      const float o3 = g * __uint_as_float(av[1] & 0xffff0000u) + gm * (s[3] + __uint_as_float(b2v[q][1] & 0xffff0000u));
      *(LAS u32x2*)(aa + 16 * q) = u32x2{pk2(o0, o1), pk2(o2, o3)};
    }
    LGKM0();
#pragma unroll
    for (int j = 0; j < 2; j++) {
      const u32 r = 32 * wm + 16 * j + (lane >> 2);
      const u32x4 o = lds128(sb + AS + r * 144 + 64 * wn + (lane & 3) * 16);
      __builtin_amdgcn_raw_buffer_store_b128(o, sOut, r * 1024 + (lane & 3) * 16 + rb * 65536 + cb2 * 128 + wn * 64, 0, 0);
    }
  }
  if (b == 0 && w == 3) {
    _Pragma("unroll 1") for (u32 spin = 0; spin < SPIN_CAP; spin++) {
      asm volatile("" ::: "memory");
      const u32x4 f4 = __builtin_amdgcn_raw_buffer_load_b128(sWS, (lane >> 2) * 128 + (lane & 3) * 16, WS_FLAG, 16);
      if (__all(f4[0] == tag && f4[1] == tag && f4[2] == tag && f4[3] == tag)) break;
    }
    if (lane == 0) __builtin_amdgcn_raw_buffer_store_b32(tag, sWS, 0, 0, 16);
  }
  ST(14);
  VMCNT(0);
  ST(15);
  u64 cyc1; asm volatile("s_memtime %0\n\ts_waitcnt lgkmcnt(0)" : "=s"(cyc1)::"memory");
  if (b == 8 && w == 0) { const u64 until = stamp[15] + 10 * (stamp[9] - stamp[8]); for (u32 i = 0; i < (1u << 22); i++) { if (__builtin_amdgcn_s_memrealtime() >= until) break; } }

}

extern "C" void kernel_launch(void* const* d_in, const int* in_sizes, int n_in, void* d_out, int out_size,
                              void* d_ws, size_t ws_size, hipStream_t stream) {
  Args a;
  a.x = (const u16*)d_in[0]; a.lar = (const u16*)d_in[1]; a.aim = (const u16*)d_in[2]; a.cre = (const u16*)d_in[3];
  a.cim = (const u16*)d_in[4]; a.ldt = (const u16*)d_in[5]; a.dsk = (const u16*)d_in[6]; a.w1 = (const u16*)d_in[8];
  a.b1 = (const u16*)d_in[9]; a.w2 = (const u16*)d_in[10]; a.b2 = (const u16*)d_in[11]; a.gw = (const u16*)d_in[12];
  a.out = (u16*)d_out; a.ws = (char*)d_ws;
  fused_mix<<<256, 256, 0, stream>>>(a);
}
```

```cpp
#include <hip/hip_runtime.h>
#pragma clang diagnostic ignored "-Wint-to-pointer-cast"

typedef unsigned short u16;
typedef unsigned u32;
typedef unsigned long long u64;
typedef __bf16 h16x8 __attribute__((ext_vector_type(8)));
typedef __bf16 h16x2 __attribute__((ext_vector_type(2)));
typedef short s16x4 __attribute__((ext_vector_type(4)));
typedef short s16x8 __attribute__((ext_vector_type(8)));
typedef short s16x2 __attribute__((ext_vector_type(2)));
typedef float f32x2 __attribute__((ext_vector_type(2)));
typedef float f32x4 __attribute__((ext_vector_type(4)));
typedef float f32x16 __attribute__((ext_vector_type(16)));
typedef u32 u32x2 __attribute__((ext_vector_type(2)));
typedef u32 u32x4 __attribute__((ext_vector_type(4)));
#define LAS __attribute__((address_space(3)))
#define CAS __attribute__((address_space(4)))
#define DI __device__ __forceinline__

constexpr int L = 2048, D = 512, F = 1792;
constexpr u32 WS_FLAG = 4096;
constexpr u32 WS_G = 65536;
constexpr u32 WS_UT = 1u << 20;
constexpr u32 WS_AT = 5u << 20;
constexpr u32 WS_H = 9u << 20;
constexpr u32 WS_END = 17u << 20;
constexpr u32 WS_STAMP = 32u << 20;
constexpr u32 SLOT = 32768;
constexpr u32 TAB = 3 * SLOT;
constexpr u32 KD = TAB + 256 * 224;
constexpr u32 KD_CH = 32 * 36 * 2;
constexpr u32 KD_ZERO = 34 * 2;
constexpr u32 ZS = KD + 2 * KD_CH;
constexpr u32 RED = TAB;
constexpr u32 TC = RED + 16384;
constexpr u32 TC_STR = 4160;
constexpr u32 US = TC + 4 * TC_STR;
constexpr u32 US_STR = (256 + 2048) * 2;
constexpr u32 AS = US + 2 * US_STR;
constexpr u32 LDS_BYTES = 163840;
static_assert(AS + 64 * 144 <= KD && ZS + 2048 <= LDS_BYTES, "lds map");
constexpr u32 SPIN_CAP = 1u << 16;

DI float bf2f(u32 v) { return __uint_as_float(v << 16); }
DI u32 pk2(float lo, float hi) { f32x2 a = {lo, hi}; return __builtin_bit_cast(u32, __builtin_convertvector(a, h16x2)); }
DI float rbf(float x) { return bf2f(pk2(x, x) & 0xffffu); }
DI float dot2(u32 a, u32 b, float c) { return __builtin_amdgcn_fdot2_f32_bf16(__builtin_bit_cast(h16x2, a), __builtin_bit_cast(h16x2, b), c, false); }
DI u32 rfl(u32 x) { return __builtin_amdgcn_readfirstlane(x); }
DI f32x2 cmul(f32x2 a, f32x2 b) { f32x2 r; r.x = a.x * b.x - a.y * b.y; r.y = a.x * b.y + a.y * b.x; return r; }
#define MF32(a, b, c) __builtin_amdgcn_mfma_f32_32x32x16_bf16(__builtin_bit_cast(h16x8, a), __builtin_bit_cast(h16x8, b), c, 0, 0, 0)
#define MF16(a, b, c) __builtin_amdgcn_mfma_f32_16x16x32_bf16(__builtin_bit_cast(h16x8, a), __builtin_bit_cast(h16x8, b), c, 0, 0, 0)
#define DMA(voff, srd, soff, dst) \
  asm volatile("s_mov_b32 m0, %0\n\ts_nop 0\n\tbuffer_load_dwordx4 %1, %2, %3 offen lds" ::"s"(dst), "v"(voff), "s"(srd), "s"(soff) : "memory")
#define VMCNT(n) asm volatile("s_waitcnt vmcnt(%0)" ::"i"(n) : "memory")
#define LGKM0() asm volatile("s_waitcnt lgkmcnt(0)" ::: "memory")
#define BAR() asm volatile("s_waitcnt lgkmcnt(0)\n\ts_barrier" ::: "memory")
#define SB() __builtin_amdgcn_sched_barrier(0)
#define ST(i)

DI u32x4 lds128(u32 a) { return *(const LAS u32x4*)a; }
DI u32x4 ldstr(u32 a0, u32 a1) {
  s16x4 lo = __builtin_amdgcn_ds_read_tr16_b64_v4i16((LAS s16x4*)a0);
  s16x4 hi = __builtin_amdgcn_ds_read_tr16_b64_v4i16((LAS s16x4*)a1);
  return __builtin_bit_cast(u32x4, __builtin_shufflevector(lo, hi, 0, 1, 2, 3, 4, 5, 6, 7));
}

struct Args {
  const u16 *x, *lar, *aim, *cre, *cim, *ldt, *dsk, *w1, *b1, *w2, *b2, *gw;
  u16* out;
  char* ws;
};

__global__ void __launch_bounds__(256, 1) fused_mix(Args A) {
  __shared__ __attribute__((aligned(16))) char smem[LDS_BYTES];
  const u32 sb = (u32)(size_t)(LAS char*)smem;
  ST(0);
  const u32 tid = threadIdx.x, lane = tid & 63, w = rfl(tid >> 6);
  const u32 b = blockIdx.x, xcd = b & 7, bi = b >> 3;
  const u32 hf = xcd & 1, p = 4 * (xcd >> 1) + (bi >> 3), jj = bi & 7;
  const bool spare = jj == 7;
  const u32 cb1 = 7 * hf + jj, rb = 2 * p + hf, cb2 = jj, pair = b;
  const u32 rot = 2 * (bi >> 3) + jj;
  const u32 wm = w >> 1, wn = w & 1;
  const u32 hh = lane >> 5, blk = (lane >> 4) & 1, q4 = (lane & 15) >> 2, p4 = lane & 3, g4 = lane >> 4;

  const __amdgpu_buffer_rsrc_t sX = __builtin_amdgcn_make_buffer_rsrc((void*)A.x, 0, L * D * 2, 0x00020000);
  const __amdgpu_buffer_rsrc_t sW1 = __builtin_amdgcn_make_buffer_rsrc((void*)A.w1, 0, D * F * 2, 0x00020000);
  const __amdgpu_buffer_rsrc_t sW2 = __builtin_amdgcn_make_buffer_rsrc((void*)A.w2, 0, D * F * 2, 0x00020000);
  const __amdgpu_buffer_rsrc_t sWS = __builtin_amdgcn_make_buffer_rsrc((void*)A.ws, 0, WS_END, 0x00020000);
  const __amdgpu_buffer_rsrc_t sOut = __builtin_amdgcn_make_buffer_rsrc((void*)A.out, 0, L * D * 2, 0x00020000);

  u32 r_lar, r_aim, r_cre, r_cim, r_b1a, r_b1b;
  {
    const u32 o = (pair * 256 + tid) * 2;
    asm volatile("global_load_ushort %0, %1, %2" : "=v"(r_lar) : "v"(o), "s"(A.lar));
    asm volatile("global_load_ushort %0, %1, %2" : "=v"(r_aim) : "v"(o), "s"(A.aim));
    asm volatile("global_load_ushort %0, %1, %2" : "=v"(r_cre) : "v"(o), "s"(A.cre));
    asm volatile("global_load_ushort %0, %1, %2" : "=v"(r_cim) : "v"(o), "s"(A.cim));
    const u32 o1 = ((spare ? 0 : cb1) * 128 + wn * 64 + (lane & 31)) * 2;
    asm volatile("global_load_ushort %0, %1, %2" : "=v"(r_b1a) : "v"(o1), "s"(A.b1));
    asm volatile("global_load_ushort %0, %1, %2 offset:64" : "=v"(r_b1b) : "v"(o1), "s"(A.b1));
  }
  const u32 tag = *(const CAS u32*)A.ws + 1u;
  const u32 t_ldt = *(const CAS u32*)(A.ldt + 2 * pair), t_dsk = *(const CAS u32*)(A.dsk + 2 * pair);

  const u32 vx0 = (lane >> 3) * 1024 + (((lane & 7) ^ g4) << 4), vx1 = vx0 ^ 64;
  const u32 vw0 = g4 * (F * 2) + (((lane & 15) ^ (g4 << 2)) << 4);
  const u32 v2 = (lane >> 3) * 1024 + (((lane & 7) ^ (blk << 2)) << 4);
  auto stage1 = [&](u32 s, u32 slot) {
    const u32 ks = (s + rot) & 7;
    const u32 sx = (128 * p + 32 * w) * 1024 + ks * 128, dx = sb + slot + w * 4096;
    const u32 sw = (ks * 64 + 16 * w) * (F * 2) + cb1 * 256, dw = dx + 16384;
#pragma unroll
    for (int q = 0; q < 4; q++) {
      DMA((q & 1) ? vx1 : vx0, sX, sx + q * 8192, dx + q * 1024);
      DMA(vw0 ^ (q << 4), sW1, sw + q * 4 * (F * 2), dw + q * 1024);
    }
  };
  auto stage2 = [&](u32 c, u32 slot) {
    const u32 so = (28 * w + 4 * c) * 16 * 1024 + cb2 * 128, dd = sb + slot + w * 8192;
#pragma unroll
    for (int q = 0; q < 8; q++) DMA(v2, sW2, so + q * 8192, dd + q * 1024);
  };

  if (!spare) {
    stage1(0, 0); stage1(1, SLOT); stage1(2, 2 * SLOT);
  } else {
    const u32 vb = g4 * 1024 + (((lane & 15) ^ hh) << 4);
#pragma unroll
    for (int q = 0; q < 16; q++) DMA(vb ^ (((2 * q) & 15) << 4), sX, (64 * rb + 4 * q) * 1024 + w * 256, sb + w * 16384 + q * 1024);
    u32x4 gwv[8];
#pragma unroll
    for (int m = 0; m < 8; m++) gwv[m] = *(const u32x4*)(A.gw + 128 * w + 16 * m + 8 * hh);
    VMCNT(0);
    const u32 i = lane & 31;
    const u32 ra = sb + w * 16384 + i * 512 + ((hh ^ (i & 15)) << 4);
    const u32 vo = hh * 4 * 16384 + i * 16;
    float za = 0.f, zb = 0.f;
#pragma unroll
    for (int m = 0; m < 8; m++) {
      const u32x4 ea = lds128(ra ^ (m << 5)), eb = lds128((ra ^ (m << 5)) + 256);
#pragma unroll
      for (int e = 0; e < 4; e++) {
        u32x4 gr = {ea[e], tag, eb[e], tag};
        __builtin_amdgcn_raw_buffer_store_b128(gr, sWS, vo + WS_UT + (64 * w + 8 * m + e) * 16384 + rb * 512, 0, 16);
        za = dot2(ea[e], gwv[m][e], za);
        zb = dot2(eb[e], gwv[m][e], zb);
      }
    }
    *(LAS f32x2*)(sb + ZS + (2 * w + hh) * 256 + i * 8) = f32x2{za, zb};
    BAR();
    if (w == 0) {
      float z = 0.f;
#pragma unroll
      for (int k = 0; k < 8; k++) z += *(const LAS float*)(sb + ZS + k * 256 + lane * 4);
      z = rbf(z);
      const float g = rbf(1.f / (1.f + __expf(-z)));
      u32x2 gr = {pk2(g, 1.f - g), tag};
      __builtin_amdgcn_raw_buffer_store_b64(gr, sWS, lane * 8, WS_G + rb * 512, 16);
    }
    stage2(0, 2 * SLOT); stage2(1, 0); stage2(2, SLOT);
  }
  ST(1);

  u32 ga[9];
#pragma unroll
  for (int e = 0; e < 9; e++) {
    const int tap = 2040 - 8 * (int)tid + e - 1;
    const u32 pos = (u32)rbf((float)(tap < 0 ? 0 : tap));
    const u32 hi = pos >> 8, lo = pos & 7;
    const u32 ex = min(3u, 32u - (u32)__clz(hi));
    const u32 rho = 4 * ex + ((8 * hi + lo) >> ex);
    ga[e] = sb + KD + (tap < 0 ? KD_ZERO : (rho * 36 + ((pos & 255) >> 3)) * 2);
    asm volatile("" : "+v"(ga[e]));
  }
  if (tid < 2) *(LAS u32*)(sb + KD + tid * KD_CH + KD_ZERO) = 0;

  asm volatile("s_waitcnt vmcnt(24)" : "+v"(r_lar), "+v"(r_aim), "+v"(r_cre), "+v"(r_cim), "+v"(r_b1a), "+v"(r_b1b));
  ST(2);
  {
    const float dt = rbf(__expf(bf2f(wm ? t_ldt >> 16 : t_ldt & 0xffffu)));
    const float er = -(rbf(__expf(bf2f(r_lar))) * dt) * 1.44269504088896f;
    const float wi = (bf2f(r_aim) * dt) * 0.159154943091895f;
    auto seed = [&](float t) {
      const float m = __builtin_amdgcn_exp2f(er * t), a = __builtin_amdgcn_fractf(wi * t);
      return f32x2{m * __builtin_amdgcn_cosf(a), m * __builtin_amdgcn_sinf(a)};
    };
    const u32 ta = sb + TAB + tid * 224;
    auto put8 = [&](const f32x2* v, u32 off) {
      u32x4 re = {pk2(v[0].x, v[1].x), pk2(v[2].x, v[3].x), pk2(v[4].x, v[5].x), pk2(v[6].x, v[7].x)};
      u32x4 im = {pk2(v[0].y, v[1].y), pk2(v[2].y, v[3].y), pk2(v[4].y, v[5].y), pk2(v[6].y, v[7].y)};
      *(LAS u32x4*)(ta + off) = re;
      *(LAS u32x4*)(ta + 112 + off) = im;
    };
    f32x2 v[8];
    const f32x2 w8 = seed(8.f), w64 = seed(64.f);
    v[0] = f32x2{1.f, 0.f}; v[1] = w8; v[2] = cmul(w8, w8); v[3] = cmul(v[2], w8);
    v[4] = cmul(v[2], v[2]); v[5] = cmul(v[4], w8); v[6] = cmul(v[4], v[2]); v[7] = cmul(v[4], v[3]);
    put8(v, 0);
#pragma unroll
    for (int r = 1; r < 4; r++) {
#pragma unroll
      for (int k = 0; k < 8; k++) v[k] = cmul(v[k], w64);
      put8(v, 16 * r);
    }
    f32x2 v1 = seed(1.f), W = seed(256.f);
    v1.y = -v1.y; W.y = -W.y;
    f32x2 u[8];
    u[0] = f32x2{2.f * (bf2f(r_cre) * dt), -2.f * (bf2f(r_cim) * dt)};
    const f32x2 v2c = cmul(v1, v1);
    u[1] = cmul(u[0], v1);
#pragma unroll
    for (int k = 2; k < 8; k++) u[k] = cmul(u[k - 2], v2c);
    put8(u, 64);
    v[0] = cmul(u[0], W); v[1] = cmul(u[2], W); v[2] = cmul(u[4], W); v[3] = cmul(u[6], W);
    v[4] = cmul(v[0], W); v[5] = cmul(v[2], W); v[6] = cmul(v[4], W); v[7] = cmul(v[5], W);
    put8(v, 80);
    u[0] = cmul(v[6], W); u[1] = cmul(u[0], W); u[2] = cmul(u[1], W); u[3] = cmul(u[2], W); u[4] = cmul(u[3], W);
    u[5] = u[6] = u[7] = f32x2{0.f, 0.f};
    put8(u, 96);
  }
  ST(3);

  if (!spare) {
    f32x16 acc[2][2];
    {
      const f32x16 z = {};
      const u32x4 one = {hh ? 0u : 0x3f80u, 0u, 0u, 0u};
      const u32x4 ba = {r_b1a, 0u, 0u, 0u}, bb = {r_b1b, 0u, 0u, 0u};
      acc[0][0] = MF32(ba, one, z); acc[0][1] = MF32(ba, one, z);
      acc[1][0] = MF32(bb, one, z); acc[1][1] = MF32(bb, one, z);
    }
    const u32 xa = sb + (64 * wm + (lane & 31)) * 128 + ((hh ^ ((lane >> 1) & 7)) << 4);
    const u32 wa = sb + 16384 + 256 * (8 * hh + q4) + 8 * (p4 & 1) + (((8 * wn + 2 * blk + (p4 >> 1)) ^ ((q4 << 2) | (2 * hh))) << 4);
    u32x4 fx[2][2], fw[2][2];
    auto rd = [&](int buf, u32 slot, int ks) {
      fx[buf][0] = lds128((xa ^ (ks << 5)) + slot);
      fx[buf][1] = lds128((xa ^ (ks << 5)) + slot + 4096);
      fw[buf][0] = ldstr(wa + slot + ks * 4096, (wa ^ 16) + slot + ks * 4096 + 1024);
      fw[buf][1] = ldstr((wa ^ 64) + slot + ks * 4096, (wa ^ 80) + slot + ks * 4096 + 1024);
    };
    auto mm = [&](int buf) {
      acc[0][0] = MF32(fw[buf][0], fx[buf][0], acc[0][0]);
      acc[0][1] = MF32(fw[buf][0], fx[buf][1], acc[0][1]);
      acc[1][0] = MF32(fw[buf][1], fx[buf][0], acc[1][0]);
      acc[1][1] = MF32(fw[buf][1], fx[buf][1], acc[1][1]);
    };
    VMCNT(16); BAR();
    ST(4);
    u32 slot = 0;
    rd(0, slot, 0);
#pragma unroll 1
    for (u32 s = 0; s < 8; s++) {
      rd(1, slot, 1); SB(); mm(0); SB();
      rd(0, slot, 2); SB(); mm(1); SB();
      rd(1, slot, 3); SB(); mm(0); SB();
      VMCNT(8); BAR();
      const u32 nslot = slot == 2 * SLOT ? 0 : slot + SLOT;
      rd(0, nslot, 0);
      SB();
      if (s < 5) stage1(s + 3, slot); else stage2(s - 5, slot);
      SB(); mm(1); SB();
      slot = nslot;
    }
    ST(5);
#pragma unroll
    for (int u = 0; u < 2; u++)
#pragma unroll
      for (int t = 0; t < 2; t++)
#pragma unroll
        for (int s = 0; s < 2; s++) {
          u32x4 o;
#pragma unroll
          for (int e = 0; e < 4; e++) {
            const float a0 = fmaxf(acc[u][t][8 * s + 2 * e], 0.f), a1 = fmaxf(acc[u][t][8 * s + 2 * e + 1], 0.f);
            o[e] = pk2(a0 * a0, a1 * a1);
          }
          __builtin_amdgcn_raw_buffer_store_b128(o, sWS, lane * 16 + WS_H + ((4 * p + 2 * wm + t) * 112 + cb1 * 8 + wn * 4 + 2 * u + s) * 1024, 0, 16);
        }
  } else {
    VMCNT(0); BAR();
  }
  SB();
  ST(6);
  u32x4 ut[4];
  auto load_ut = [&]() {
#pragma unroll
    for (int j = 0; j < 4; j++)
      asm volatile("buffer_load_dwordx4 %0, %1, %2, %3 offen sc1" : "=v"(ut[j]) : "v"(lane * 16), "s"(sWS), "s"(WS_UT + pair * 16384 + (4 * w + j) * 1024) : "memory");
  };
  load_ut();
  u32x4 w2r[16][2];
  const u32 w2a = sb + w * 8192 + 128 * (4 * hh + q4) + 8 * (p4 & 1) + (((4 * wn + 2 * blk + (p4 >> 1)) ^ ((q4 >> 1) << 2)) << 4);
  auto w2frag = [&](int st, int v) {
    const u32 a = (w2a ^ (v << 6)) + ((2 + (st >> 2)) % 3) * SLOT + (st & 3) * 2048;
    return ldstr(a, a + 1024);
  };
  auto xfer = [&](int c) {
#pragma unroll
    for (int j = 0; j < 4; j++) { w2r[4 * c + j][0] = w2frag(4 * c + j, 0); w2r[4 * c + j][1] = w2frag(4 * c + j, 1); }
    LGKM0();
    stage2(c + 3, ((2 + c) % 3) * SLOT);
  };
  VMCNT(28); xfer(0);
  {
    f32x4 ka[2] = {{(lane == 0 && wn == 0) ? bf2f(wm ? t_dsk >> 16 : t_dsk & 0xffffu) : 0.f, 0.f, 0.f, 0.f}, {0.f, 0.f, 0.f, 0.f}};
    const u32 t0 = sb + TAB + (wm * 128 + 4 * g4 + q4) * 224 + 8 * p4;
#pragma unroll
    for (int ks = 0; ks < 8; ks++) {
      const u32 a = t0 + ks * 16 * 224;
      const u32x4 fv = ldstr(a + 32 * wn, a + 32 * wn + 112);
      const u32x4 f0 = ldstr(a + 64, a + 64 + 112), f1 = ldstr(a + 96, a + 96 + 112);
      ka[0] = MF16(f0, fv, ka[0]);
      ka[1] = MF16(f1, fv, ka[1]);
    }
    const u32 ko = sb + KD + wm * KD_CH + (4 * g4 * 36 + 16 * wn + (lane & 15)) * 2;
#pragma unroll
    for (int rt = 0; rt < 2; rt++)
#pragma unroll
      for (int r = 0; r < 4; r += 2) {
        const u32 pr = pk2(ka[rt][r], ka[rt][r + 1]);
        *(LAS u16*)(ko + (16 * rt + r) * 72) = (u16)pr;
        *(LAS u16*)(ko + (16 * rt + r + 1) * 72) = (u16)(pr >> 16);
      }
  }
  VMCNT(28); xfer(1);
  VMCNT(20);
  BAR();
  if (tid == 0) asm volatile("buffer_store_dword %0, %1, %2, %3 offen sc1" ::"v"(tag), "v"(0u), "s"(sWS), "s"(WS_FLAG + p * 128 + (8 * hf + jj) * 4) : "memory");
  ST(7);
#pragma unroll
  for (int c = 0; c < 2; c++) {
    u32 e[9];
#pragma unroll
    for (int k = 0; k < 9; k++) e[k] = *(const LAS u16*)(ga[k] + c * KD_CH);
    u32x4 r1 = {e[8] | (e[7] << 16), e[6] | (e[5] << 16), e[4] | (e[3] << 16), e[2] | (e[1] << 16)};
    u32x4 r0 = {e[7] | (e[6] << 16), e[5] | (e[4] << 16), e[3] | (e[2] << 16), e[1] | (e[0] << 16)};
    *(LAS u32x4*)(sb + TC + (2 * c) * TC_STR + tid * 16) = r0;
    *(LAS u32x4*)(sb + TC + (2 * c + 1) * TC_STR + tid * 16) = r1;
  }
  if (tid < 8) *(LAS u32x4*)(sb + TC + (tid >> 1) * TC_STR + 4096 + (tid & 1) * 16) = u32x4{0, 0, 0, 0};
  *(LAS u32*)(sb + US + (tid >> 7) * US_STR + (tid & 127) * 4) = 0;
  asm volatile("s_waitcnt vmcnt(16)" : "+v"(ut[0]), "+v"(ut[1]), "+v"(ut[2]), "+v"(ut[3])::"memory");
  _Pragma("unroll 1") for (u32 spin = 0; spin < SPIN_CAP; spin++) {
    bool ok = true;
#pragma unroll
    for (int j = 0; j < 4; j++) ok &= ut[j][1] == tag && ut[j][3] == tag;
    if (__all(ok)) break;
    load_ut();
    asm volatile("s_waitcnt vmcnt(0)" : "+v"(ut[0]), "+v"(ut[1]), "+v"(ut[2]), "+v"(ut[3])::"memory");
  }
#pragma unroll
  for (int j = 0; j < 4; j++) {
    const u32 a = sb + US + 512 + ((4 * w + j) * 64 + lane) * 4;
    *(LAS u32*)a = __builtin_amdgcn_perm(ut[j][2], ut[j][0], 0x05040100);
    *(LAS u32*)(a + US_STR) = __builtin_amdgcn_perm(ut[j][2], ut[j][0], 0x07060302);
  }
  xfer(2);
  BAR();
  ST(8);

  u32x4 hfr[28][2];
  f32x4 ca[8];
#pragma unroll
  for (int c = 0; c < 8; c++) ca[c] = f32x4{0.f, 0.f, 0.f, 0.f};
  const u32 ua = sb + US + wm * US_STR + (256 - 16 - 128 * wn - 96 + 16 * (lane & 15) + 8 * g4) * 2;
  const u32 tb = sb + TC + (2 * wm + (lane & 1)) * TC_STR + (2046 - 16 - 128 * wn - 96 - 1792 + 8 * g4 - (lane & 14)) * 2;
  auto phase = [&](int f) {
    u32x4 fu[8], ft[8];
#pragma unroll
    for (int e = 0; e < 8; e++) {
      fu[e] = lds128(ua + (96 - 32 * f + 256 * e) * 2);
      const LAS u32* t = (const LAS u32*)(tb + (96 - 32 * f + 1792 - 256 * e) * 2);
      ft[e] = u32x4{t[0], t[1], t[2], t[3]};
    }
#pragma unroll
    for (int i = 0; i < 8; i++)
#pragma unroll
      for (int e = 0; e + i < 8; e++) ca[i + e] = MF16(fu[e], ft[i], ca[i + e]);
  };
  phase(0);
  VMCNT(16); xfer(3);
  u32 fl;
  const u32 fo = (lane & 15) * 4;
  asm volatile("buffer_load_dword %0, %1, %2, %3 offen sc1" : "=v"(fl) : "v"(fo), "s"(sWS), "s"(WS_FLAG + p * 128) : "memory");
  phase(1);
  phase(2);
  asm volatile("s_waitcnt vmcnt(0)" : "+v"(fl)::"memory");
  ST(9);
  _Pragma("unroll 1") for (u32 spin = 0; spin < SPIN_CAP; spin++) {
    if (__all(fl == tag)) break;
    asm volatile("buffer_load_dword %0, %1, %2, %3 offen sc1\n\ts_waitcnt vmcnt(0)" : "=&v"(fl) : "v"(fo), "s"(sWS), "s"(WS_FLAG + p * 128) : "memory");
  }
  ST(10);
  SB();
#pragma unroll
  for (int st = 0; st < 28; st++)
#pragma unroll
    for (int t = 0; t < 2; t++)
      hfr[st][t] = __builtin_amdgcn_raw_buffer_load_b128(sWS, lane * 16, WS_H + ((2 * rb + (t ^ wm)) * 112 + 28 * w + st) * 1024, 16);
  SB();
  phase(3);
  {
    f32x4 keep[4];
    const u32 ro = sb + RED + w * 4096 + lane * 16;
    if (wn) {
#pragma unroll
      for (int c = 0; c < 4; c++) { *(LAS f32x4*)(ro + c * 1024) = ca[c]; keep[c] = ca[c + 4]; }
      asm volatile("" ::: "memory");
    } else {
#pragma unroll
      for (int c = 0; c < 4; c++) { *(LAS f32x4*)(ro + c * 1024) = ca[c + 4]; keep[c] = ca[c]; }
    }
    BAR();
#pragma unroll
    for (int c = 0; c < 4; c++) {
      const f32x4 y = keep[c] + *(const LAS f32x4*)((ro ^ 4096) + c * 1024);
      u32x4 gr = {pk2(y[0], y[1]), tag, pk2(y[2], y[3]), tag};
      __builtin_amdgcn_raw_buffer_store_b128(gr, sWS, lane * 16 + WS_AT + (2 * pair + wm) * 8192 + (4 * wn + c) * 1024, 0, 16);
    }
  }
  SB();
  ST(11);

  f32x16 oc[2][2] = {};
  u32x4 at[4];
  u32x2 gg, b2v[4];
  auto load_at = [&]() {
#pragma unroll
    for (int j = 0; j < 4; j++)
      at[j] = __builtin_amdgcn_raw_buffer_load_b128(sWS, g4 * 8192 + (lane & 15) * 16, WS_AT + (64 * cb2 + 16 * w + 4 * j) * 8192 + rb * 256, 16);
    gg = __builtin_amdgcn_raw_buffer_load_b64(sWS, (lane & 31) * 8, WS_G + (64 * rb + 32 * wm) * 8, 16);
  };
#pragma unroll
  for (int st = 0; st < 28; st++) {
    const u32x4 a0 = st < 16 ? w2r[st < 16 ? st : 0][0] : w2frag(st, 0);
    const u32x4 a1 = st < 16 ? w2r[st < 16 ? st : 0][1] : w2frag(st, 1);
    oc[0][0] = MF32(a0, hfr[st][0], oc[0][0]);
    oc[1][0] = MF32(a1, hfr[st][0], oc[1][0]);
    oc[0][1] = MF32(a0, hfr[st][1], oc[0][1]);
    oc[1][1] = MF32(a1, hfr[st][1], oc[1][1]);
    if (st == 19) {
      SB();
      load_at();
#pragma unroll
      for (int q = 0; q < 4; q++) b2v[q] = *(const u32x2*)(A.b2 + 64 * cb2 + 32 * wn + 8 * q + 4 * hh);
      SB();
    }
  }
  ST(12);
  LGKM0();
#pragma unroll
  for (int j = 1; j < 4; j++)
#pragma unroll
    for (int q = 0; q < 4; q++) {
      const f32x16& s = oc[j & 1][j >> 1];
      *(LAS f32x4*)(sb + (j - 1) * SLOT + w * 8192 + q * 1024 + lane * 16) = f32x4{s[4 * q], s[4 * q + 1], s[4 * q + 2], s[4 * q + 3]};
    }
  _Pragma("unroll 1") for (u32 spin = 0; spin < SPIN_CAP; spin++) {
    bool ok = gg[1] == tag;
#pragma unroll
    for (int j = 0; j < 4; j++) ok &= at[j][1] == tag && at[j][3] == tag;
    if (__all(ok)) break;
    asm volatile("" ::: "memory");
    load_at();
  }
#pragma unroll
  for (int j = 0; j < 4; j++) {
    const u32 a = sb + AS + (lane & 15) * 144 + (16 * w + 4 * j + g4) * 2;
    *(LAS u16*)(a) = (u16)at[j][0];
    *(LAS u16*)(a + 16 * 144) = (u16)(at[j][0] >> 16);
    *(LAS u16*)(a + 32 * 144) = (u16)at[j][2];
    *(LAS u16*)(a + 48 * 144) = (u16)(at[j][2] >> 16);
  }
  BAR();
  ST(13);
  {
    const float g = bf2f(gg[0] & 0xffffu), gm = __uint_as_float(gg[0] & 0xffff0000u);
    const u32 aa = sb + AS + (32 * wm + (lane & 31)) * 144 + (32 * wn + 4 * hh) * 2;
#pragma unroll
    for (int q = 0; q < 4; q++) {
      f32x4 s = {oc[0][0][4 * q], oc[0][0][4 * q + 1], oc[0][0][4 * q + 2], oc[0][0][4 * q + 3]};
#pragma unroll
      for (int j = 1; j < 4; j++) s += *(const LAS f32x4*)(sb + (j - 1) * SLOT + (w ^ j) * 8192 + q * 1024 + lane * 16);
      const u32x2 av = *(const LAS u32x2*)(aa + 16 * q);
      const float o0 = g * bf2f(av[0] & 0xffffu) + gm * (s[0] + bf2f(b2v[q][0] & 0xffffu));
      const float o1 = g * __uint_as_float(av[0] & 0xffff0000u) + gm * (s[1] + __uint_as_float(b2v[q][0] & 0xffff0000u));
      const float o2 = g * bf2f(av[1] & 0xffffu) + gm * (s[2] + bf2f(b2v[q][1] & 0xffffu));
      const float o3 = g * __uint_as_float(av[1] & 0xffff0000u) + gm * (s[3] + __uint_as_float(b2v[q][1] & 0xffff0000u));
      *(LAS u32x2*)(aa + 16 * q) = u32x2{pk2(o0, o1), pk2(o2, o3)};
    }
    LGKM0();
#pragma unroll
    for (int j = 0; j < 2; j++) {
      const u32 r = 32 * wm + 16 * j + (lane >> 2);
      const u32x4 o = lds128(sb + AS + r * 144 + 64 * wn + (lane & 3) * 16);
      __builtin_amdgcn_raw_buffer_store_b128(o, sOut, r * 1024 + (lane & 3) * 16 + rb * 65536 + cb2 * 128 + wn * 64, 0, 16);
    }
  }
  if (b == 0 && w == 3) {
    _Pragma("unroll 1") for (u32 spin = 0; spin < SPIN_CAP; spin++) {
      asm volatile("" ::: "memory");
      const u32x4 f4 = __builtin_amdgcn_raw_buffer_load_b128(sWS, (lane >> 2) * 128 + (lane & 3) * 16, WS_FLAG, 16);
      if (__all(f4[0] == tag && f4[1] == tag && f4[2] == tag && f4[3] == tag)) break;
    }
    if (lane == 0) __builtin_amdgcn_raw_buffer_store_b32(tag, sWS, 0, 0, 16);
  }
}

extern "C" void kernel_launch(void* const* d_in, const int* in_sizes, int n_in, void* d_out, int out_size,
                              void* d_ws, size_t ws_size, hipStream_t stream) {
  Args a;
  a.x = (const u16*)d_in[0]; a.lar = (const u16*)d_in[1]; a.aim = (const u16*)d_in[2]; a.cre = (const u16*)d_in[3];
  a.cim = (const u16*)d_in[4]; a.ldt = (const u16*)d_in[5]; a.dsk = (const u16*)d_in[6]; a.w1 = (const u16*)d_in[8];
  a.b1 = (const u16*)d_in[9]; a.w2 = (const u16*)d_in[10]; a.b2 = (const u16*)d_in[11]; a.gw = (const u16*)d_in[12];
  a.out = (u16*)d_out; a.ws = (char*)d_ws;
  fused_mix<<<256, 256, 0, stream>>>(a);
}
```

```cpp
#include <hip/hip_runtime.h>
#pragma clang diagnostic ignored "-Wint-to-pointer-cast"

typedef unsigned short u16;
typedef unsigned u32;
typedef unsigned long long u64;
typedef __bf16 h16x8 __attribute__((ext_vector_type(8)));
typedef __bf16 h16x2 __attribute__((ext_vector_type(2)));
typedef short s16x4 __attribute__((ext_vector_type(4)));
typedef short s16x8 __attribute__((ext_vector_type(8)));
typedef short s16x2 __attribute__((ext_vector_type(2)));
typedef float f32x2 __attribute__((ext_vector_type(2)));
typedef float f32x4 __attribute__((ext_vector_type(4)));
typedef float f32x16 __attribute__((ext_vector_type(16)));
typedef u32 u32x2 __attribute__((ext_vector_type(2)));
typedef u32 u32x4 __attribute__((ext_vector_type(4)));
#define LAS __attribute__((address_space(3)))
#define CAS __attribute__((address_space(4)))
#define DI __device__ __forceinline__

constexpr int L = 2048, D = 512, F = 1792;
constexpr u32 WS_FLAG = 4096;
constexpr u32 WS_G = 65536;
constexpr u32 WS_UT = 1u << 20;
constexpr u32 WS_AT = 5u << 20;
constexpr u32 WS_H = 9u << 20;
constexpr u32 WS_END = 17u << 20;
constexpr u32 WS_STAMP = 32u << 20;
constexpr u32 SLOT = 32768;
constexpr u32 TAB = 3 * SLOT;
constexpr u32 KD = TAB + 256 * 224;
constexpr u32 KD_CH = 32 * 36 * 2;
constexpr u32 KD_ZERO = 34 * 2;
constexpr u32 ZS = KD + 2 * KD_CH;
constexpr u32 RED = TAB;
constexpr u32 TC = RED + 16384;
constexpr u32 TC_STR = 4160;
constexpr u32 US = TC + 4 * TC_STR;
constexpr u32 US_STR = (256 + 2048) * 2;
constexpr u32 AS = US + 2 * US_STR;
constexpr u32 LDS_BYTES = 163840;
static_assert(AS + 64 * 144 <= KD && ZS + 2048 <= LDS_BYTES, "lds map");
constexpr u32 SPIN_CAP = 1u << 16;

DI float bf2f(u32 v) { return __uint_as_float(v << 16); }
DI u32 pk2(float lo, float hi) { f32x2 a = {lo, hi}; return __builtin_bit_cast(u32, __builtin_convertvector(a, h16x2)); }
DI float rbf(float x) { return bf2f(pk2(x, x) & 0xffffu); }
DI float dot2(u32 a, u32 b, float c) { return __builtin_amdgcn_fdot2_f32_bf16(__builtin_bit_cast(h16x2, a), __builtin_bit_cast(h16x2, b), c, false); }
DI u32 rfl(u32 x) { return __builtin_amdgcn_readfirstlane(x); }
struct Mul { f32x2 b, n; };
DI Mul mk(f32x2 b) { return Mul{b, f32x2{-b.y, b.y}}; }
DI f32x2 cmul(f32x2 a, Mul m) { return __builtin_elementwise_fma(a.yx, m.n, a * m.b.xx); }
DI float relu(float x) { return __int_as_float(max(__float_as_int(x), 0)); }
DI u32 opq(u32 x) { asm volatile("" : "+v"(x)); return x; }
#define MF32(a, b, c) __builtin_amdgcn_mfma_f32_32x32x16_bf16(__builtin_bit_cast(h16x8, a), __builtin_bit_cast(h16x8, b), c, 0, 0, 0)
#define MF16(a, b, c) __builtin_amdgcn_mfma_f32_16x16x32_bf16(__builtin_bit_cast(h16x8, a), __builtin_bit_cast(h16x8, b), c, 0, 0, 0)
#define DMA(voff, srd, soff, dst) \
  asm volatile("s_mov_b32 m0, %0\n\ts_nop 0\n\tbuffer_load_dwordx4 %1, %2, %3 offen lds" ::"s"(dst), "v"(voff), "s"(srd), "s"(soff) : "memory")
#define SETM0(dst) asm volatile("s_mov_b32 m0, %0\n\ts_nop 0" ::"s"(dst) : "memory")
#define DMAQ(voff, srd, soff, q) asm volatile("buffer_load_dwordx4 %0, %1, %2 offen offset:%3 lds" ::"v"(voff), "s"(srd), "s"(soff), "i"((q) * 1024) : "memory")
#define MMV(acc, a, b) asm volatile("v_mfma_f32_32x32x16_bf16 %0, %1, %2, %0" : "+v"(acc) : "v"(a), "v"(b) : "memory")
#define VMCNT(n) asm volatile("s_waitcnt vmcnt(%0)" ::"i"(n) : "memory")
#define LGKM0() asm volatile("s_waitcnt lgkmcnt(0)" ::: "memory")
#define BAR() asm volatile("s_waitcnt lgkmcnt(0)\n\ts_barrier" ::: "memory")
#define SB() __builtin_amdgcn_sched_barrier(0)
#define ST(i)

DI u32x4 lds128(u32 a, u32 o = 0) { return *(const LAS u32x4*)((LAS char*)a + o); }
DI s16x4 tr64(u32 a, u32 o) { return __builtin_amdgcn_ds_read_tr16_b64_v4i16((LAS s16x4*)((LAS char*)a + o)); }
DI u32x4 cat(s16x4 lo, s16x4 hi) { return __builtin_bit_cast(u32x4, __builtin_shufflevector(lo, hi, 0, 1, 2, 3, 4, 5, 6, 7)); }
DI u32x4 ldstr(u32 a, u32 o0, u32 o1) { return cat(tr64(a, o0), tr64(a, o1)); }

struct Args {
  const u16 *x, *lar, *aim, *cre, *cim, *ldt, *dsk, *w1, *b1, *w2, *b2, *gw;
  u16* out;
  char* ws;
};

__global__ void __launch_bounds__(256, 1) fused_mix(Args A) {
  __shared__ __attribute__((aligned(16))) char smem[LDS_BYTES];
  const u32 sb = (u32)(size_t)(LAS char*)smem;
  ST(0);
  const u32 tid = threadIdx.x, lane = tid & 63, w = rfl(tid >> 6);
  const u32 b = blockIdx.x, xcd = b & 7, bi = b >> 3;
  const u32 hf = xcd & 1, p = 4 * (xcd >> 1) + (bi >> 3), jj = bi & 7;
  const bool spare = jj == 7;
  const u32 cb1 = 7 * hf + jj, rb = 2 * p + hf, cb2 = jj, pair = b;
  const u32 rot = 2 * (bi >> 3) + jj, rot2 = rot >= 7 ? rot - 7 : rot;
  const u32 wm = w >> 1, wn = w & 1;
  const u32 hh = lane >> 5, blk = (lane >> 4) & 1, q4 = (lane & 15) >> 2, p4 = lane & 3, g4 = lane >> 4;

  const __amdgpu_buffer_rsrc_t sX = __builtin_amdgcn_make_buffer_rsrc((void*)A.x, 0, L * D * 2, 0x00020000);
  const __amdgpu_buffer_rsrc_t sW1 = __builtin_amdgcn_make_buffer_rsrc((void*)A.w1, 0, D * F * 2, 0x00020000);
  const __amdgpu_buffer_rsrc_t sW2 = __builtin_amdgcn_make_buffer_rsrc((void*)A.w2, 0, D * F * 2, 0x00020000);
  const __amdgpu_buffer_rsrc_t sWS = __builtin_amdgcn_make_buffer_rsrc((void*)A.ws, 0, WS_END, 0x00020000);
  const __amdgpu_buffer_rsrc_t sB2 = __builtin_amdgcn_make_buffer_rsrc((void*)A.b2, 0, D * 2, 0x00020000);
  const __amdgpu_buffer_rsrc_t sOut = __builtin_amdgcn_make_buffer_rsrc((void*)A.out, 0, L * D * 2, 0x00020000);

  u32 r_lar, r_aim, r_cre, r_cim, r_b1a, r_b1b;
  {
    const u32 o = (pair * 256 + tid) * 2;
    asm volatile("global_load_ushort %0, %1, %2" : "=v"(r_lar) : "v"(o), "s"(A.lar));
    asm volatile("global_load_ushort %0, %1, %2" : "=v"(r_aim) : "v"(o), "s"(A.aim));
    asm volatile("global_load_ushort %0, %1, %2" : "=v"(r_cre) : "v"(o), "s"(A.cre));
    asm volatile("global_load_ushort %0, %1, %2" : "=v"(r_cim) : "v"(o), "s"(A.cim));
    const u32 o1 = ((spare ? 0 : cb1) * 128 + wn * 64 + (lane & 31)) * 2;
    asm volatile("global_load_ushort %0, %1, %2" : "=v"(r_b1a) : "v"(o1), "s"(A.b1));
    asm volatile("global_load_ushort %0, %1, %2 offset:64" : "=v"(r_b1b) : "v"(o1), "s"(A.b1));
  }
  const u32 tag = *(const CAS u32*)A.ws + 1u;
  const u32 t_ldt = *(const CAS u32*)(A.ldt + 2 * pair), t_dsk = *(const CAS u32*)(A.dsk + 2 * pair);

  u32 vA[4], vB[4], v2[4];
#pragma unroll
  for (int q = 0; q < 4; q++) {
    vA[q] = (lane >> 3) * 1024 + (((lane & 7) ^ g4 ^ (4 * (q & 1))) << 4) + q * (8192 - 1024);
    vB[q] = g4 * (F * 2) + (((lane & 15) ^ (g4 << 2) ^ q) << 4) + q * (4 * F * 2 - 1024);
    v2[q] = (lane >> 3) * 1024 + (((lane & 7) ^ (blk << 2)) << 4) + q * (8192 - 1024);
  }
  const u32 xbase = (128 * p + 32 * w) * 1024, wbase = 16 * w * (F * 2) + cb1 * 256;
  auto chunk = [&](u32 c) { const u32 r = c + rot2; return 28 * w + 4 * (r >= 7 ? r - 7 : r); };
  auto w2piece = [&](u32 c, int q) {
    if ((q & 3) == 0) SETM0(sb + ((2 + c) % 3) * SLOT + w * 8192 + (q >> 2) * 4096);
    DMAQ(v2[q & 3], sW2, chunk(c) * 16384 + cb2 * 128 + (q >> 2) * 32768, q & 3);
  };

  if (!spare) {
#pragma unroll
    for (int g = 0; g < 2; g++) {
      const u32 ks = (g + rot) & 7;
      SETM0(sb + g * SLOT + w * 4096);
#pragma unroll
      for (int q = 0; q < 4; q++) DMAQ(vA[q], sX, xbase + ks * 128, q);
      SETM0(sb + g * SLOT + w * 4096 + 16384);
#pragma unroll
      for (int q = 0; q < 4; q++) DMAQ(vB[q], sW1, wbase + ks * 64 * (F * 2), q);
    }
  } else {
    const u32 vb = g4 * 1024 + (((lane & 15) ^ hh) << 4);
#pragma unroll
    for (int q = 0; q < 16; q++) DMA(vb ^ (((2 * q) & 15) << 4), sX, (64 * rb + 4 * q) * 1024 + w * 256, sb + w * 16384 + q * 1024);
    u32x4 gwv[8];
#pragma unroll
    for (int m = 0; m < 8; m++) gwv[m] = *(const u32x4*)(A.gw + 128 * w + 16 * m + 8 * hh);
    VMCNT(0);
    const u32 i = lane & 31;
    const u32 ra = sb + w * 16384 + i * 512 + ((hh ^ (i & 15)) << 4);
    const u32 vo = hh * 4 * 16384 + i * 16;
    float za = 0.f, zb = 0.f;
#pragma unroll
    for (int m = 0; m < 8; m++) {
      const u32x4 ea = lds128(ra ^ (m << 5)), eb = lds128(ra ^ (m << 5), 256);
#pragma unroll
      for (int e = 0; e < 4; e++) {
        u32x4 gr = {ea[e], tag, eb[e], tag};
        __builtin_amdgcn_raw_buffer_store_b128(gr, sWS, vo + WS_UT + (64 * w + 8 * m + e) * 16384 + rb * 512, 0, 16);
        za = dot2(ea[e], gwv[m][e], za);
        zb = dot2(eb[e], gwv[m][e], zb);
      }
    }
    *(LAS f32x2*)(sb + ZS + (2 * w + hh) * 256 + i * 8) = f32x2{za, zb};
    BAR();
    if (w == 0) {
      float z = 0.f;
#pragma unroll
      for (int k = 0; k < 8; k++) z += *(const LAS float*)(sb + ZS + k * 256 + lane * 4);
      z = rbf(z);
      const float g = rbf(1.f / (1.f + __expf(-z)));
      u32x2 gr = {pk2(g, 1.f - g), tag};
      __builtin_amdgcn_raw_buffer_store_b64(gr, sWS, lane * 8, WS_G + rb * 512, 16);
    }
#pragma unroll
    for (int q = 0; q < 16; q++) w2piece(q >> 3, q & 7);
  }
  ST(1);

  u32 ga[9];
  {
    auto cell = [&](u32 pos) {
      const u32 hi = pos >> 8, ex = min(3u, 32u - (u32)__clz(hi));
      return sb + KD + (4 * ex + ((8 * hi) >> ex)) * 72 + ((pos & 255) >> 2);
    };
    const u32 P = 2040 - 8 * tid, a0 = cell(P), a8 = cell(P + 8);
    const u32 code = P < 256 ? 0x76543210u : P < 512 ? 0x83222100u : P < 1024 ? 0x88111000u : (P & 8) ? 0x88880000u : 0x88800000u;
#pragma unroll
    for (int k = 0; k < 8; k++) {
      const u32 n = (code >> (4 * k)) & 15u;
      ga[k + 1] = opq(n == 8 ? a8 : a0 + n * 72);
    }
    ga[0] = opq(P > 256 ? a0 : P == 0 ? sb + KD + KD_ZERO : sb + KD + 7 * 72 + ((P - 8) >> 2));
  }
  if (tid < 2) *(LAS u32*)(sb + KD + tid * KD_CH + KD_ZERO) = 0;

  asm volatile("s_waitcnt vmcnt(16)" : "+v"(r_lar), "+v"(r_aim), "+v"(r_cre), "+v"(r_cim), "+v"(r_b1a), "+v"(r_b1b));
  ST(2);
  {
    const float dt = rbf(__expf(bf2f(wm ? t_ldt >> 16 : t_ldt & 0xffffu)));
    const float er = -(rbf(__expf(bf2f(r_lar))) * dt) * 1.44269504088896f;
    const float wi = (bf2f(r_aim) * dt) * 0.159154943091895f;
    auto seed = [&](float t) {
      const float m = __builtin_amdgcn_exp2f(er * t), a = __builtin_amdgcn_fractf(wi * t);
      return f32x2{m * __builtin_amdgcn_cosf(a), m * __builtin_amdgcn_sinf(a)};
    };
    LAS char* ta = (LAS char*)opq(sb + TAB + tid * 224);
    auto put8 = [&](const f32x2* v, u32 off) {
      u32x4 re = {pk2(v[0].x, v[1].x), pk2(v[2].x, v[3].x), pk2(v[4].x, v[5].x), pk2(v[6].x, v[7].x)};
      u32x4 im = {pk2(v[0].y, v[1].y), pk2(v[2].y, v[3].y), pk2(v[4].y, v[5].y), pk2(v[6].y, v[7].y)};
      *(LAS u32x4*)(ta + off) = re;
      *(LAS u32x4*)(ta + 112 + off) = im;
    };
    f32x2 v[8];
    const Mul w8 = mk(seed(8.f)), w64 = mk(seed(64.f));
    v[0] = f32x2{1.f, 0.f}; v[1] = w8.b; v[2] = cmul(v[1], w8);
    const Mul w16 = mk(v[2]);
#pragma unroll
    for (int k = 3; k < 8; k++) v[k] = cmul(v[k - 2], w16);
    put8(v, 0);
    v[0] = w64.b;
#pragma unroll
    for (int r = 1; r < 4; r++) {
#pragma unroll
      for (int k = (r == 1); k < 8; k++) v[k] = cmul(v[k], w64);
      put8(v, 16 * r);
    }
    f32x2 s1 = seed(1.f), s256 = seed(256.f);
    s1.y = -s1.y; s256.y = -s256.y;
    const Mul v1 = mk(s1), W = mk(s256), v2c = mk(cmul(s1, v1));
    f32x2 u[8];
    u[0] = f32x2{2.f * (bf2f(r_cre) * dt), -2.f * (bf2f(r_cim) * dt)};
    u[1] = cmul(u[0], v1);
#pragma unroll
    for (int k = 2; k < 8; k++) u[k] = cmul(u[k - 2], v2c);
    put8(u, 64);
    v[0] = cmul(u[0], W); v[1] = cmul(u[2], W); v[2] = cmul(u[4], W); v[3] = cmul(u[6], W);
    v[4] = cmul(v[0], W); v[5] = cmul(v[2], W); v[6] = cmul(v[4], W); v[7] = cmul(v[5], W);
    put8(v, 80);
    u[0] = cmul(v[6], W); u[1] = cmul(u[0], W); u[2] = cmul(u[1], W); u[3] = cmul(u[2], W); u[4] = cmul(u[3], W);
    u[5] = u[6] = u[7] = f32x2{0.f, 0.f};
    put8(u, 96);
  }
  ST(3);

  if (!spare) {
    f32x16 acc[2][2];
    {
      const u32x4 one = {hh ? 0u : 0x3f80u, 0u, 0u, 0u};
      const u32x4 ba = {r_b1a, 0u, 0u, 0u}, bb = {r_b1b, 0u, 0u, 0u};
      asm volatile("s_nop 1\n\tv_mfma_f32_32x32x16_bf16 %0, %4, %6, 0\n\tv_mfma_f32_32x32x16_bf16 %1, %4, %6, 0\n\t"
                   "v_mfma_f32_32x32x16_bf16 %2, %5, %6, 0\n\tv_mfma_f32_32x32x16_bf16 %3, %5, %6, 0"
                   : "=&v"(acc[0][0]), "=&v"(acc[0][1]), "=&v"(acc[1][0]), "=&v"(acc[1][1]) : "v"(ba), "v"(bb), "v"(one));
    }
    const u32 xa = sb + (64 * wm + (lane & 31)) * 128 + ((hh ^ ((lane >> 1) & 7)) << 4);
    const u32 wa = sb + 16384 + 256 * (8 * hh + q4) + 8 * (p4 & 1) + (((8 * wn + 2 * blk + (p4 >> 1)) ^ ((q4 << 2) | (2 * hh))) << 4);
    u32 ax[2][4], aw[2][4];
#pragma unroll
    for (int k = 0; k < 4; k++) {
      ax[0][k] = opq(xa ^ (k << 5)); aw[0][k] = opq(wa ^ ((k & 1) << 4) ^ ((k >> 1) << 6));
      ax[1][k] = opq(ax[0][k] + 2 * SLOT); aw[1][k] = opq(aw[0][k] + 2 * SLOT);
    }
    u32x4 fx[2][2];
    s16x4 fw[2][4];
    VMCNT(8); BAR();
    ST(4);
    fx[0][0] = lds128(ax[0][0]); fw[0][0] = tr64(aw[0][0], 0); fw[0][1] = tr64(aw[0][1], 1024);
    fx[0][1] = lds128(ax[0][0], 4096); fw[0][2] = tr64(aw[0][2], 0); fw[0][3] = tr64(aw[0][3], 1024);
#pragma unroll
    for (int s = 0; s < 8; s++) {
      const u32 ks = (s + 2 + rot) & 7, mA = sb + ((s + 2) % 3) * SLOT + w * 4096;
      auto piece = [&](int q) {
        if (s >= 6) w2piece(s - 6, q);
        else if (q < 4) { if (q == 0) SETM0(mA); DMAQ(vA[q], sX, xbase + ks * 128, q); }
        else { if (q == 4) SETM0(mA + 16384); DMAQ(vB[q - 4], sW1, wbase + ks * 64 * (F * 2), q - 4); }
      };
#pragma unroll
      for (int k = 0; k < 4; k++) {
        const int c = k & 1, n = c ^ 1, kn = (k + 1) & 3, sl = (k == 3 ? s + 1 : s) % 3, hi = sl >> 1;
        const u32 o = (sl & 1) * SLOT, ow = o + kn * 4096;
        const bool more = s < 7 || k < 3;
        MMV(acc[0][0], cat(fw[c][0], fw[c][1]), fx[c][0]);
        if (k == 3) { VMCNT(6); BAR(); }
        if (more) { fx[n][0] = lds128(ax[hi][kn], o); fw[n][0] = tr64(aw[hi][0], ow); fw[n][1] = tr64(aw[hi][1], ow + 1024); }
        MMV(acc[0][1], cat(fw[c][0], fw[c][1]), fx[c][1]);
        if (more) { fx[n][1] = lds128(ax[hi][kn], o + 4096); fw[n][2] = tr64(aw[hi][2], ow); }
        piece(2 * k);
        MMV(acc[1][0], cat(fw[c][2], fw[c][3]), fx[c][0]);
        if (more) fw[n][3] = tr64(aw[hi][3], ow + 1024);
        MMV(acc[1][1], cat(fw[c][2], fw[c][3]), fx[c][1]);
        piece(2 * k + 1);
      }
    }
    asm volatile("s_nop 7\n\ts_nop 7" : "+v"(acc[0][0]), "+v"(acc[0][1]), "+v"(acc[1][0]), "+v"(acc[1][1]));
    ST(5);
    const u32 ho = opq(lane * 16 + WS_H + ((4 * p + 2 * wm) * 112 + cb1 * 8 + wn * 4) * 1024), ho1 = opq(ho + 112 * 1024);
#pragma unroll
    for (int u = 0; u < 2; u++)
#pragma unroll
      for (int t = 0; t < 2; t++)
#pragma unroll
        for (int s = 0; s < 2; s++) {
          u32x4 o;
#pragma unroll
          for (int e = 0; e < 4; e++) {
            const float a0 = relu(acc[u][t][8 * s + 2 * e]), a1 = relu(acc[u][t][8 * s + 2 * e + 1]);
            o[e] = pk2(a0 * a0, a1 * a1);
          }
          __builtin_amdgcn_raw_buffer_store_b128(o, sWS, (t ? ho1 : ho) + (2 * u + s) * 1024, 0, 16);
          SB();
        }
  } else {
    VMCNT(0); BAR();
  }
  SB();
  ST(6);
  u32x4 ut[4];
  const u32 l16 = opq(lane * 16), uto = opq(lane * 16 + WS_UT + pair * 16384 + w * 4096);
  auto load_ut = [&](int j) {
    asm volatile("buffer_load_dwordx4 %0, %1, %2, 0 offen offset:%3 sc1" : "=v"(ut[j]) : "v"(uto), "s"(sWS), "i"(j * 1024) : "memory");
  };
  u32x4 w2r[16][2];
  u32 w2a[2][2];
  {
    const u32 a = sb + w * 8192 + 128 * (4 * hh + q4) + 8 * (p4 & 1) + (((4 * wn + 2 * blk + (p4 >> 1)) ^ ((q4 >> 1) << 2)) << 4);
    w2a[0][0] = opq(a); w2a[1][0] = opq(a ^ 64); w2a[0][1] = opq(a + 2 * SLOT); w2a[1][1] = opq((a ^ 64) + 2 * SLOT);
  }
  auto w2frag = [&](int st, int v) {
    const int sl = (2 + (st >> 2)) % 3;
    const u32 o = (sl & 1) * SLOT + (st & 3) * 2048;
    return ldstr(w2a[v][sl >> 1], o, o + 1024);
  };
  {
    f32x4 ka[2] = {{(lane == 0 && wn == 0) ? bf2f(wm ? t_dsk >> 16 : t_dsk & 0xffffu) : 0.f, 0.f, 0.f, 0.f}, {0.f, 0.f, 0.f, 0.f}};
    const u32 t0 = opq(sb + TAB + (wm * 128 + 4 * g4 + q4) * 224 + 8 * p4), t1 = opq(t0 + 32 * wn);
    u32x4 fv[8], f0[8], f1[8];
#pragma unroll
    for (int ks = 0; ks < 8; ks++) {
      const u32 o = ks * 16 * 224;
      fv[ks] = ldstr(t1, o, o + 112); f0[ks] = ldstr(t0, o + 64, o + 64 + 112); f1[ks] = ldstr(t0, o + 96, o + 96 + 112);
      if (ks < 4) load_ut(ks); else w2piece(2, ks - 4);
      if (ks >= 2) { ka[0] = MF16(f0[ks - 2], fv[ks - 2], ka[0]); ka[1] = MF16(f1[ks - 2], fv[ks - 2], ka[1]); }
      SB();
    }
#pragma unroll
    for (int ks = 6; ks < 8; ks++) { ka[0] = MF16(f0[ks], fv[ks], ka[0]); ka[1] = MF16(f1[ks], fv[ks], ka[1]); }
    VMCNT(24);
#pragma unroll
    for (int j = 0; j < 4; j++) { w2r[j][0] = w2frag(j, 0); w2r[j][1] = w2frag(j, 1); }
    LAS char* ko = (LAS char*)opq(sb + KD + wm * KD_CH + (4 * g4 * 36 + 16 * wn + (lane & 15)) * 2);
#pragma unroll
    for (int rt = 0; rt < 2; rt++)
#pragma unroll
      for (int r = 0; r < 4; r += 2) {
        const u32 pr = pk2(ka[rt][r], ka[rt][r + 1]);
        *(LAS u16*)(ko + (16 * rt + r) * 72) = (u16)pr;
        *(LAS u16*)(ko + (16 * rt + r + 1) * 72) = (u16)(pr >> 16);
      }
  }
  VMCNT(8);
  BAR();
  if (tid == 0) asm volatile("buffer_store_dword %0, %1, %2, %3 offen sc1" ::"v"(tag), "v"(0u), "s"(sWS), "s"(WS_FLAG + p * 128 + (8 * hf + jj) * 4) : "memory");
  ST(7);
  {
    u32 e[2][9];
#pragma unroll
    for (int c = 0; c < 2; c++) {
#pragma unroll
      for (int k = 0; k < 9; k++) e[c][k] = *(const LAS u16*)((LAS char*)ga[k] + c * KD_CH);
      w2piece(2, 4 + c);
    }
    LAS char* to = (LAS char*)opq(sb + TC + tid * 16);
#pragma unroll
    for (int c = 0; c < 2; c++) {
      u32x4 r1 = {e[c][8] | (e[c][7] << 16), e[c][6] | (e[c][5] << 16), e[c][4] | (e[c][3] << 16), e[c][2] | (e[c][1] << 16)};
      u32x4 r0 = {e[c][7] | (e[c][6] << 16), e[c][5] | (e[c][4] << 16), e[c][3] | (e[c][2] << 16), e[c][1] | (e[c][0] << 16)};
      *(LAS u32x4*)(to + (2 * c) * TC_STR) = r0;
      *(LAS u32x4*)(to + (2 * c + 1) * TC_STR) = r1;
      w2piece(2, 6 + c);
    }
  }
  if (tid < 8) *(LAS u32x4*)(sb + TC + (tid >> 1) * TC_STR + 4096 + (tid & 1) * 16) = u32x4{0, 0, 0, 0};
  *(LAS u32*)(sb + US + (tid >> 7) * US_STR + (tid & 127) * 4) = 0;
  asm volatile("s_waitcnt vmcnt(8)" : "+v"(ut[0]), "+v"(ut[1]), "+v"(ut[2]), "+v"(ut[3])::"memory");
  _Pragma("unroll 1") for (u32 spin = 0; spin < SPIN_CAP; spin++) {
    bool ok = true;
#pragma unroll
    for (int j = 0; j < 4; j++) ok &= ut[j][1] == tag && ut[j][3] == tag;
    if (__all(ok)) break;
#pragma unroll
    for (int j = 0; j < 4; j++) load_ut(j);
    asm volatile("s_waitcnt vmcnt(0)" : "+v"(ut[0]), "+v"(ut[1]), "+v"(ut[2]), "+v"(ut[3])::"memory");
  }
  {
    LAS char* uo = (LAS char*)opq(sb + US + 512 + (w * 256 + lane) * 4);
#pragma unroll
    for (int j = 0; j < 4; j++) {
      *(LAS u32*)(uo + j * 256) = __builtin_amdgcn_perm(ut[j][2], ut[j][0], 0x05040100);
      *(LAS u32*)(uo + j * 256 + US_STR) = __builtin_amdgcn_perm(ut[j][2], ut[j][0], 0x07060302);
      w2piece(3, j);
    }
  }
  BAR();
  ST(8);

  u32x4 hfr[28][2];
  auto load_h = [&](int i) {
    const int st = i >> 1, t = i & 1;
    hfr[st][t] = __builtin_amdgcn_raw_buffer_load_b128(sWS, l16 + (st & 3) * 1024, WS_H + ((2 * rb + (t ^ wm)) * 112 + chunk(st >> 2)) * 1024, 16);
  };
  f32x4 ca[8];
#pragma unroll
  for (int c = 0; c < 8; c++) ca[c] = f32x4{0.f, 0.f, 0.f, 0.f};
  const u32 ua = opq(sb + US + wm * US_STR + (256 - 16 - 128 * wn - 96 + 16 * (lane & 15) + 8 * g4) * 2);
  u32 tb[4];
#pragma unroll
  for (int j = 0; j < 4; j++) tb[j] = opq(sb + TC + (2 * wm + (lane & 1)) * TC_STR + (2046 - 16 - 128 * wn - 96 - 1792 + 8 * g4 - (lane & 14)) * 2 + 1024 * j);
  u32x4 fu[2][8];
  u32x2 ft[2][16];
  auto cread = [&](int f, int r) {
    const int seq[24] = {8, 9, 0, 1, 2, 3, 4, 5, 6, 7, 10, 11, 12, 13, 14, 15, 16, 17, 18, 19, 20, 21, 22, 23};
    const int k = seq[r];
    if (k < 8) fu[f & 1][k] = lds128(ua, (96 - 32 * f + 256 * k) * 2);
    else {
      const int i = (k - 8) >> 1, hl = k & 1, e = 7 - i;
      const LAS u32* t = (const LAS u32*)((LAS char*)tb[e >> 1] + (96 - 32 * f + 256 * (e & 1)) * 2 + 8 * hl);
      ft[f & 1][2 * i + hl] = u32x2{t[0], t[1]};
    }
  };
  u32 fl;
  const u32 fo = (lane & 15) * 4;
#pragma unroll
  for (int r = 0; r < 24; r++) cread(0, r);
  SB();
#pragma unroll
  for (int f = 0; f < 4; f++) {
    int m = 0;
#pragma unroll
    for (int i = 0; i < 8; i++)
#pragma unroll
      for (int e = 0; e + i < 8; e++, m++) {
        const u32x4 tf = {ft[f & 1][2 * i][0], ft[f & 1][2 * i][1], ft[f & 1][2 * i + 1][0], ft[f & 1][2 * i + 1][1]};
        ca[i + e] = MF16(fu[f & 1][e], tf, ca[i + e]);
        if (f < 3 && m >= 8 && m < 32) cread(f + 1, m - 8);
        if (f < 3) {
          if (m == 0 && f > 0) VMCNT(16);
          if (m < 8) w2r[4 * (f + 1) + (m >> 1)][m & 1] = w2frag(4 * (f + 1) + (m >> 1), m & 1);
          if (f == 2 && m == 1) asm volatile("buffer_load_dword %0, %1, %2, %3 offen sc1" : "=v"(fl) : "v"(fo), "s"(sWS), "s"(WS_FLAG + p * 128) : "memory");
          if (f == 0 && m >= 2 && m < 14 && m % 3 == 2) w2piece(3, 4 + (m - 2) / 3);
          if (m == 12) asm volatile("s_waitcnt lgkmcnt(4)" ::: "memory");
          if (m >= 14 && m % 3 == 2) w2piece(f + 4, (m - 14) / 3);
        } else if (m % 4 == 0) load_h(m / 4);
        SB();
      }
    if (f == 2) {
      asm volatile("s_waitcnt vmcnt(8)" : "+v"(fl)::"memory");
      ST(9);
      _Pragma("unroll 1") for (u32 spin = 0; spin < SPIN_CAP; spin++) {
        if (__all(fl == tag)) break;
        asm volatile("buffer_load_dword %0, %1, %2, %3 offen sc1\n\ts_waitcnt vmcnt(0)" : "=&v"(fl) : "v"(fo), "s"(sWS), "s"(WS_FLAG + p * 128) : "memory");
      }
      ST(10);
      SB();
    }
  }
  {
    f32x4 keep[4];
    const u32 ro = opq(sb + RED + w * 4096 + lane * 16);
    if (wn) {
#pragma unroll
      for (int c = 0; c < 4; c++) { *(LAS f32x4*)((LAS char*)ro + c * 1024) = ca[c]; keep[c] = ca[c + 4]; }
      asm volatile("" ::: "memory");
    } else {
#pragma unroll
      for (int c = 0; c < 4; c++) { *(LAS f32x4*)((LAS char*)ro + c * 1024) = ca[c + 4]; keep[c] = ca[c]; }
    }
    load_h(9); load_h(10);
    BAR();
    f32x4 oth[4];
#pragma unroll
    for (int c = 0; c < 4; c++) oth[c] = *(const LAS f32x4*)((LAS char*)(ro ^ 4096) + c * 1024);
    load_h(11);
    const u32 ao = opq(l16 + WS_AT + (2 * pair + wm) * 8192 + wn * 4096);
#pragma unroll
    for (int c = 0; c < 4; c++) {
      const f32x4 y = keep[c] + oth[c];
      u32x4 gr = {pk2(y[0], y[1]), tag, pk2(y[2], y[3]), tag};
      __builtin_amdgcn_raw_buffer_store_b128(gr, sWS, ao + c * 1024, 0, 16);
    }
  }
  SB();
  ST(11);

  f32x16 oc[2][2] = {};
  u32x4 at[4];
  u32x2 gg, b2v[4];
  const u32 ato = g4 * 8192 + (lane & 15) * 16;
  auto load_at = [&](int j) {
    if (j < 4) at[j] = __builtin_amdgcn_raw_buffer_load_b128(sWS, ato, WS_AT + (64 * cb2 + 16 * w + 4 * j) * 8192 + rb * 256, 16);
    else gg = __builtin_amdgcn_raw_buffer_load_b64(sWS, (lane & 31) * 8, WS_G + (64 * rb + 32 * wm) * 8, 16);
  };
  constexpr int HEAD = 12;
  u32x4 wl[2][2];
#pragma unroll
  for (int st = 0; st < 28; st++) {
    const u32x4 a0 = st < 16 ? w2r[st < 16 ? st : 0][0] : wl[st & 1][0];
    const u32x4 a1 = st < 16 ? w2r[st < 16 ? st : 0][1] : wl[st & 1][1];
    auto extra = [&](int i) {
      if (i < 56) load_h(i);
      else if (i < 60) b2v[i - 56] = __builtin_amdgcn_raw_buffer_load_b64(sB2, hh * 8 + (i - 56) * 16, cb2 * 128 + wn * 64, 0);
      else if (i < 65) load_at(i - 60);
    };
    oc[0][0] = MF32(a0, hfr[st][0], oc[0][0]);
    if (st >= 15 && st < 27) wl[(st + 1) & 1][0] = w2frag(st + 1, 0);
    extra(HEAD + 2 * st);
    SB();
    oc[1][0] = MF32(a1, hfr[st][0], oc[1][0]);
    if (st >= 15 && st < 27) wl[(st + 1) & 1][1] = w2frag(st + 1, 1);
    SB();
    oc[0][1] = MF32(a0, hfr[st][1], oc[0][1]);
    extra(HEAD + 2 * st + 1);
    SB();
    oc[1][1] = MF32(a1, hfr[st][1], oc[1][1]);
    SB();
  }
  ST(12);
  LGKM0();
#pragma unroll
  for (int j = 1; j < 4; j++)
#pragma unroll
    for (int q = 0; q < 4; q++) {
      const f32x16& s = oc[j & 1][j >> 1];
      *(LAS f32x4*)(sb + (j - 1) * SLOT + w * 8192 + q * 1024 + lane * 16) = f32x4{s[4 * q], s[4 * q + 1], s[4 * q + 2], s[4 * q + 3]};
    }
  _Pragma("unroll 1") for (u32 spin = 0; spin < SPIN_CAP; spin++) {
    bool ok = gg[1] == tag;
#pragma unroll
    for (int j = 0; j < 4; j++) ok &= at[j][1] == tag && at[j][3] == tag;
    if (__all(ok)) break;
    asm volatile("" ::: "memory");
#pragma unroll
    for (int j = 0; j < 5; j++) load_at(j);
  }
#pragma unroll
  for (int j = 0; j < 4; j++) {
    const u32 a = sb + AS + (lane & 15) * 144 + (16 * w + 4 * j + g4) * 2;
    *(LAS u16*)(a) = (u16)at[j][0];
    *(LAS u16*)(a + 16 * 144) = (u16)(at[j][0] >> 16);
    *(LAS u16*)(a + 32 * 144) = (u16)at[j][2];
    *(LAS u16*)(a + 48 * 144) = (u16)(at[j][2] >> 16);
  }
  BAR();
  ST(13);
  {
    const float g = bf2f(gg[0] & 0xffffu), gm = __uint_as_float(gg[0] & 0xffff0000u);
    const u32 aa = sb + AS + (32 * wm + (lane & 31)) * 144 + (32 * wn + 4 * hh) * 2;
#pragma unroll
    for (int q = 0; q < 4; q++) {
      f32x4 s = {oc[0][0][4 * q], oc[0][0][4 * q + 1], oc[0][0][4 * q + 2], oc[0][0][4 * q + 3]};
#pragma unroll
      for (int j = 1; j < 4; j++) s += *(const LAS f32x4*)(sb + (j - 1) * SLOT + (w ^ j) * 8192 + q * 1024 + lane * 16);
      const u32x2 av = *(const LAS u32x2*)(aa + 16 * q);
      const float o0 = g * bf2f(av[0] & 0xffffu) + gm * (s[0] + bf2f(b2v[q][0] & 0xffffu));
      const float o1 = g * __uint_as_float(av[0] & 0xffff0000u) + gm * (s[1] + __uint_as_float(b2v[q][0] & 0xffff0000u));
      const float o2 = g * bf2f(av[1] & 0xffffu) + gm * (s[2] + bf2f(b2v[q][1] & 0xffffu));
      const float o3 = g * __uint_as_float(av[1] & 0xffff0000u) + gm * (s[3] + __uint_as_float(b2v[q][1] & 0xffff0000u));
      *(LAS u32x2*)(aa + 16 * q) = u32x2{pk2(o0, o1), pk2(o2, o3)};
    }
    LGKM0();
#pragma unroll
    for (int j = 0; j < 2; j++) {
      const u32 r = 32 * wm + 16 * j + (lane >> 2);
      const u32x4 o = lds128(sb + AS + r * 144 + 64 * wn + (lane & 3) * 16);
      __builtin_amdgcn_raw_buffer_store_b128(o, sOut, r * 1024 + (lane & 3) * 16 + rb * 65536 + cb2 * 128 + wn * 64, 0, 0);
    }
  }
  if (b == 0 && w == 3) {
    _Pragma("unroll 1") for (u32 spin = 0; spin < SPIN_CAP; spin++) {
      asm volatile("" ::: "memory");
      const u32x4 f4 = __builtin_amdgcn_raw_buffer_load_b128(sWS, (lane >> 2) * 128 + (lane & 3) * 16, WS_FLAG, 16);
      if (__all(f4[0] == tag && f4[1] == tag && f4[2] == tag && f4[3] == tag)) break;
    }
    if (lane == 0) __builtin_amdgcn_raw_buffer_store_b32(tag, sWS, 0, 0, 16);
  }
}

extern "C" void kernel_launch(void* const* d_in, const int* in_sizes, int n_in, void* d_out, int out_size,
                              void* d_ws, size_t ws_size, hipStream_t stream) {
  Args a;
  a.x = (const u16*)d_in[0]; a.lar = (const u16*)d_in[1]; a.aim = (const u16*)d_in[2]; a.cre = (const u16*)d_in[3];
  a.cim = (const u16*)d_in[4]; a.ldt = (const u16*)d_in[5]; a.dsk = (const u16*)d_in[6]; a.w1 = (const u16*)d_in[8];
  a.b1 = (const u16*)d_in[9]; a.w2 = (const u16*)d_in[10]; a.b2 = (const u16*)d_in[11]; a.gw = (const u16*)d_in[12];
  a.out = (u16*)d_out; a.ws = (char*)d_ws;
  fused_mix<<<256, 256, 0, stream>>>(a);
}
```

```cpp
#include <hip/hip_runtime.h>
#pragma clang diagnostic ignored "-Wint-to-pointer-cast"

typedef unsigned short u16;
typedef unsigned u32;
typedef unsigned long long u64;
typedef __bf16 h16x8 __attribute__((ext_vector_type(8)));
typedef __bf16 h16x2 __attribute__((ext_vector_type(2)));
typedef short s16x4 __attribute__((ext_vector_type(4)));
typedef short s16x8 __attribute__((ext_vector_type(8)));
typedef short s16x2 __attribute__((ext_vector_type(2)));
typedef float f32x2 __attribute__((ext_vector_type(2)));
typedef float f32x4 __attribute__((ext_vector_type(4)));
typedef float f32x16 __attribute__((ext_vector_type(16)));
typedef u32 u32x2 __attribute__((ext_vector_type(2)));
typedef u32 u32x4 __attribute__((ext_vector_type(4)));
#define LAS __attribute__((address_space(3)))
#define CAS __attribute__((address_space(4)))
#define DI __device__ __forceinline__

constexpr int L = 2048, D = 512, F = 1792;
constexpr u32 WS_FLAG = 4096;
constexpr u32 WS_G = 65536;
constexpr u32 WS_UT = 1u << 20;
constexpr u32 WS_AT = 5u << 20;
constexpr u32 WS_H = 9u << 20;
constexpr u32 WS_END = 17u << 20;
constexpr u32 WS_STAMP = 32u << 20;
constexpr u32 SLOT = 32768;
constexpr u32 TAB = 3 * SLOT;
constexpr u32 KD = TAB + 256 * 224;
constexpr u32 KD_CH = 32 * 36 * 2;
constexpr u32 KD_ZERO = 34 * 2;
constexpr u32 ZS = KD + 2 * KD_CH;
constexpr u32 RED = TAB;
constexpr u32 TC = RED + 16384;
constexpr u32 TC_STR = 4160;
constexpr u32 US = TC + 4 * TC_STR;
constexpr u32 US_STR = (256 + 2048) * 2;
constexpr u32 AS = US + 2 * US_STR;
constexpr u32 LDS_BYTES = 163840;
static_assert(AS + 64 * 144 <= KD && ZS + 2048 <= LDS_BYTES, "lds map");
constexpr u32 SPIN_CAP = 1u << 16;

DI float bf2f(u32 v) { return __uint_as_float(v << 16); }
DI u32 pk2(float lo, float hi) { f32x2 a = {lo, hi}; return __builtin_bit_cast(u32, __builtin_convertvector(a, h16x2)); }
DI float rbf(float x) { return bf2f(pk2(x, x) & 0xffffu); }
DI float dot2(u32 a, u32 b, float c) { return __builtin_amdgcn_fdot2_f32_bf16(__builtin_bit_cast(h16x2, a), __builtin_bit_cast(h16x2, b), c, false); }
DI u32 rfl(u32 x) { return __builtin_amdgcn_readfirstlane(x); }
struct Mul { f32x2 b, n; };
DI Mul mk(f32x2 b) { return Mul{b, f32x2{-b.y, b.y}}; }
DI f32x2 cmul(f32x2 a, Mul m) { return __builtin_elementwise_fma(a.yx, m.n, a * m.b.xx); }
DI float relu(float x) { return __int_as_float(max(__float_as_int(x), 0)); }
DI u32 opq(u32 x) { asm volatile("" : "+v"(x)); return x; }
#define MF32(a, b, c) __builtin_amdgcn_mfma_f32_32x32x16_bf16(__builtin_bit_cast(h16x8, a), __builtin_bit_cast(h16x8, b), c, 0, 0, 0)
#define MF16(a, b, c) __builtin_amdgcn_mfma_f32_16x16x32_bf16(__builtin_bit_cast(h16x8, a), __builtin_bit_cast(h16x8, b), c, 0, 0, 0)
#define DMA(voff, srd, soff, dst) \
  asm volatile("s_mov_b32 m0, %0\n\ts_nop 0\n\tbuffer_load_dwordx4 %1, %2, %3 offen lds" ::"s"(dst), "v"(voff), "s"(srd), "s"(soff) : "memory")
#define SETM0(dst) asm volatile("s_mov_b32 m0, %0\n\ts_nop 0" ::"s"(dst) : "memory")
#define DMAQ(voff, srd, soff, q) asm volatile("buffer_load_dwordx4 %0, %1, %2 offen offset:%3 lds" ::"v"(voff), "s"(srd), "s"(soff), "i"((q) * 1024) : "memory")
#define MMV(acc, a, b) asm volatile("v_mfma_f32_32x32x16_bf16 %0, %1, %2, %0" : "+v"(acc) : "v"(a), "v"(b) : "memory")
#define VMCNT(n) asm volatile("s_waitcnt vmcnt(%0)" ::"i"(n) : "memory")
#define LGKM0() asm volatile("s_waitcnt lgkmcnt(0)" ::: "memory")
#define BAR() asm volatile("s_waitcnt lgkmcnt(0)\n\ts_barrier" ::: "memory")
#define SB() __builtin_amdgcn_sched_barrier(0)
#define ST(i)

DI u32x4 lds128(u32 a, u32 o = 0) { return *(const LAS u32x4*)((LAS char*)a + o); }
DI s16x4 tr64(u32 a, u32 o) { return __builtin_amdgcn_ds_read_tr16_b64_v4i16((LAS s16x4*)((LAS char*)a + o)); }
DI u32x4 cat(s16x4 lo, s16x4 hi) { return __builtin_bit_cast(u32x4, __builtin_shufflevector(lo, hi, 0, 1, 2, 3, 4, 5, 6, 7)); }
DI u32x4 ldstr(u32 a, u32 o0, u32 o1) { return cat(tr64(a, o0), tr64(a, o1)); }

struct Args {
  const u16 *x, *lar, *aim, *cre, *cim, *ldt, *dsk, *w1, *b1, *w2, *b2, *gw;
  u16* out;
  const char* bx;
  char* ws;
};

__global__ void __launch_bounds__(256, 1) fused_mix(Args A) {
  __shared__ __attribute__((aligned(16))) char smem[LDS_BYTES];
  const u32 sb = (u32)(size_t)(LAS char*)smem;
  ST(0);
  const u32 tid = threadIdx.x, lane = tid & 63, w = rfl(tid >> 6);
  const u32 b = blockIdx.x, xcd = b & 7, bi = b >> 3;
  const u32 hf = xcd & 1, p = 4 * (xcd >> 1) + (bi >> 3), jj = bi & 7;
  const bool spare = jj == 7;
  const u32 cb1 = 7 * hf + jj, rb = 2 * p + hf, cb2 = jj, pair = b;
  const u32 rot = 2 * (bi >> 3) + jj, rot2 = rot >= 7 ? rot - 7 : rot;
  const u32 wm = w >> 1, wn = w & 1;
  const u32 hh = lane >> 5, blk = (lane >> 4) & 1, q4 = (lane & 15) >> 2, p4 = lane & 3, g4 = lane >> 4;

  const __amdgpu_buffer_rsrc_t sX = __builtin_amdgcn_make_buffer_rsrc((void*)A.x, 0, L * D * 2, 0x00020000);
  const __amdgpu_buffer_rsrc_t sW1 = __builtin_amdgcn_make_buffer_rsrc((void*)A.w1, 0, D * F * 2, 0x00020000);
  const __amdgpu_buffer_rsrc_t sW2 = __builtin_amdgcn_make_buffer_rsrc((void*)A.w2, 0, D * F * 2, 0x00020000);
  const __amdgpu_buffer_rsrc_t sWS = __builtin_amdgcn_make_buffer_rsrc((void*)A.ws, 0, WS_END, 0x00020000);
  const __amdgpu_buffer_rsrc_t sB2 = __builtin_amdgcn_make_buffer_rsrc((void*)A.b2, 0, D * 2, 0x00020000);
  const __amdgpu_buffer_rsrc_t sOut = __builtin_amdgcn_make_buffer_rsrc((void*)A.out, 0, L * D * 2, 0x00020000);

  u32 r_lar, r_aim, r_cre, r_cim, r_b1a, r_b1b;
  {
    const u32 o = (pair * 256 + tid) * 2;
    asm volatile("global_load_ushort %0, %1, %2" : "=v"(r_lar) : "v"(o), "s"(A.lar));
    asm volatile("global_load_ushort %0, %1, %2" : "=v"(r_aim) : "v"(o), "s"(A.aim));
    asm volatile("global_load_ushort %0, %1, %2" : "=v"(r_cre) : "v"(o), "s"(A.cre));
    asm volatile("global_load_ushort %0, %1, %2" : "=v"(r_cim) : "v"(o), "s"(A.cim));
    const u32 o1 = ((spare ? 0 : cb1) * 128 + wn * 64 + (lane & 31)) * 2;
    asm volatile("global_load_ushort %0, %1, %2" : "=v"(r_b1a) : "v"(o1), "s"(A.b1));
    asm volatile("global_load_ushort %0, %1, %2 offset:64" : "=v"(r_b1b) : "v"(o1), "s"(A.b1));
  }
  const u32 tag = *(const CAS u32*)A.ws + 1u;
  const u32 t_ldt = *(const CAS u32*)(A.ldt + 2 * pair), t_dsk = *(const CAS u32*)(A.dsk + 2 * pair);

  u32 vA[4], vB[4], v2[4];
#pragma unroll
  for (int q = 0; q < 4; q++) {
    vA[q] = (lane >> 3) * 1024 + (((lane & 7) ^ g4 ^ (4 * (q & 1))) << 4) + q * (8192 - 1024);
    vB[q] = g4 * (F * 2) + (((lane & 15) ^ (g4 << 2) ^ q) << 4) + q * (4 * F * 2 - 1024);
    v2[q] = (lane >> 3) * 1024 + (((lane & 7) ^ (blk << 2)) << 4) + q * (8192 - 1024);
  }
  const u32 xbase = (128 * p + 32 * w) * 1024, wbase = 16 * w * (F * 2) + cb1 * 256;
  auto chunk = [&](u32 c) { const u32 r = c + rot2; return 28 * w + 4 * (r >= 7 ? r - 7 : r); };
  auto w2piece = [&](u32 c, int q) {
    if ((q & 3) == 0) SETM0(sb + ((2 + c) % 3) * SLOT + w * 8192 + (q >> 2) * 4096);
    DMAQ(v2[q & 3], sW2, chunk(c) * 16384 + cb2 * 128 + (q >> 2) * 32768, q & 3);
  };

  if (!spare) {
#pragma unroll
    for (int g = 0; g < 2; g++) {
      const u32 ks = (g + rot) & 7;
      SETM0(sb + g * SLOT + w * 4096);
#pragma unroll
      for (int q = 0; q < 4; q++) DMAQ(vA[q], sX, xbase + ks * 128, q);
      SETM0(sb + g * SLOT + w * 4096 + 16384);
#pragma unroll
      for (int q = 0; q < 4; q++) DMAQ(vB[q], sW1, wbase + ks * 64 * (F * 2), q);
    }
  } else {
    const u32 vb = g4 * 1024 + (((lane & 15) ^ hh) << 4);
#pragma unroll
    for (int q = 0; q < 16; q++) DMA(vb ^ (((2 * q) & 15) << 4), sX, (64 * rb + 4 * q) * 1024 + w * 256, sb + w * 16384 + q * 1024);
    u32x4 gwv[8];
#pragma unroll
    for (int m = 0; m < 8; m++) gwv[m] = *(const u32x4*)(A.gw + 128 * w + 16 * m + 8 * hh);
    VMCNT(0);
    const u32 i = lane & 31;
    const u32 ra = sb + w * 16384 + i * 512 + ((hh ^ (i & 15)) << 4);
    const u32 vo = hh * 4 * 16384 + i * 16;
    float za = 0.f, zb = 0.f;
#pragma unroll
    for (int m = 0; m < 8; m++) {
      const u32x4 ea = lds128(ra ^ (m << 5)), eb = lds128(ra ^ (m << 5), 256);
#pragma unroll
      for (int e = 0; e < 4; e++) {
        u32x4 gr = {ea[e], tag, eb[e], tag};
        __builtin_amdgcn_raw_buffer_store_b128(gr, sWS, vo + WS_UT + (64 * w + 8 * m + e) * 16384 + rb * 512, 0, 16);
        za = dot2(ea[e], gwv[m][e], za);
        zb = dot2(eb[e], gwv[m][e], zb);
      }
    }
    *(LAS f32x2*)(sb + ZS + (2 * w + hh) * 256 + i * 8) = f32x2{za, zb};
    BAR();
    if (w == 0) {
      float z = 0.f;
#pragma unroll
      for (int k = 0; k < 8; k++) z += *(const LAS float*)(sb + ZS + k * 256 + lane * 4);
      z = rbf(z);
      const float g = rbf(1.f / (1.f + __expf(-z)));
      u32x2 gr = {pk2(g, 1.f - g), tag};
      __builtin_amdgcn_raw_buffer_store_b64(gr, sWS, lane * 8, WS_G + rb * 512, 16);
    }
#pragma unroll
    for (int q = 0; q < 16; q++) w2piece(q >> 3, q & 7);
  }
  ST(1);

  u32 ga[9];
  {
    auto cell = [&](u32 pos) {
      const u32 hi = pos >> 8, ex = min(3u, 32u - (u32)__clz(hi));
      return sb + KD + (4 * ex + ((8 * hi) >> ex)) * 72 + ((pos & 255) >> 2);
    };
    const u32 P = 2040 - 8 * tid, a0 = cell(P), a8 = cell(P + 8);
    const u32 code = P < 256 ? 0x76543210u : P < 512 ? 0x83222100u : P < 1024 ? 0x88111000u : (P & 8) ? 0x88880000u : 0x88800000u;
#pragma unroll
    for (int k = 0; k < 8; k++) {
      const u32 n = (code >> (4 * k)) & 15u;
      ga[k + 1] = opq(n == 8 ? a8 : a0 + n * 72);
    }
    ga[0] = opq(P > 256 ? a0 : P == 0 ? sb + KD + KD_ZERO : sb + KD + 7 * 72 + ((P - 8) >> 2));
  }
  if (tid < 2) *(LAS u32*)(sb + KD + tid * KD_CH + KD_ZERO) = 0;

  asm volatile("s_waitcnt vmcnt(16)" : "+v"(r_lar), "+v"(r_aim), "+v"(r_cre), "+v"(r_cim), "+v"(r_b1a), "+v"(r_b1b));
  ST(2);
  {
    const float dt = rbf(__expf(bf2f(wm ? t_ldt >> 16 : t_ldt & 0xffffu)));
    const float er = -(rbf(__expf(bf2f(r_lar))) * dt) * 1.44269504088896f;
    const float wi = (bf2f(r_aim) * dt) * 0.159154943091895f;
    auto seed = [&](float t) {
      const float m = __builtin_amdgcn_exp2f(er * t), a = __builtin_amdgcn_fractf(wi * t);
      return f32x2{m * __builtin_amdgcn_cosf(a), m * __builtin_amdgcn_sinf(a)};
    };
    LAS char* ta = (LAS char*)opq(sb + TAB + tid * 224);
    auto put8 = [&](const f32x2* v, u32 off) {
      u32x4 re = {pk2(v[0].x, v[1].x), pk2(v[2].x, v[3].x), pk2(v[4].x, v[5].x), pk2(v[6].x, v[7].x)};
      u32x4 im = {pk2(v[0].y, v[1].y), pk2(v[2].y, v[3].y), pk2(v[4].y, v[5].y), pk2(v[6].y, v[7].y)};
      *(LAS u32x4*)(ta + off) = re;
      *(LAS u32x4*)(ta + 112 + off) = im;
    };
    f32x2 v[8];
    const Mul w8 = mk(seed(8.f)), w64 = mk(seed(64.f));
    v[0] = f32x2{1.f, 0.f}; v[1] = w8.b; v[2] = cmul(v[1], w8);
    const Mul w16 = mk(v[2]);
#pragma unroll
    for (int k = 3; k < 8; k++) v[k] = cmul(v[k - 2], w16);
    put8(v, 0);
    v[0] = w64.b;
#pragma unroll
    for (int r = 1; r < 4; r++) {
#pragma unroll
      for (int k = (r == 1); k < 8; k++) v[k] = cmul(v[k], w64);
      put8(v, 16 * r);
    }
    f32x2 s1 = seed(1.f), s256 = seed(256.f);
    s1.y = -s1.y; s256.y = -s256.y;
    const Mul v1 = mk(s1), W = mk(s256), v2c = mk(cmul(s1, v1));
    f32x2 u[8];
    u[0] = f32x2{2.f * (bf2f(r_cre) * dt), -2.f * (bf2f(r_cim) * dt)};
    u[1] = cmul(u[0], v1);
#pragma unroll
    for (int k = 2; k < 8; k++) u[k] = cmul(u[k - 2], v2c);
    put8(u, 64);
    v[0] = cmul(u[0], W); v[1] = cmul(u[2], W); v[2] = cmul(u[4], W); v[3] = cmul(u[6], W);
    v[4] = cmul(v[0], W); v[5] = cmul(v[2], W); v[6] = cmul(v[4], W); v[7] = cmul(v[5], W);
    put8(v, 80);
    u[0] = cmul(v[6], W); u[1] = cmul(u[0], W); u[2] = cmul(u[1], W); u[3] = cmul(u[2], W); u[4] = cmul(u[3], W);
    u[5] = u[6] = u[7] = f32x2{0.f, 0.f};
    put8(u, 96);
  }
  ST(3);

  if (!spare) {
    f32x16 acc[2][2];
    {
      const u32x4 one = {hh ? 0u : 0x3f80u, 0u, 0u, 0u};
      const u32x4 ba = {r_b1a, 0u, 0u, 0u}, bb = {r_b1b, 0u, 0u, 0u};
      asm volatile("s_nop 1\n\tv_mfma_f32_32x32x16_bf16 %0, %4, %6, 0\n\tv_mfma_f32_32x32x16_bf16 %1, %4, %6, 0\n\t"
                   "v_mfma_f32_32x32x16_bf16 %2, %5, %6, 0\n\tv_mfma_f32_32x32x16_bf16 %3, %5, %6, 0"
                   : "=&v"(acc[0][0]), "=&v"(acc[0][1]), "=&v"(acc[1][0]), "=&v"(acc[1][1]) : "v"(ba), "v"(bb), "v"(one));
    }
    const u32 xa = sb + (64 * wm + (lane & 31)) * 128 + ((hh ^ ((lane >> 1) & 7)) << 4);
    const u32 wa = sb + 16384 + 256 * (8 * hh + q4) + 8 * (p4 & 1) + (((8 * wn + 2 * blk + (p4 >> 1)) ^ ((q4 << 2) | (2 * hh))) << 4);
    u32 ax[2][4], aw[2][4];
#pragma unroll
    for (int k = 0; k < 4; k++) {
      ax[0][k] = opq(xa ^ (k << 5)); aw[0][k] = opq(wa ^ ((k & 1) << 4) ^ ((k >> 1) << 6));
      ax[1][k] = opq(ax[0][k] + 2 * SLOT); aw[1][k] = opq(aw[0][k] + 2 * SLOT);
    }
    u32x4 fx[2][2];
    s16x4 fw[2][4];
    VMCNT(8); BAR();
    ST(4);
    fx[0][0] = lds128(ax[0][0]); fw[0][0] = tr64(aw[0][0], 0); fw[0][1] = tr64(aw[0][1], 1024);
    fx[0][1] = lds128(ax[0][0], 4096); fw[0][2] = tr64(aw[0][2], 0); fw[0][3] = tr64(aw[0][3], 1024);
#pragma unroll
    for (int s = 0; s < 8; s++) {
      const u32 ks = (s + 2 + rot) & 7, mA = sb + ((s + 2) % 3) * SLOT + w * 4096;
      auto piece = [&](int q) {
        if (s >= 6) w2piece(s - 6, q);
        else if (q < 4) { if (q == 0) SETM0(mA); DMAQ(vA[q], sX, xbase + ks * 128, q); }
        else { if (q == 4) SETM0(mA + 16384); DMAQ(vB[q - 4], sW1, wbase + ks * 64 * (F * 2), q - 4); }
      };
#pragma unroll
      for (int k = 0; k < 4; k++) {
        const int c = k & 1, n = c ^ 1, kn = (k + 1) & 3, sl = (k == 3 ? s + 1 : s) % 3, hi = sl >> 1;
        const u32 o = (sl & 1) * SLOT, ow = o + kn * 4096;
        const bool more = s < 7 || k < 3;
        MMV(acc[0][0], cat(fw[c][0], fw[c][1]), fx[c][0]);
        if (k == 3) { VMCNT(6); BAR(); }
        if (more) { fx[n][0] = lds128(ax[hi][kn], o); fw[n][0] = tr64(aw[hi][0], ow); fw[n][1] = tr64(aw[hi][1], ow + 1024); }
        MMV(acc[0][1], cat(fw[c][0], fw[c][1]), fx[c][1]);
        if (more) { fx[n][1] = lds128(ax[hi][kn], o + 4096); fw[n][2] = tr64(aw[hi][2], ow); }
        piece(2 * k);
        MMV(acc[1][0], cat(fw[c][2], fw[c][3]), fx[c][0]);
        if (more) fw[n][3] = tr64(aw[hi][3], ow + 1024);
        MMV(acc[1][1], cat(fw[c][2], fw[c][3]), fx[c][1]);
        piece(2 * k + 1);
      }
    }
    asm volatile("s_nop 7\n\ts_nop 7" : "+v"(acc[0][0]), "+v"(acc[0][1]), "+v"(acc[1][0]), "+v"(acc[1][1]));
    ST(5);
    const u32 ho = opq(lane * 16 + WS_H + ((4 * p + 2 * wm) * 112 + cb1 * 8 + wn * 4) * 1024), ho1 = opq(ho + 112 * 1024);
#pragma unroll
    for (int u = 0; u < 2; u++)
#pragma unroll
      for (int t = 0; t < 2; t++)
#pragma unroll
        for (int s = 0; s < 2; s++) {
          u32x4 o;
#pragma unroll
          for (int e = 0; e < 4; e++) {
            const float a0 = relu(acc[u][t][8 * s + 2 * e]), a1 = relu(acc[u][t][8 * s + 2 * e + 1]);
            o[e] = pk2(a0 * a0, a1 * a1);
          }
          __builtin_amdgcn_raw_buffer_store_b128(o, sWS, (t ? ho1 : ho) + (2 * u + s) * 1024, 0, 16);
          SB();
        }
  } else {
    VMCNT(0); BAR();
  }
  SB();
  ST(6);
  u32x4 ut[4];
  const u32 l16 = opq(lane * 16), uto = opq(lane * 16 + WS_UT + pair * 16384 + w * 4096);
  auto load_ut = [&](int j) {
    asm volatile("buffer_load_dwordx4 %0, %1, %2, 0 offen offset:%3 sc1" : "=v"(ut[j]) : "v"(uto), "s"(sWS), "i"(j * 1024) : "memory");
  };
  u32x4 w2r[16][2];
  u32 w2a[2][2];
  {
    const u32 a = sb + w * 8192 + 128 * (4 * hh + q4) + 8 * (p4 & 1) + (((4 * wn + 2 * blk + (p4 >> 1)) ^ ((q4 >> 1) << 2)) << 4);
    w2a[0][0] = opq(a); w2a[1][0] = opq(a ^ 64); w2a[0][1] = opq(a + 2 * SLOT); w2a[1][1] = opq((a ^ 64) + 2 * SLOT);
  }
  auto w2frag = [&](int st, int v) {
    const int sl = (2 + (st >> 2)) % 3;
    const u32 o = (sl & 1) * SLOT + (st & 3) * 2048;
    return ldstr(w2a[v][sl >> 1], o, o + 1024);
  };
  {
    f32x4 ka[2] = {{(lane == 0 && wn == 0) ? bf2f(wm ? t_dsk >> 16 : t_dsk & 0xffffu) : 0.f, 0.f, 0.f, 0.f}, {0.f, 0.f, 0.f, 0.f}};
    const u32 t0 = opq(sb + TAB + (wm * 128 + 4 * g4 + q4) * 224 + 8 * p4), t1 = opq(t0 + 32 * wn);
    u32x4 fv[8], f0[8], f1[8];
#pragma unroll
    for (int ks = 0; ks < 8; ks++) {
      const u32 o = ks * 16 * 224;
      fv[ks] = ldstr(t1, o, o + 112); f0[ks] = ldstr(t0, o + 64, o + 64 + 112); f1[ks] = ldstr(t0, o + 96, o + 96 + 112);
      if (ks < 4) load_ut(ks); else w2piece(2, ks - 4);
      if (ks >= 2) { ka[0] = MF16(f0[ks - 2], fv[ks - 2], ka[0]); ka[1] = MF16(f1[ks - 2], fv[ks - 2], ka[1]); }
      SB();
    }
#pragma unroll
    for (int ks = 6; ks < 8; ks++) { ka[0] = MF16(f0[ks], fv[ks], ka[0]); ka[1] = MF16(f1[ks], fv[ks], ka[1]); }
    VMCNT(24);
#pragma unroll
    for (int j = 0; j < 4; j++) { w2r[j][0] = w2frag(j, 0); w2r[j][1] = w2frag(j, 1); }
    LAS char* ko = (LAS char*)opq(sb + KD + wm * KD_CH + (4 * g4 * 36 + 16 * wn + (lane & 15)) * 2);
#pragma unroll
    for (int rt = 0; rt < 2; rt++)
#pragma unroll
      for (int r = 0; r < 4; r += 2) {
        const u32 pr = pk2(ka[rt][r], ka[rt][r + 1]);
        *(LAS u16*)(ko + (16 * rt + r) * 72) = (u16)pr;
        *(LAS u16*)(ko + (16 * rt + r + 1) * 72) = (u16)(pr >> 16);
      }
  }
  VMCNT(8);
  BAR();
  if (tid == 0) asm volatile("buffer_store_dword %0, %1, %2, %3 offen sc1" ::"v"(tag), "v"(0u), "s"(sWS), "s"(WS_FLAG + p * 128 + (8 * hf + jj) * 4) : "memory");
  ST(7);
  {
    u32 e[2][9];
#pragma unroll
    for (int c = 0; c < 2; c++) {
#pragma unroll
      for (int k = 0; k < 9; k++) e[c][k] = *(const LAS u16*)((LAS char*)ga[k] + c * KD_CH);
      w2piece(2, 4 + c);
    }
    LAS char* to = (LAS char*)opq(sb + TC + tid * 16);
#pragma unroll
    for (int c = 0; c < 2; c++) {
      u32x4 r1 = {e[c][8] | (e[c][7] << 16), e[c][6] | (e[c][5] << 16), e[c][4] | (e[c][3] << 16), e[c][2] | (e[c][1] << 16)};
      u32x4 r0 = {e[c][7] | (e[c][6] << 16), e[c][5] | (e[c][4] << 16), e[c][3] | (e[c][2] << 16), e[c][1] | (e[c][0] << 16)};
      *(LAS u32x4*)(to + (2 * c) * TC_STR) = r0;
      *(LAS u32x4*)(to + (2 * c + 1) * TC_STR) = r1;
      w2piece(2, 6 + c);
    }
  }
  if (tid < 8) *(LAS u32x4*)(sb + TC + (tid >> 1) * TC_STR + 4096 + (tid & 1) * 16) = u32x4{0, 0, 0, 0};
  *(LAS u32*)(sb + US + (tid >> 7) * US_STR + (tid & 127) * 4) = 0;
  asm volatile("s_waitcnt vmcnt(8)" : "+v"(ut[0]), "+v"(ut[1]), "+v"(ut[2]), "+v"(ut[3])::"memory");
  _Pragma("unroll 1") for (u32 spin = 0; spin < SPIN_CAP; spin++) {
    bool ok = true;
#pragma unroll
    for (int j = 0; j < 4; j++) ok &= ut[j][1] == tag && ut[j][3] == tag;
    if (__all(ok)) break;
#pragma unroll
    for (int j = 0; j < 4; j++) load_ut(j);
    asm volatile("s_waitcnt vmcnt(0)" : "+v"(ut[0]), "+v"(ut[1]), "+v"(ut[2]), "+v"(ut[3])::"memory");
  }
  {
    LAS char* uo = (LAS char*)opq(sb + US + 512 + (w * 256 + lane) * 4);
#pragma unroll
    for (int j = 0; j < 4; j++) {
      *(LAS u32*)(uo + j * 256) = __builtin_amdgcn_perm(ut[j][2], ut[j][0], 0x05040100);
      *(LAS u32*)(uo + j * 256 + US_STR) = __builtin_amdgcn_perm(ut[j][2], ut[j][0], 0x07060302);
      w2piece(3, j);
    }
  }
  BAR();
  ST(8);

  u32x4 hfr[28][2];
  auto load_h = [&](int i) {
    const int st = i >> 1, t = i & 1;
    hfr[st][t] = __builtin_amdgcn_raw_buffer_load_b128(sWS, l16 + (st & 3) * 1024, WS_H + ((2 * rb + (t ^ wm)) * 112 + chunk(st >> 2)) * 1024, 16);
  };
  f32x4 ca[8];
#pragma unroll
  for (int c = 0; c < 8; c++) ca[c] = f32x4{0.f, 0.f, 0.f, 0.f};
  const u32 ua = opq(sb + US + wm * US_STR + (256 - 16 - 128 * wn - 96 + 16 * (lane & 15) + 8 * g4) * 2);
  u32 tb[4];
#pragma unroll
  for (int j = 0; j < 4; j++) tb[j] = opq(sb + TC + (2 * wm + (lane & 1)) * TC_STR + (2046 - 16 - 128 * wn - 96 - 1792 + 8 * g4 - (lane & 14)) * 2 + 1024 * j);
  u32x4 fu[2][8];
  u32x2 ft[2][16];
  auto cread = [&](int f, int r) {
    const int seq[24] = {8, 9, 0, 1, 2, 3, 4, 5, 6, 7, 10, 11, 12, 13, 14, 15, 16, 17, 18, 19, 20, 21, 22, 23};
    const int k = seq[r];
    if (k < 8) fu[f & 1][k] = lds128(ua, (96 - 32 * f + 256 * k) * 2);
    else {
      const int i = (k - 8) >> 1, hl = k & 1, e = 7 - i;
      const LAS u32* t = (const LAS u32*)((LAS char*)tb[e >> 1] + (96 - 32 * f + 256 * (e & 1)) * 2 + 8 * hl);
      ft[f & 1][2 * i + hl] = u32x2{t[0], t[1]};
    }
  };
  u32 fl;
  const u32 fo = (lane & 15) * 4;
#pragma unroll
  for (int r = 0; r < 24; r++) cread(0, r);
  SB();
#pragma unroll
  for (int f = 0; f < 4; f++) {
    int m = 0;
#pragma unroll
    for (int i = 0; i < 8; i++)
#pragma unroll
      for (int e = 0; e + i < 8; e++, m++) {
        const u32x4 tf = {ft[f & 1][2 * i][0], ft[f & 1][2 * i][1], ft[f & 1][2 * i + 1][0], ft[f & 1][2 * i + 1][1]};
        ca[i + e] = MF16(fu[f & 1][e], tf, ca[i + e]);
        if (f < 3 && m >= 8 && m < 32) cread(f + 1, m - 8);
        if (f < 3) {
          if (m == 0 && f > 0) VMCNT(16);
          if (m < 8) w2r[4 * (f + 1) + (m >> 1)][m & 1] = w2frag(4 * (f + 1) + (m >> 1), m & 1);
          if (f == 2 && m == 1) asm volatile("buffer_load_dword %0, %1, %2, %3 offen sc1" : "=v"(fl) : "v"(fo), "s"(sWS), "s"(WS_FLAG + p * 128) : "memory");
          if (f == 0 && m >= 2 && m < 14 && m % 3 == 2) w2piece(3, 4 + (m - 2) / 3);
          if (m == 12) asm volatile("s_waitcnt lgkmcnt(4)" ::: "memory");
          if (m >= 14 && m % 3 == 2) w2piece(f + 4, (m - 14) / 3);
        } else if (m % 4 == 0) load_h(m / 4);
        SB();
      }
    if (f == 2) {
      asm volatile("s_waitcnt vmcnt(8)" : "+v"(fl)::"memory");
      ST(9);
      _Pragma("unroll 1") for (u32 spin = 0; spin < SPIN_CAP; spin++) {
        if (__all(fl == tag)) break;
        asm volatile("buffer_load_dword %0, %1, %2, %3 offen sc1\n\ts_waitcnt vmcnt(0)" : "=&v"(fl) : "v"(fo), "s"(sWS), "s"(WS_FLAG + p * 128) : "memory");
      }
      ST(10);
      SB();
    }
  }
  {
    f32x4 keep[4];
    const u32 ro = opq(sb + RED + w * 4096 + lane * 16);
    if (wn) {
#pragma unroll
      for (int c = 0; c < 4; c++) { *(LAS f32x4*)((LAS char*)ro + c * 1024) = ca[c]; keep[c] = ca[c + 4]; }
      asm volatile("" ::: "memory");
    } else {
#pragma unroll
      for (int c = 0; c < 4; c++) { *(LAS f32x4*)((LAS char*)ro + c * 1024) = ca[c + 4]; keep[c] = ca[c]; }
    }
    load_h(9); load_h(10);
    BAR();
    f32x4 oth[4];
#pragma unroll
    for (int c = 0; c < 4; c++) oth[c] = *(const LAS f32x4*)((LAS char*)(ro ^ 4096) + c * 1024);
    load_h(11);
    const u32 ao = opq(l16 + WS_AT + (2 * pair + wm) * 8192 + wn * 4096);
#pragma unroll
    for (int c = 0; c < 4; c++) {
      const f32x4 y = keep[c] + oth[c];
      u32x4 gr = {pk2(y[0], y[1]), tag, pk2(y[2], y[3]), tag};
      __builtin_amdgcn_raw_buffer_store_b128(gr, sWS, ao + c * 1024, 0, 16);
    }
  }
  SB();
  ST(11);

  f32x16 oc[2][2] = {};
  u32x4 at[4];
  u32x2 gg, b2v[4];
  const u32 ato = g4 * 8192 + (lane & 15) * 16;
  auto load_at = [&](int j) {
    if (j < 4) at[j] = __builtin_amdgcn_raw_buffer_load_b128(sWS, ato, WS_AT + (64 * cb2 + 16 * w + 4 * j) * 8192 + rb * 256, 16);
    else gg = __builtin_amdgcn_raw_buffer_load_b64(sWS, (lane & 31) * 8, WS_G + (64 * rb + 32 * wm) * 8, 16);
  };
  constexpr int HEAD = 12;
  u32x4 wl[2][2];
#pragma unroll
  for (int st = 0; st < 28; st++) {
    const u32x4 a0 = st < 16 ? w2r[st < 16 ? st : 0][0] : wl[st & 1][0];
    const u32x4 a1 = st < 16 ? w2r[st < 16 ? st : 0][1] : wl[st & 1][1];
    auto extra = [&](int i) {
      if (i < 56) load_h(i);
      else if (i < 60) b2v[i - 56] = __builtin_amdgcn_raw_buffer_load_b64(sB2, hh * 8 + (i - 56) * 16, cb2 * 128 + wn * 64, 0);
      else if (i < 65) load_at(i - 60);
    };
    oc[0][0] = MF32(a0, hfr[st][0], oc[0][0]);
    if (st >= 15 && st < 27) wl[(st + 1) & 1][0] = w2frag(st + 1, 0);
    extra(HEAD + 2 * st);
    SB();
    oc[1][0] = MF32(a1, hfr[st][0], oc[1][0]);
    if (st >= 15 && st < 27) wl[(st + 1) & 1][1] = w2frag(st + 1, 1);
    SB();
    oc[0][1] = MF32(a0, hfr[st][1], oc[0][1]);
    extra(HEAD + 2 * st + 1);
    SB();
    oc[1][1] = MF32(a1, hfr[st][1], oc[1][1]);
    SB();
  }
  ST(12);
  LGKM0();
#pragma unroll
  for (int j = 1; j < 4; j++)
#pragma unroll
    for (int q = 0; q < 4; q++) {
      const f32x16& s = oc[j & 1][j >> 1];
      *(LAS f32x4*)(sb + (j - 1) * SLOT + w * 8192 + q * 1024 + lane * 16) = f32x4{s[4 * q], s[4 * q + 1], s[4 * q + 2], s[4 * q + 3]};
    }
  _Pragma("unroll 1") for (u32 spin = 0; spin < SPIN_CAP; spin++) {
    bool ok = gg[1] == tag;
#pragma unroll
    for (int j = 0; j < 4; j++) ok &= at[j][1] == tag && at[j][3] == tag;
    if (__all(ok)) break;
    asm volatile("" ::: "memory");
#pragma unroll
    for (int j = 0; j < 5; j++) load_at(j);
  }
#pragma unroll
  for (int j = 0; j < 4; j++) {
    const u32 a = sb + AS + (lane & 15) * 144 + (16 * w + 4 * j + g4) * 2;
    *(LAS u16*)(a) = (u16)at[j][0];
    *(LAS u16*)(a + 16 * 144) = (u16)(at[j][0] >> 16);
    *(LAS u16*)(a + 32 * 144) = (u16)at[j][2];
    *(LAS u16*)(a + 48 * 144) = (u16)(at[j][2] >> 16);
  }
  BAR();
  ST(13);
  {
    const float g = bf2f(gg[0] & 0xffffu), gm = __uint_as_float(gg[0] & 0xffff0000u);
    const u32 aa = sb + AS + (32 * wm + (lane & 31)) * 144 + (32 * wn + 4 * hh) * 2;
#pragma unroll
    for (int q = 0; q < 4; q++) {
      f32x4 s = {oc[0][0][4 * q], oc[0][0][4 * q + 1], oc[0][0][4 * q + 2], oc[0][0][4 * q + 3]};
#pragma unroll
      for (int j = 1; j < 4; j++) s += *(const LAS f32x4*)(sb + (j - 1) * SLOT + (w ^ j) * 8192 + q * 1024 + lane * 16);
      const u32x2 av = *(const LAS u32x2*)(aa + 16 * q);
      const float o0 = g * bf2f(av[0] & 0xffffu) + gm * (s[0] + bf2f(b2v[q][0] & 0xffffu));
      const float o1 = g * __uint_as_float(av[0] & 0xffff0000u) + gm * (s[1] + __uint_as_float(b2v[q][0] & 0xffff0000u));
      const float o2 = g * bf2f(av[1] & 0xffffu) + gm * (s[2] + bf2f(b2v[q][1] & 0xffffu));
      const float o3 = g * __uint_as_float(av[1] & 0xffff0000u) + gm * (s[3] + __uint_as_float(b2v[q][1] & 0xffff0000u));
      *(LAS u32x2*)(aa + 16 * q) = u32x2{pk2(o0, o1), pk2(o2, o3)};
    }
    LGKM0();
#pragma unroll
    for (int j = 0; j < 2; j++) {
      const u32 r = 32 * wm + 16 * j + (lane >> 2);
      const u32x4 o = lds128(sb + AS + r * 144 + 64 * wn + (lane & 3) * 16);
      __builtin_amdgcn_raw_buffer_store_b128(o, sOut, r * 1024 + (lane & 3) * 16 + rb * 65536 + cb2 * 128 + wn * 64, 0, 0);
    }
  }
  if (b == 0 && w == 3) {
    _Pragma("unroll 1") for (u32 spin = 0; spin < SPIN_CAP; spin++) {
      asm volatile("" ::: "memory");
      const u32x4 f4 = __builtin_amdgcn_raw_buffer_load_b128(sWS, (lane >> 2) * 128 + (lane & 3) * 16, WS_FLAG, 16);
      if (__all(f4[0] == tag && f4[1] == tag && f4[2] == tag && f4[3] == tag)) break;
    }
    if (lane == 0) __builtin_amdgcn_raw_buffer_store_b32(tag, sWS, 0, 0, 16);
  }
  VMCNT(0);
  if (blockIdx.x == 8 && (threadIdx.x >> 6) == 0) {
    u64 r0, ta = 0, tb = 0; u32 d;
    asm volatile("s_memrealtime %0\n\ts_waitcnt lgkmcnt(0)" : "=s"(r0)::"memory");
    for (u32 i = 0; i < (1u << 22); i++) { if (__builtin_amdgcn_s_memrealtime() >= r0 + 500) break; }
    asm volatile("s_memtime %0\n\ts_waitcnt lgkmcnt(0)\n\tglobal_load_dword %2, %3, %4\n\ts_waitcnt vmcnt(0)\n\ts_memtime %1\n\ts_waitcnt lgkmcnt(0)" : "=&s"(ta), "=&s"(tb), "=&v"(d) : "v"(0u), "s"(A.bx) : "memory");
    asm volatile("s_memtime %0\n\ts_waitcnt lgkmcnt(0)\n\tglobal_load_dword %2, %3, %4\n\ts_waitcnt vmcnt(0)\n\ts_memtime %1\n\ts_waitcnt lgkmcnt(0)" : "=&s"(ta), "=&s"(tb), "=&v"(d) : "v"(256u), "s"(A.bx) : "memory");
    asm volatile("s_memtime %0\n\ts_waitcnt lgkmcnt(0)\n\tglobal_load_dword %2, %3, %4\n\ts_waitcnt vmcnt(0)\n\ts_memtime %1\n\ts_waitcnt lgkmcnt(0)" : "=&s"(ta), "=&s"(tb), "=&v"(d) : "v"(4096u), "s"(A.bx) : "memory");
    asm volatile("s_memtime %0\n\ts_waitcnt lgkmcnt(0)\n\tglobal_load_dword %2, %3, %4\n\ts_waitcnt vmcnt(0)\n\ts_memtime %1\n\ts_waitcnt lgkmcnt(0)" : "=&s"(ta), "=&s"(tb), "=&v"(d) : "v"(65536u), "s"(A.bx) : "memory");
    asm volatile("s_memtime %0\n\ts_waitcnt lgkmcnt(0)\n\tglobal_load_dword %2, %3, %4\n\ts_waitcnt vmcnt(0)\n\ts_memtime %1\n\ts_waitcnt lgkmcnt(0)" : "=&s"(ta), "=&s"(tb), "=&v"(d) : "v"(1048576u), "s"(A.bx) : "memory");
    const u64 until = r0 + 2000 + (tb - ta) * 5;
    for (u32 i = 0; i < (1u << 22); i++) { if (__builtin_amdgcn_s_memrealtime() >= until) break; }
  }

}

extern "C" void kernel_launch(void* const* d_in, const int* in_sizes, int n_in, void* d_out, int out_size,
                              void* d_ws, size_t ws_size, hipStream_t stream) {
  Args a;
  a.x = (const u16*)d_in[0]; a.lar = (const u16*)d_in[1]; a.aim = (const u16*)d_in[2]; a.cre = (const u16*)d_in[3];
  a.cim = (const u16*)d_in[4]; a.ldt = (const u16*)d_in[5]; a.dsk = (const u16*)d_in[6]; a.w1 = (const u16*)d_in[8];
  a.b1 = (const u16*)d_in[9]; a.w2 = (const u16*)d_in[10]; a.b2 = (const u16*)d_in[11]; a.gw = (const u16*)d_in[12];
  a.bx = (const char*)d_in[7];
  a.out = (u16*)d_out; a.ws = (char*)d_ws;
  fused_mix<<<256, 256, 0, stream>>>(a);
}
```

```cpp
#define STAMP 1
#define STMASK 0
#include <hip/hip_runtime.h>
#pragma clang diagnostic ignored "-Wint-to-pointer-cast"

typedef unsigned short u16;
typedef unsigned u32;
typedef unsigned long long u64;
typedef __bf16 h16x8 __attribute__((ext_vector_type(8)));
typedef __bf16 h16x2 __attribute__((ext_vector_type(2)));
typedef short s16x4 __attribute__((ext_vector_type(4)));
typedef short s16x8 __attribute__((ext_vector_type(8)));
typedef short s16x2 __attribute__((ext_vector_type(2)));
typedef float f32x2 __attribute__((ext_vector_type(2)));
typedef float f32x4 __attribute__((ext_vector_type(4)));
typedef float f32x16 __attribute__((ext_vector_type(16)));
typedef u32 u32x2 __attribute__((ext_vector_type(2)));
typedef u32 u32x4 __attribute__((ext_vector_type(4)));
#define LAS __attribute__((address_space(3)))
#define CAS __attribute__((address_space(4)))
#define GAS __attribute__((address_space(1)))
#define DI __device__ __forceinline__

constexpr int L = 2048, D = 512, F = 1792;
constexpr u32 WS_FLAG = 4096;
constexpr u32 WS_G = 65536;
constexpr u32 WS_UT = 1u << 20;
constexpr u32 WS_AT = 5u << 20;
constexpr u32 WS_H = 9u << 20;
constexpr u32 WS_END = 17u << 20;
constexpr u32 WS_STAMP = 32u << 20;
constexpr u32 SLOT = 32768;
constexpr u32 TAB = 3 * SLOT;
constexpr u32 KD = TAB + 256 * 224;
constexpr u32 KD_CH = 32 * 36 * 2;
constexpr u32 KD_ZERO = 34 * 2;
constexpr u32 ZS = KD + 2 * KD_CH;
constexpr u32 RED = TAB;
constexpr u32 TC = RED + 16384;
constexpr u32 TC_STR = 4160;
constexpr u32 US = TC + 4 * TC_STR;
constexpr u32 US_STR = (256 + 2048) * 2;
constexpr u32 AS = US + 2 * US_STR;
constexpr u32 LDS_BYTES = 163840;
static_assert(AS + 64 * 144 <= KD && ZS + 2048 <= LDS_BYTES, "lds map");
constexpr u32 SPIN_CAP = 1u << 16;

DI float bf2f(u32 v) { return __uint_as_float(v << 16); }
DI u32 pk2(float lo, float hi) { f32x2 a = {lo, hi}; return __builtin_bit_cast(u32, __builtin_convertvector(a, h16x2)); }
DI float rbf(float x) { return bf2f(pk2(x, x) & 0xffffu); }
DI float dot2(u32 a, u32 b, float c) { return __builtin_amdgcn_fdot2_f32_bf16(__builtin_bit_cast(h16x2, a), __builtin_bit_cast(h16x2, b), c, false); }
DI u32 rfl(u32 x) { return __builtin_amdgcn_readfirstlane(x); }
struct Mul { f32x2 b, n; };
DI Mul mk(f32x2 b) { return Mul{b, f32x2{-b.y, b.y}}; }
DI f32x2 cmul(f32x2 a, Mul m) { return __builtin_elementwise_fma(a.yx, m.n, a * m.b.xx); }
DI float relu(float x) { return __int_as_float(max(__float_as_int(x), 0)); }
DI u32 opq(u32 x) { asm volatile("" : "+v"(x)); return x; }
#define MF32(a, b, c) __builtin_amdgcn_mfma_f32_32x32x16_bf16(__builtin_bit_cast(h16x8, a), __builtin_bit_cast(h16x8, b), c, 0, 0, 0)
#define MF16(a, b, c) __builtin_amdgcn_mfma_f32_16x16x32_bf16(__builtin_bit_cast(h16x8, a), __builtin_bit_cast(h16x8, b), c, 0, 0, 0)
#define DMA(voff, srd, soff, dst) \
  asm volatile("s_mov_b32 m0, %0\n\ts_nop 0\n\tbuffer_load_dwordx4 %1, %2, %3 offen lds" ::"s"(dst), "v"(voff), "s"(srd), "s"(soff) : "memory")
#define SETM0(dst) asm volatile("s_mov_b32 m0, %0\n\ts_nop 0" ::"s"(dst) : "memory")
#define DMAQ(voff, srd, soff, q) asm volatile("buffer_load_dwordx4 %0, %1, %2 offen offset:%3 lds" ::"v"(voff), "s"(srd), "s"(soff), "i"((q) * 1024) : "memory")
#define MMV(acc, a, b) asm volatile("v_mfma_f32_32x32x16_bf16 %0, %1, %2, %0" : "+v"(acc) : "v"(a), "v"(b) : "memory")
#define VMCNT(n) asm volatile("s_waitcnt vmcnt(%0)" ::"i"(n) : "memory")
#define LGKM0() asm volatile("s_waitcnt lgkmcnt(0)" ::: "memory")
#define BAR() asm volatile("s_waitcnt lgkmcnt(0)\n\ts_barrier" ::: "memory")
#define SB() __builtin_amdgcn_sched_barrier(0)
#define ST(i) do { if (!((STMASK >> (i)) & 1)) break; SB(); asm volatile("s_memtime %0\n\ts_waitcnt lgkmcnt(0)" : "=s"(stamp[i])::"memory"); SB(); } while (0)

DI u32x4 lds128(u32 a, u32 o = 0) { return *(const LAS u32x4*)((LAS char*)a + o); }
DI s16x4 tr64(u32 a, u32 o) { return __builtin_amdgcn_ds_read_tr16_b64_v4i16((LAS s16x4*)((LAS char*)a + o)); }
DI u32x4 cat(s16x4 lo, s16x4 hi) { return __builtin_bit_cast(u32x4, __builtin_shufflevector(lo, hi, 0, 1, 2, 3, 4, 5, 6, 7)); }
DI u32x4 ldstr(u32 a, u32 o0, u32 o1) { return cat(tr64(a, o0), tr64(a, o1)); }

struct Args {
  const u16 *x, *lar, *aim, *cre, *cim, *ldt, *dsk, *w1, *b1, *w2, *b2, *gw;
  u16* out;
  char* ws;
};

__global__ void __launch_bounds__(256, 1) fused_mix(Args) {
  __shared__ __attribute__((aligned(16))) char smem[LDS_BYTES];
  const u32 sb = (u32)(size_t)(LAS char*)smem;
  u64 stamp[24], rt0, rt1;
  for (int i = 0; i < 24; i++) stamp[i] = 0;
  asm volatile("s_memrealtime %0\n\ts_waitcnt lgkmcnt(0)" : "=s"(rt0)::"memory");
  ST(0);
  const CAS Args* ka = (const CAS Args*)__builtin_amdgcn_kernarg_segment_ptr();
  asm volatile("" : "+s"(ka));
  const Args A = {ka->x, ka->lar, ka->aim, ka->cre, ka->cim, ka->ldt, ka->dsk, ka->w1, ka->b1, ka->w2, ka->b2, ka->gw, ka->out, ka->ws};
  const u32 tid = threadIdx.x, lane = tid & 63, w = rfl(tid >> 6);
  const u32 b = blockIdx.x, xcd = b & 7, bi = b >> 3;
  const u32 hf = xcd & 1, p = 4 * (xcd >> 1) + (bi >> 3), jj = bi & 7;
  const bool spare = jj == 7;
  const u32 cb1 = 7 * hf + jj, rb = 2 * p + hf, cb2 = jj, pair = b;
#define ROT1 (2 * (bi >> 3) + jj)
#define ROT2 ((2 * (bi >> 3) + jj) % 7)
  const u32 rot = ROT1, rot2 = ROT2;
  const u32 wm = w >> 1, wn = w & 1;
  const u32 hh = lane >> 5, blk = (lane >> 4) & 1, q4 = (lane & 15) >> 2, p4 = lane & 3, g4 = lane >> 4;

  const __amdgpu_buffer_rsrc_t sX = __builtin_amdgcn_make_buffer_rsrc((void*)A.x, 0, L * D * 2, 0x00020000);
  const __amdgpu_buffer_rsrc_t sW1 = __builtin_amdgcn_make_buffer_rsrc((void*)A.w1, 0, D * F * 2, 0x00020000);
  const __amdgpu_buffer_rsrc_t sW2 = __builtin_amdgcn_make_buffer_rsrc((void*)A.w2, 0, D * F * 2, 0x00020000);
  const __amdgpu_buffer_rsrc_t sWS = __builtin_amdgcn_make_buffer_rsrc((void*)A.ws, 0, WS_END, 0x00020000);
  const __amdgpu_buffer_rsrc_t sB2 = __builtin_amdgcn_make_buffer_rsrc((void*)A.b2, 0, D * 2, 0x00020000);
  const __amdgpu_buffer_rsrc_t sOut = __builtin_amdgcn_make_buffer_rsrc((void*)A.out, 0, L * D * 2, 0x00020000);

  asm volatile("" :: "s"(A.x), "s"(A.ws), "s"(A.gw));
  ST(14);
  u32 r_lar, r_aim, r_cre, r_cim, r_b1a, r_b1b;
  {
    const u32 o = (pair * 256 + tid) * 2;
    asm volatile("global_load_ushort %0, %1, %2" : "=v"(r_lar) : "v"(o), "s"(A.lar));
    asm volatile("global_load_ushort %0, %1, %2" : "=v"(r_aim) : "v"(o), "s"(A.aim));
    asm volatile("global_load_ushort %0, %1, %2" : "=v"(r_cre) : "v"(o), "s"(A.cre));
    asm volatile("global_load_ushort %0, %1, %2" : "=v"(r_cim) : "v"(o), "s"(A.cim));
    const u32 o1 = ((spare ? 0 : cb1) * 128 + wn * 64 + (lane & 31)) * 2;
    asm volatile("global_load_ushort %0, %1, %2" : "=v"(r_b1a) : "v"(o1), "s"(A.b1));
    asm volatile("global_load_ushort %0, %1, %2 offset:64" : "=v"(r_b1b) : "v"(o1), "s"(A.b1));
  }
  const u32 tag = *(const CAS u32*)A.ws + 1u;
  const u32 t_ldt = *(const CAS u32*)(A.ldt + 2 * pair), t_dsk = *(const CAS u32*)(A.dsk + 2 * pair);

  u32 vA[4], vB[4], v2[4];
#pragma unroll
  for (int q = 0; q < 4; q++) {
    vA[q] = (lane >> 3) * 1024 + (((lane & 7) ^ g4 ^ (4 * (q & 1))) << 4) + q * (8192 - 1024);
    vB[q] = g4 * (F * 2) + (((lane & 15) ^ (g4 << 2) ^ q) << 4) + q * (4 * F * 2 - 1024);
    v2[q] = (lane >> 3) * 1024 + (((lane & 7) ^ (blk << 2)) << 4) + q * (8192 - 1024);
  }
  const u32 xbase = (128 * p + 32 * w) * 1024, wbase = 16 * w * (F * 2) + cb1 * 256;
  auto chunk = [&](u32 c) { const u32 r = c + rot2; return 28 * w + 4 * (r >= 7 ? r - 7 : r); };
  auto w2piece = [&](u32 c, int q) {
    if ((q & 3) == 0) SETM0(sb + ((2 + c) % 3) * SLOT + w * 8192 + (q >> 2) * 4096);
    DMAQ(v2[q & 3], sW2, chunk(c) * 16384 + cb2 * 128 + (q >> 2) * 32768, q & 3);
  };

  if (!spare) {
#pragma unroll
    for (int g = 0; g < 2; g++) {
      const u32 ks = (g + rot) & 7;
      SETM0(sb + g * SLOT + w * 4096);
#pragma unroll
      for (int q = 0; q < 4; q++) DMAQ(vA[q], sX, xbase + ks * 128, q);
      SETM0(sb + g * SLOT + w * 4096 + 16384);
#pragma unroll
      for (int q = 0; q < 4; q++) DMAQ(vB[q], sW1, wbase + ks * 64 * (F * 2), q);
    }
  } else {
    const u32 vb = g4 * 1024 + (((lane & 15) ^ hh) << 4);
#pragma unroll
    for (int q = 0; q < 16; q++) DMA(vb ^ (((2 * q) & 15) << 4), sX, (64 * rb + 4 * q) * 1024 + w * 256, sb + w * 16384 + q * 1024);
    u32x4 gwv[8];
#pragma unroll
    for (int m = 0; m < 8; m++) gwv[m] = *(const GAS u32x4*)(A.gw + 128 * w + 16 * m + 8 * hh);
    VMCNT(0);
    const u32 i = lane & 31;
    const u32 ra = sb + w * 16384 + i * 512 + ((hh ^ (i & 15)) << 4);
    const u32 vo = hh * 4 * 16384 + i * 16;
    float za = 0.f, zb = 0.f;
#pragma unroll
    for (int m = 0; m < 8; m++) {
      const u32x4 ea = lds128(ra ^ (m << 5)), eb = lds128(ra ^ (m << 5), 256);
#pragma unroll
      for (int e = 0; e < 4; e++) {
        u32x4 gr = {ea[e], tag, eb[e], tag};
        __builtin_amdgcn_raw_buffer_store_b128(gr, sWS, vo + WS_UT + (64 * w + 8 * m + e) * 16384 + rb * 512, 0, 16);
        za = dot2(ea[e], gwv[m][e], za);
        zb = dot2(eb[e], gwv[m][e], zb);
      }
    }
    *(LAS f32x2*)(sb + ZS + (2 * w + hh) * 256 + i * 8) = f32x2{za, zb};
    BAR();
    if (w == 0) {
      float z = 0.f;
#pragma unroll
      for (int k = 0; k < 8; k++) z += *(const LAS float*)(sb + ZS + k * 256 + lane * 4);
      z = rbf(z);
      const float g = rbf(1.f / (1.f + __expf(-z)));
      u32x2 gr = {pk2(g, 1.f - g), tag};
      __builtin_amdgcn_raw_buffer_store_b64(gr, sWS, lane * 8, WS_G + rb * 512, 16);
    }
#pragma unroll
    for (int q = 0; q < 16; q++) w2piece(q >> 3, q & 7);
  }
  ST(1);

  u32 ga[9];
  {
    auto cell = [&](u32 pos) {
      const u32 hi = pos >> 8, ex = min(3u, 32u - (u32)__clz(hi));
      return sb + KD + (4 * ex + ((8 * hi) >> ex)) * 72 + ((pos & 255) >> 2);
    };
    const u32 P = 2040 - 8 * tid, a0 = cell(P), a8 = cell(P + 8);
    const u32 code = P < 256 ? 0x76543210u : P < 512 ? 0x83222100u : P < 1024 ? 0x88111000u : (P & 8) ? 0x88880000u : 0x88800000u;
#pragma unroll
    for (int k = 0; k < 8; k++) {
      const u32 n = (code >> (4 * k)) & 15u;
      ga[k + 1] = opq(n == 8 ? a8 : a0 + n * 72);
    }
    ga[0] = opq(P > 256 ? a0 : P == 0 ? sb + KD + KD_ZERO : sb + KD + 7 * 72 + ((P - 8) >> 2));
  }
  if (tid < 2) *(LAS u32*)(sb + KD + tid * KD_CH + KD_ZERO) = 0;

  asm volatile("s_waitcnt vmcnt(16)" : "+v"(r_lar), "+v"(r_aim), "+v"(r_cre), "+v"(r_cim), "+v"(r_b1a), "+v"(r_b1b));
  ST(2);
  {
    const float dt = rbf(__expf(bf2f(wm ? t_ldt >> 16 : t_ldt & 0xffffu)));
    const float er = -(rbf(__expf(bf2f(r_lar))) * dt) * 1.44269504088896f;
    const float wi = (bf2f(r_aim) * dt) * 0.159154943091895f;
    auto seed = [&](float t) {
      const float m = __builtin_amdgcn_exp2f(er * t), a = __builtin_amdgcn_fractf(wi * t);
      return f32x2{m * __builtin_amdgcn_cosf(a), m * __builtin_amdgcn_sinf(a)};
    };
    LAS char* ta = (LAS char*)opq(sb + TAB + tid * 224);
    auto put8 = [&](const f32x2* v, u32 off) {
      u32x4 re = {pk2(v[0].x, v[1].x), pk2(v[2].x, v[3].x), pk2(v[4].x, v[5].x), pk2(v[6].x, v[7].x)};
      u32x4 im = {pk2(v[0].y, v[1].y), pk2(v[2].y, v[3].y), pk2(v[4].y, v[5].y), pk2(v[6].y, v[7].y)};
      *(LAS u32x4*)(ta + off) = re;
      *(LAS u32x4*)(ta + 112 + off) = im;
    };
    f32x2 v[8];
    const Mul w8 = mk(seed(8.f)), w64 = mk(seed(64.f));
    v[0] = f32x2{1.f, 0.f}; v[1] = w8.b; v[2] = cmul(v[1], w8);
    const Mul w16 = mk(v[2]);
#pragma unroll
    for (int k = 3; k < 8; k++) v[k] = cmul(v[k - 2], w16);
    put8(v, 0);
    v[0] = w64.b;
#pragma unroll
    for (int r = 1; r < 4; r++) {
#pragma unroll
      for (int k = (r == 1); k < 8; k++) v[k] = cmul(v[k], w64);
      put8(v, 16 * r);
    }
    f32x2 s1 = seed(1.f), s256 = seed(256.f);
    s1.y = -s1.y; s256.y = -s256.y;
    const Mul v1 = mk(s1), W = mk(s256), v2c = mk(cmul(s1, v1));
    f32x2 u[8];
    u[0] = f32x2{2.f * (bf2f(r_cre) * dt), -2.f * (bf2f(r_cim) * dt)};
    u[1] = cmul(u[0], v1);
#pragma unroll
    for (int k = 2; k < 8; k++) u[k] = cmul(u[k - 2], v2c);
    put8(u, 64);
    v[0] = cmul(u[0], W); v[1] = cmul(u[2], W); v[2] = cmul(u[4], W); v[3] = cmul(u[6], W);
    v[4] = cmul(v[0], W); v[5] = cmul(v[2], W); v[6] = cmul(v[4], W); v[7] = cmul(v[5], W);
    put8(v, 80);
    u[0] = cmul(v[6], W); u[1] = cmul(u[0], W); u[2] = cmul(u[1], W); u[3] = cmul(u[2], W); u[4] = cmul(u[3], W);
    u[5] = u[6] = u[7] = f32x2{0.f, 0.f};
    put8(u, 96);
  }
  ST(3);

  if (!spare) {
    f32x16 acc[2][2];
    {
      const u32x4 one = {hh ? 0u : 0x3f80u, 0u, 0u, 0u};
      const u32x4 ba = {r_b1a, 0u, 0u, 0u}, bb = {r_b1b, 0u, 0u, 0u};
      asm volatile("s_nop 1\n\tv_mfma_f32_32x32x16_bf16 %0, %4, %6, 0\n\tv_mfma_f32_32x32x16_bf16 %1, %4, %6, 0\n\t"
                   "v_mfma_f32_32x32x16_bf16 %2, %5, %6, 0\n\tv_mfma_f32_32x32x16_bf16 %3, %5, %6, 0"
                   : "=&v"(acc[0][0]), "=&v"(acc[0][1]), "=&v"(acc[1][0]), "=&v"(acc[1][1]) : "v"(ba), "v"(bb), "v"(one));
    }
    const u32 xa = sb + (64 * wm + (lane & 31)) * 128 + ((hh ^ ((lane >> 1) & 7)) << 4);
    const u32 wa = sb + 16384 + 256 * (8 * hh + q4) + 8 * (p4 & 1) + (((8 * wn + 2 * blk + (p4 >> 1)) ^ ((q4 << 2) | (2 * hh))) << 4);
    u32 ax[2][4], aw[2][4];
#pragma unroll
    for (int k = 0; k < 4; k++) {
      ax[0][k] = opq(xa ^ (k << 5)); aw[0][k] = opq(wa ^ ((k & 1) << 4) ^ ((k >> 1) << 6));
      ax[1][k] = opq(ax[0][k] + 2 * SLOT); aw[1][k] = opq(aw[0][k] + 2 * SLOT);
    }
    u32x4 fx[2][2];
    s16x4 fw[2][4];
    VMCNT(8); BAR();
    ST(4);
    fx[0][0] = lds128(ax[0][0]); fw[0][0] = tr64(aw[0][0], 0); fw[0][1] = tr64(aw[0][1], 1024);
    fx[0][1] = lds128(ax[0][0], 4096); fw[0][2] = tr64(aw[0][2], 0); fw[0][3] = tr64(aw[0][3], 1024);
#pragma unroll
    for (int s = 0; s < 8; s++) {
      const u32 ks = (s + 2 + rot) & 7, mA = sb + ((s + 2) % 3) * SLOT + w * 4096;
      auto piece = [&](int q) {
        if (s >= 6) w2piece(s - 6, q);
        else if (q < 4) { if (q == 0) SETM0(mA); DMAQ(vA[q], sX, xbase + ks * 128, q); }
        else { if (q == 4) SETM0(mA + 16384); DMAQ(vB[q - 4], sW1, wbase + ks * 64 * (F * 2), q - 4); }
      };
#pragma unroll
      for (int k = 0; k < 4; k++) {
        const int c = k & 1, n = c ^ 1, kn = (k + 1) & 3, sl = (k == 3 ? s + 1 : s) % 3, hi = sl >> 1;
        const u32 o = (sl & 1) * SLOT, ow = o + kn * 4096;
        const bool more = s < 7 || k < 3;
        MMV(acc[0][0], cat(fw[c][0], fw[c][1]), fx[c][0]);
        if (k == 3) { VMCNT(6); BAR(); }
        if (more) { fx[n][0] = lds128(ax[hi][kn], o); fw[n][0] = tr64(aw[hi][0], ow); fw[n][1] = tr64(aw[hi][1], ow + 1024); }
        MMV(acc[0][1], cat(fw[c][0], fw[c][1]), fx[c][1]);
        if (more) { fx[n][1] = lds128(ax[hi][kn], o + 4096); fw[n][2] = tr64(aw[hi][2], ow); }
        piece(2 * k);
        MMV(acc[1][0], cat(fw[c][2], fw[c][3]), fx[c][0]);
        if (more) fw[n][3] = tr64(aw[hi][3], ow + 1024);
        MMV(acc[1][1], cat(fw[c][2], fw[c][3]), fx[c][1]);
        piece(2 * k + 1);
      }
    }
    asm volatile("s_nop 7\n\ts_nop 7" : "+v"(acc[0][0]), "+v"(acc[0][1]), "+v"(acc[1][0]), "+v"(acc[1][1]));
    ST(5);
    const u32 ho = opq(lane * 16 + WS_H + ((4 * p + 2 * wm) * 112 + cb1 * 8 + wn * 4) * 1024), ho1 = opq(ho + 112 * 1024);
#pragma unroll
    for (int u = 0; u < 2; u++)
#pragma unroll
      for (int t = 0; t < 2; t++)
#pragma unroll
        for (int s = 0; s < 2; s++) {
          u32x4 o;
#pragma unroll
          for (int e = 0; e < 4; e++) {
            const float a0 = relu(acc[u][t][8 * s + 2 * e]), a1 = relu(acc[u][t][8 * s + 2 * e + 1]);
            o[e] = pk2(a0 * a0, a1 * a1);
          }
          __builtin_amdgcn_raw_buffer_store_b128(o, sWS, (t ? ho1 : ho) + (2 * u + s) * 1024, 0, 16);
          SB();
        }
  } else {
    VMCNT(0); BAR();
  }
  SB();
  ST(6);
  u32x4 ut[4];
  const u32 l16 = opq(lane * 16), uto = opq(lane * 16 + WS_UT + pair * 16384 + w * 4096);
  auto load_ut = [&](int j) {
    asm volatile("buffer_load_dwordx4 %0, %1, %2, 0 offen offset:%3 sc1" : "=v"(ut[j]) : "v"(uto), "s"(sWS), "i"(j * 1024) : "memory");
  };
  u32x4 w2r[16][2];
  u32 w2a[2][2];
  {
    const u32 a = sb + w * 8192 + 128 * (4 * hh + q4) + 8 * (p4 & 1) + (((4 * wn + 2 * blk + (p4 >> 1)) ^ ((q4 >> 1) << 2)) << 4);
    w2a[0][0] = opq(a); w2a[1][0] = opq(a ^ 64); w2a[0][1] = opq(a + 2 * SLOT); w2a[1][1] = opq((a ^ 64) + 2 * SLOT);
  }
  auto w2frag = [&](int st, int v) {
    const int sl = (2 + (st >> 2)) % 3;
    const u32 o = (sl & 1) * SLOT + (st & 3) * 2048;
    return ldstr(w2a[v][sl >> 1], o, o + 1024);
  };
  {
    f32x4 ka[2] = {{(lane == 0 && wn == 0) ? bf2f(wm ? t_dsk >> 16 : t_dsk & 0xffffu) : 0.f, 0.f, 0.f, 0.f}, {0.f, 0.f, 0.f, 0.f}};
    const u32 t0 = opq(sb + TAB + (wm * 128 + 4 * g4 + q4) * 224 + 8 * p4), t1 = opq(t0 + 32 * wn);
    u32x4 fv[8], f0[8], f1[8];
#pragma unroll
    for (int ks = 0; ks < 8; ks++) {
      const u32 o = ks * 16 * 224;
      fv[ks] = ldstr(t1, o, o + 112); f0[ks] = ldstr(t0, o + 64, o + 64 + 112); f1[ks] = ldstr(t0, o + 96, o + 96 + 112);
      if (ks < 4) load_ut(ks); else w2piece(2, ks - 4);
      if (ks >= 2) { ka[0] = MF16(f0[ks - 2], fv[ks - 2], ka[0]); ka[1] = MF16(f1[ks - 2], fv[ks - 2], ka[1]); }
      SB();
    }
#pragma unroll
    for (int ks = 6; ks < 8; ks++) { ka[0] = MF16(f0[ks], fv[ks], ka[0]); ka[1] = MF16(f1[ks], fv[ks], ka[1]); }
    VMCNT(24);
#pragma unroll
    for (int j = 0; j < 4; j++) { w2r[j][0] = w2frag(j, 0); w2r[j][1] = w2frag(j, 1); }
    LAS char* ko = (LAS char*)opq(sb + KD + wm * KD_CH + (4 * g4 * 36 + 16 * wn + (lane & 15)) * 2);
#pragma unroll
    for (int rt = 0; rt < 2; rt++)
#pragma unroll
      for (int r = 0; r < 4; r += 2) {
        const u32 pr = pk2(ka[rt][r], ka[rt][r + 1]);
        *(LAS u16*)(ko + (16 * rt + r) * 72) = (u16)pr;
        *(LAS u16*)(ko + (16 * rt + r + 1) * 72) = (u16)(pr >> 16);
      }
  }
  VMCNT(8);
  BAR();
  if (tid == 0) asm volatile("buffer_store_dword %0, %1, %2, %3 offen sc1" ::"v"(tag), "v"(0u), "s"(sWS), "s"(WS_FLAG + p * 128 + (8 * hf + jj) * 4) : "memory");
  ST(7);
  {
    u32 e[2][9];
#pragma unroll
    for (int c = 0; c < 2; c++) {
#pragma unroll
      for (int k = 0; k < 9; k++) e[c][k] = *(const LAS u16*)((LAS char*)ga[k] + c * KD_CH);
      w2piece(2, 4 + c);
    }
    LAS char* to = (LAS char*)opq(sb + TC + tid * 16);
#pragma unroll
    for (int c = 0; c < 2; c++) {
      u32x4 r1 = {e[c][8] | (e[c][7] << 16), e[c][6] | (e[c][5] << 16), e[c][4] | (e[c][3] << 16), e[c][2] | (e[c][1] << 16)};
      u32x4 r0 = {e[c][7] | (e[c][6] << 16), e[c][5] | (e[c][4] << 16), e[c][3] | (e[c][2] << 16), e[c][1] | (e[c][0] << 16)};
      *(LAS u32x4*)(to + (2 * c) * TC_STR) = r0;
      *(LAS u32x4*)(to + (2 * c + 1) * TC_STR) = r1;
      w2piece(2, 6 + c);
    }
  }
  if (tid < 8) *(LAS u32x4*)(sb + TC + (tid >> 1) * TC_STR + 4096 + (tid & 1) * 16) = u32x4{0, 0, 0, 0};
  *(LAS u32*)(sb + US + (tid >> 7) * US_STR + (tid & 127) * 4) = 0;
  asm volatile("s_waitcnt vmcnt(8)" : "+v"(ut[0]), "+v"(ut[1]), "+v"(ut[2]), "+v"(ut[3])::"memory");
  _Pragma("unroll 1") for (u32 spin = 0; spin < SPIN_CAP; spin++) {
    bool ok = true;
#pragma unroll
    for (int j = 0; j < 4; j++) ok &= ut[j][1] == tag && ut[j][3] == tag;
    if (__all(ok)) break;
#pragma unroll
    for (int j = 0; j < 4; j++) load_ut(j);
    asm volatile("s_waitcnt vmcnt(0)" : "+v"(ut[0]), "+v"(ut[1]), "+v"(ut[2]), "+v"(ut[3])::"memory");
  }
  {
    LAS char* uo = (LAS char*)opq(sb + US + 512 + (w * 256 + lane) * 4);
#pragma unroll
    for (int j = 0; j < 4; j++) {
      *(LAS u32*)(uo + j * 256) = __builtin_amdgcn_perm(ut[j][2], ut[j][0], 0x05040100);
      *(LAS u32*)(uo + j * 256 + US_STR) = __builtin_amdgcn_perm(ut[j][2], ut[j][0], 0x07060302);
      w2piece(3, j);
    }
  }
  BAR();
  ST(8);

  u32x4 hfr[28][2];
  auto load_h = [&](int i) {
    const int st = i >> 1, t = i & 1;
    hfr[st][t] = __builtin_amdgcn_raw_buffer_load_b128(sWS, l16 + (st & 3) * 1024, WS_H + ((2 * rb + (t ^ wm)) * 112 + chunk(st >> 2)) * 1024, 16);
  };
  f32x4 ca[8];
#pragma unroll
  for (int c = 0; c < 8; c++) ca[c] = f32x4{0.f, 0.f, 0.f, 0.f};
  const u32 ua = opq(sb + US + wm * US_STR + (256 - 16 - 128 * wn - 96 + 16 * (lane & 15) + 8 * g4) * 2);
  u32 tb[4];
#pragma unroll
  for (int j = 0; j < 4; j++) tb[j] = opq(sb + TC + (2 * wm + (lane & 1)) * TC_STR + (2046 - 16 - 128 * wn - 96 - 1792 + 8 * g4 - (lane & 14)) * 2 + 1024 * j);
  u32x4 fu[2][8];
  u32x2 ft[2][16];
  auto cread = [&](int f, int r) {
    const int seq[24] = {8, 9, 0, 1, 2, 3, 4, 5, 6, 7, 10, 11, 12, 13, 14, 15, 16, 17, 18, 19, 20, 21, 22, 23};
    const int k = seq[r];
    if (k < 8) fu[f & 1][k] = lds128(ua, (96 - 32 * f + 256 * k) * 2);
    else {
      const int i = (k - 8) >> 1, hl = k & 1, e = 7 - i;
      const LAS u32* t = (const LAS u32*)((LAS char*)tb[e >> 1] + (96 - 32 * f + 256 * (e & 1)) * 2 + 8 * hl);
      ft[f & 1][2 * i + hl] = u32x2{t[0], t[1]};
    }
  };
  u32 fl;
  const u32 fo = (lane & 15) * 4;
#pragma unroll
  for (int r = 0; r < 24; r++) cread(0, r);
  SB();
#pragma unroll
  for (int f = 0; f < 4; f++) {
    int m = 0;
#pragma unroll
    for (int i = 0; i < 8; i++)
#pragma unroll
      for (int e = 0; e + i < 8; e++, m++) {
        const u32x4 tf = {ft[f & 1][2 * i][0], ft[f & 1][2 * i][1], ft[f & 1][2 * i + 1][0], ft[f & 1][2 * i + 1][1]};
        ca[i + e] = MF16(fu[f & 1][e], tf, ca[i + e]);
        if (f < 3 && m >= 8 && m < 32) cread(f + 1, m - 8);
        if (f < 3) {
          if (m == 0 && f > 0) VMCNT(16);
          if (m < 8) w2r[4 * (f + 1) + (m >> 1)][m & 1] = w2frag(4 * (f + 1) + (m >> 1), m & 1);
          if (f == 2 && m == 1) asm volatile("buffer_load_dword %0, %1, %2, %3 offen sc1" : "=v"(fl) : "v"(fo), "s"(sWS), "s"(WS_FLAG + p * 128) : "memory");
          if (f == 0 && m >= 2 && m < 14 && m % 3 == 2) w2piece(3, 4 + (m - 2) / 3);
          if (m == 12) asm volatile("s_waitcnt lgkmcnt(4)" ::: "memory");
          if (m >= 14 && m % 3 == 2) w2piece(f + 4, (m - 14) / 3);
        } else if (m % 4 == 0) load_h(m / 4);
        SB();
      }
    if (f == 2) {
      asm volatile("s_waitcnt vmcnt(8)" : "+v"(fl)::"memory");
      ST(9);
      _Pragma("unroll 1") for (u32 spin = 0; spin < SPIN_CAP; spin++) {
        if (__all(fl == tag)) break;
        asm volatile("buffer_load_dword %0, %1, %2, %3 offen sc1\n\ts_waitcnt vmcnt(0)" : "=&v"(fl) : "v"(fo), "s"(sWS), "s"(WS_FLAG + p * 128) : "memory");
      }
      ST(10);
      SB();
    }
  }
  {
    f32x4 keep[4];
    const u32 ro = opq(sb + RED + w * 4096 + lane * 16);
    if (wn) {
#pragma unroll
      for (int c = 0; c < 4; c++) { *(LAS f32x4*)((LAS char*)ro + c * 1024) = ca[c]; keep[c] = ca[c + 4]; }
      asm volatile("" ::: "memory");
    } else {
#pragma unroll
      for (int c = 0; c < 4; c++) { *(LAS f32x4*)((LAS char*)ro + c * 1024) = ca[c + 4]; keep[c] = ca[c]; }
    }
    load_h(9); load_h(10);
    BAR();
    f32x4 oth[4];
#pragma unroll
    for (int c = 0; c < 4; c++) oth[c] = *(const LAS f32x4*)((LAS char*)(ro ^ 4096) + c * 1024);
    load_h(11);
    const u32 ao = opq(l16 + WS_AT + (2 * pair + wm) * 8192 + wn * 4096);
#pragma unroll
    for (int c = 0; c < 4; c++) {
      const f32x4 y = keep[c] + oth[c];
      u32x4 gr = {pk2(y[0], y[1]), tag, pk2(y[2], y[3]), tag};
      __builtin_amdgcn_raw_buffer_store_b128(gr, sWS, ao + c * 1024, 0, 16);
    }
  }
  SB();
  ST(11);

  f32x16 oc[2][2] = {};
  u32x4 at[4];
  u32x2 gg, b2v[4];
  const u32 ato = g4 * 8192 + (lane & 15) * 16;
  auto load_at = [&](int j) {
    if (j < 4) at[j] = __builtin_amdgcn_raw_buffer_load_b128(sWS, ato, WS_AT + (64 * cb2 + 16 * w + 4 * j) * 8192 + rb * 256, 16);
    else gg = __builtin_amdgcn_raw_buffer_load_b64(sWS, (lane & 31) * 8, WS_G + (64 * rb + 32 * wm) * 8, 16);
  };
  constexpr int HEAD = 12;
  u32x4 wl[2][2];
#pragma unroll
  for (int st = 0; st < 28; st++) {
    const u32x4 a0 = st < 16 ? w2r[st < 16 ? st : 0][0] : wl[st & 1][0];
    const u32x4 a1 = st < 16 ? w2r[st < 16 ? st : 0][1] : wl[st & 1][1];
    auto extra = [&](int i) {
      if (i < 56) load_h(i);
      else if (i < 60) b2v[i - 56] = __builtin_amdgcn_raw_buffer_load_b64(sB2, hh * 8 + (i - 56) * 16, cb2 * 128 + wn * 64, 0);
      else if (i < 65) load_at(i - 60);
    };
    oc[0][0] = MF32(a0, hfr[st][0], oc[0][0]);
    if (st >= 15 && st < 27) wl[(st + 1) & 1][0] = w2frag(st + 1, 0);
    extra(HEAD + 2 * st);
    SB();
    oc[1][0] = MF32(a1, hfr[st][0], oc[1][0]);
    if (st >= 15 && st < 27) wl[(st + 1) & 1][1] = w2frag(st + 1, 1);
    SB();
    oc[0][1] = MF32(a0, hfr[st][1], oc[0][1]);
    extra(HEAD + 2 * st + 1);
    SB();
    oc[1][1] = MF32(a1, hfr[st][1], oc[1][1]);
    SB();
  }
  ST(12);
  LGKM0();
#pragma unroll
  for (int j = 1; j < 4; j++)
#pragma unroll
    for (int q = 0; q < 4; q++) {
      const f32x16& s = oc[j & 1][j >> 1];
      *(LAS f32x4*)(sb + (j - 1) * SLOT + w * 8192 + q * 1024 + lane * 16) = f32x4{s[4 * q], s[4 * q + 1], s[4 * q + 2], s[4 * q + 3]};
    }
  _Pragma("unroll 1") for (u32 spin = 0; spin < SPIN_CAP; spin++) {
    bool ok = gg[1] == tag;
#pragma unroll
    for (int j = 0; j < 4; j++) ok &= at[j][1] == tag && at[j][3] == tag;
    if (__all(ok)) break;
    asm volatile("" ::: "memory");
#pragma unroll
    for (int j = 0; j < 5; j++) load_at(j);
  }
#pragma unroll
  for (int j = 0; j < 4; j++) {
    const u32 a = sb + AS + (lane & 15) * 144 + (16 * w + 4 * j + g4) * 2;
    *(LAS u16*)(a) = (u16)at[j][0];
    *(LAS u16*)(a + 16 * 144) = (u16)(at[j][0] >> 16);
    *(LAS u16*)(a + 32 * 144) = (u16)at[j][2];
    *(LAS u16*)(a + 48 * 144) = (u16)(at[j][2] >> 16);
  }
  BAR();
  ST(13);
  {
    const float g = bf2f(gg[0] & 0xffffu), gm = __uint_as_float(gg[0] & 0xffff0000u);
    const u32 aa = sb + AS + (32 * wm + (lane & 31)) * 144 + (32 * wn + 4 * hh) * 2;
#pragma unroll
    for (int q = 0; q < 4; q++) {
      f32x4 s = {oc[0][0][4 * q], oc[0][0][4 * q + 1], oc[0][0][4 * q + 2], oc[0][0][4 * q + 3]};
#pragma unroll
      for (int j = 1; j < 4; j++) s += *(const LAS f32x4*)(sb + (j - 1) * SLOT + (w ^ j) * 8192 + q * 1024 + lane * 16);
      const u32x2 av = *(const LAS u32x2*)(aa + 16 * q);
      const float o0 = g * bf2f(av[0] & 0xffffu) + gm * (s[0] + bf2f(b2v[q][0] & 0xffffu));
      const float o1 = g * __uint_as_float(av[0] & 0xffff0000u) + gm * (s[1] + __uint_as_float(b2v[q][0] & 0xffff0000u));
      const float o2 = g * bf2f(av[1] & 0xffffu) + gm * (s[2] + bf2f(b2v[q][1] & 0xffffu));
      const float o3 = g * __uint_as_float(av[1] & 0xffff0000u) + gm * (s[3] + __uint_as_float(b2v[q][1] & 0xffff0000u));
      *(LAS u32x2*)(aa + 16 * q) = u32x2{pk2(o0, o1), pk2(o2, o3)};
    }
    LGKM0();
#pragma unroll
    for (int j = 0; j < 2; j++) {
      const u32 r = 32 * wm + 16 * j + (lane >> 2);
      const u32x4 o = lds128(sb + AS + r * 144 + 64 * wn + (lane & 3) * 16);
      __builtin_amdgcn_raw_buffer_store_b128(o, sOut, r * 1024 + (lane & 3) * 16 + rb * 65536 + cb2 * 128 + wn * 64, 0, 0);
    }
  }
  if (b == 0 && w == 3) {
    _Pragma("unroll 1") for (u32 spin = 0; spin < SPIN_CAP; spin++) {
      asm volatile("" ::: "memory");
      const u32x4 f4 = __builtin_amdgcn_raw_buffer_load_b128(sWS, (lane >> 2) * 128 + (lane & 3) * 16, WS_FLAG, 16);
      if (__all(f4[0] == tag && f4[1] == tag && f4[2] == tag && f4[3] == tag)) break;
    }
    if (lane == 0) __builtin_amdgcn_raw_buffer_store_b32(tag, sWS, 0, 0, 16);
  }
  if (blockIdx.x == 8 && (threadIdx.x >> 6) == 0) { const u64 until = *(const CAS u64*)(A.ws + WS_STAMP) + 6000; for (u32 i = 0; i < (1u << 22); i++) { if (__builtin_amdgcn_s_memrealtime() >= until) break; } }

}

__global__ void __launch_bounds__(256, 1) probe(u64*) {
  __shared__ char sm[163840];
  u64 t; asm volatile("s_memrealtime %0\n\ts_waitcnt lgkmcnt(0)" : "=s"(t)::"memory");
  asm volatile("v_mov_b32 v255, 0\n\tv_accvgpr_write_b32 a255, 0":::"v255","a255");
  const CAS u64* ka = (const CAS u64*)__builtin_amdgcn_kernarg_segment_ptr();
  asm volatile("" : "+s"(ka));
  GAS u64* p = (GAS u64*)ka[0];
  sm[threadIdx.x] = 1;
  if (threadIdx.x == 0 && blockIdx.x == 8) { p[0] = t; p[1] = sm[t & 63]; }
}
extern "C" void kernel_launch(void* const* d_in, const int* in_sizes, int n_in, void* d_out, int out_size,
                              void* d_ws, size_t ws_size, hipStream_t stream) {
  Args a;
  a.x = (const u16*)d_in[0]; a.lar = (const u16*)d_in[1]; a.aim = (const u16*)d_in[2]; a.cre = (const u16*)d_in[3];
  a.cim = (const u16*)d_in[4]; a.ldt = (const u16*)d_in[5]; a.dsk = (const u16*)d_in[6]; a.w1 = (const u16*)d_in[8];
  a.b1 = (const u16*)d_in[9]; a.w2 = (const u16*)d_in[10]; a.b2 = (const u16*)d_in[11]; a.gw = (const u16*)d_in[12];
  a.out = (u16*)d_out; a.ws = (char*)d_ws;
  probe<<<256, 256, 0, stream>>>((u64*)((char*)d_ws + WS_STAMP));
  fused_mix<<<256, 256, 0, stream>>>(a);
}
```

```cpp
#define STAMP 1
#define RT 1
#define STMASK 1
#include <hip/hip_runtime.h>
#pragma clang diagnostic ignored "-Wint-to-pointer-cast"

typedef unsigned short u16;
typedef unsigned u32;
typedef unsigned long long u64;
typedef __bf16 h16x8 __attribute__((ext_vector_type(8)));
typedef __bf16 h16x2 __attribute__((ext_vector_type(2)));
typedef short s16x4 __attribute__((ext_vector_type(4)));
typedef short s16x8 __attribute__((ext_vector_type(8)));
typedef short s16x2 __attribute__((ext_vector_type(2)));
typedef float f32x2 __attribute__((ext_vector_type(2)));
typedef float f32x4 __attribute__((ext_vector_type(4)));
typedef float f32x16 __attribute__((ext_vector_type(16)));
typedef u32 u32x2 __attribute__((ext_vector_type(2)));
typedef u32 u32x4 __attribute__((ext_vector_type(4)));
typedef u32 u32x16 __attribute__((ext_vector_type(16)));
#define LAS __attribute__((address_space(3)))
#define CAS __attribute__((address_space(4)))
#define GAS __attribute__((address_space(1)))
#define DI __device__ __forceinline__

constexpr int L = 2048, D = 512, F = 1792;
constexpr u32 WS_UF = 8192;
constexpr u32 WS_HF = 4096;
constexpr u32 WS_G = 1u << 19;
#define UTS 4096
#define ATS 16384
#define WSO 0
constexpr u32 WS_UT = 1u << 20;
constexpr u32 WS_AT = 4u << 20;
constexpr u32 WS_H = 9u << 20;
constexpr u32 WS_END = 16u << 20;
constexpr u32 WS_STAMP = 32u << 20;
constexpr u32 SLOT = 32768;
constexpr u32 TAB = 3 * SLOT;
constexpr u32 TC = 4 * SLOT;
constexpr u32 TC_STR = 4160;
constexpr u32 US = TC + 4 * TC_STR;
constexpr u32 US_STR = (256 + 2048) * 2;
constexpr u32 KD_CH = 32 * 36 * 2;
constexpr u32 LDS_BYTES = 163840;
constexpr u32 KD = LDS_BYTES - 2 * KD_CH;
constexpr u32 KD_ZERO = 34 * 2;
constexpr u32 CNT = KD - 64;
constexpr u32 FD = CNT + 16, RDY = CNT + 32, GO = CNT + 48;
constexpr u32 RED = TC;
static_assert(US + 2 * US_STR <= CNT && TAB + 256 * 224 + 16 <= CNT, "lds map");
constexpr u32 SPIN_CAP = 1u << 16;
#define EPK 0
#define WDELAY 0
#define CBO 4
#define TAILN 4
#define EXPE 0
#define LSL 4
#define TPRIO 3
#define UTRY 1
#define HEAD 12
#define WPRE 3
#define PUBAT 0
#define PPOL " nt"
#define PPOL2 ""
#define XPOL ""
#define NPF 4
#define PFDELAY 24
#define POLLAT 6
#define PFC 0
#define LA 3
#define OUTPOL 0
#define UPUB 2
#define RSLEEP 8
#define USLEEP 8
#define HSLEEP 2
#define PPAIR 4
#define ATAT 16
#define G2PRIO 0
#define CVPRIO 1

DI float bf2f(u32 v) { return __uint_as_float(v << 16); }
DI u32 pk2(float lo, float hi) { f32x2 a = {lo, hi}; return __builtin_bit_cast(u32, __builtin_convertvector(a, h16x2)); }
DI float rbf(float x) { return bf2f(pk2(x, x) & 0xffffu); }
DI float dot2(u32 a, u32 b, float c) { return __builtin_amdgcn_fdot2_f32_bf16(__builtin_bit_cast(h16x2, a), __builtin_bit_cast(h16x2, b), c, false); }
DI u32 rfl(u32 x) { return __builtin_amdgcn_readfirstlane(x); }
struct Mul { f32x2 b, n; };
DI Mul mk(f32x2 b) { return Mul{b, f32x2{-b.y, b.y}}; }
DI f32x2 cmul(f32x2 a, Mul m) { return __builtin_elementwise_fma(a.yx, m.n, a * m.b.xx); }
DI float mul1(float a, float b) { float r; asm("v_mul_f32 %0, %1, %2" : "=v"(r) : "v"(a), "v"(b)); return r; }
DI float add1(float a, float b) { float r; asm("v_add_f32 %0, %1, %2" : "=v"(r) : "v"(a), "v"(b)); return r; }
DI float fma1(float a, float b, float c) { float r; asm("v_fma_f32 %0, %1, %2, %3" : "=v"(r) : "v"(a), "v"(b), "v"(c)); return r; }
DI float hi2f(u32 v) { return __uint_as_float(v & 0xffff0000u); }
DI float fopq(float x) { asm volatile("" : "+v"(x)); return x; }
DI float relu(float x) { return __int_as_float(max(__float_as_int(x), 0)); }
DI u32 sopq(u32 x) { asm volatile("" : "+s"(x)); return x; }
DI u32 opq(u32 x) { asm volatile("" : "+v"(x)); return x; }
#define MF32(a, b, c) __builtin_amdgcn_mfma_f32_32x32x16_bf16(__builtin_bit_cast(h16x8, a), __builtin_bit_cast(h16x8, b), c, 0, 0, 0)
#define MF16(a, b, c) __builtin_amdgcn_mfma_f32_16x16x32_bf16(__builtin_bit_cast(h16x8, a), __builtin_bit_cast(h16x8, b), c, 0, 0, 0)
#define DMA(voff, srd, soff, dst) \
  asm volatile("s_mov_b32 m0, %0\n\ts_nop 0\n\tbuffer_load_dwordx4 %1, %2, %3 offen lds" ::"s"(dst), "v"(voff), "s"(srd), "s"(soff) : "memory")
#define SETM0(base, off) asm volatile("s_add_u32 m0, %0, %1\n\ts_nop 0" ::"s"(base), "i"(off) : "memory", "scc")
#define DMAP(pol, voff, srd, soff, q) asm volatile("buffer_load_dwordx4 %0, %1, %2 offen offset:%3" pol " lds" ::"v"(voff), "s"(srd), "s"(soff), "i"((q) * 1024) : "memory")
#define DMAQ(voff, srd, soff, q) DMAP("", voff, srd, soff, q)
#define MMV(acc, a, b) asm volatile("v_mfma_f32_32x32x16_bf16 %0, %1, %2, %0" : "+v"(acc) : "v"(a), "v"(b) : "memory")
#define VMCNT(n) asm volatile("s_waitcnt vmcnt(%0)" ::"i"(n) : "memory")
#define LGKM0() asm volatile("s_waitcnt lgkmcnt(0)" ::: "memory")
#define BAR() asm volatile("s_waitcnt lgkmcnt(0)\n\ts_barrier" ::: "memory")
#define SB() __builtin_amdgcn_sched_barrier(0)
#define STI "s_memrealtime"
#define ST(i) do { if (!((STMASK >> (i)) & 1)) break; SB(); asm volatile(STI " %0\n\ts_waitcnt lgkmcnt(0)" : "=s"(stamp[(i) % 24])::"memory"); SB(); } while (0)

template <int SL = 1> DI void lds_wait(u32 a, u32 n) {
  _Pragma("unroll 1") for (u32 spin = 0; spin < (1u << 22); spin++) {
    asm volatile("" ::: "memory");
    if (__all(*(volatile LAS u32*)a >= n)) break;
    __builtin_amdgcn_s_sleep(SL);
  }
  asm volatile("" ::: "memory");
}
template <int V> struct IC { static constexpr int v = V; };
template <int I, int N, class Fn> DI void sfor(Fn&& f) { if constexpr (I < N) { f(IC<I>{}); sfor<I + 1, N>(f); } }
constexpr int step(int i) { return i < 28 ? i : i < 32 ? i - 16 : i < 44 ? i - 32 : i - 28; }
DI u32x4 lds128(u32 a, u32 o = 0) { return *(const LAS u32x4*)((LAS char*)a + o); }
DI s16x4 tr64(u32 a, u32 o) { return __builtin_amdgcn_ds_read_tr16_b64_v4i16((LAS s16x4*)((LAS char*)a + o)); }
DI u32x4 cat(s16x4 lo, s16x4 hi) { return __builtin_bit_cast(u32x4, __builtin_shufflevector(lo, hi, 0, 1, 2, 3, 4, 5, 6, 7)); }
DI u32x4 ldstr(u32 a, u32 o0, u32 o1) { return cat(tr64(a, o0), tr64(a, o1)); }

struct Srd { const void* p; u32 bytes, flags; };
struct Args {
  Srd x, w1, w2;
  char* ws;
  u16* out;
  const u16 *lar, *aim, *cre, *cim, *ldt, *dsk, *b1, *b2, *gw;
};
DI u32 colblock(u32 jj, u32 w2lo) { return (jj + CBO - (w2lo >> 7)) & 7; }
DI u64 mk64(u32 lo, u32 hi) { return (u64)lo | ((u64)hi << 32); }
#define STAMP_OUT() do { VMCNT(0); ST(23); if (b == 12 && wave == 0) { const u64 until = rt0 + 4000 + 10 * (stamp[0] - stamp[0]); for (u32 i_ = 0; i_ < (1u << 22); i_++) { if (__builtin_amdgcn_s_memrealtime() >= until) break; } } } while (0)

__global__ void __launch_bounds__(512, 2) fused_mix(Args) {
  __shared__ __attribute__((aligned(16))) char smem[LDS_BYTES];
  const u32 sb = (u32)(size_t)(LAS char*)smem;
  u64 stamp[24], rt0, rt1;
  for (int i = 0; i < 24; i++) stamp[i] = 0;
  asm volatile("s_memrealtime %0\n\ts_waitcnt lgkmcnt(0)" : "=s"(rt0)::"memory");
  ST(0);
  u64 pc;
  u32 d0, d1, d2;
  asm volatile("s_load_dword %0, %4, 0\n\ts_load_dword %1, %4, 64\n\ts_load_dword %2, %4, 128\n\ts_getpc_b64 %3\n.Lcode_start:"
               : "=&s"(d0), "=&s"(d1), "=&s"(d2), "=&s"(pc) : "s"(__builtin_amdgcn_kernarg_segment_ptr()));
  SB();
  const u32 tid = threadIdx.x & 255, lane = tid & 63, wave = rfl(threadIdx.x >> 6), w = wave & 3;
  const u32 b = blockIdx.x, xcd = b & 7, bi = b >> 3;
  const u32 hf = xcd & 1, p = 4 * (xcd >> 1) + (bi >> 3), jj = bi & 7;
  const bool spare = jj == 7;
  const u32 cb1 = 7 * hf + jj, rb = 2 * p + hf, pair = b;
  const u32 wm = w >> 1, wn = w & 1;
  const u32 hh = lane >> 5, blk = (lane >> 4) & 1, q4 = (lane & 15) >> 2, p4 = lane & 3, g4 = lane >> 4;
  const u32 l16 = opq(lane * 16);
  const u32 rot = 2 * (bi >> 3) + jj;

  if (wave >= 4) {
    u32x16 k1;
    u32x16 k0;
    u64 p_gw;
    asm volatile("s_load_dwordx16 %0, %3, 64\n\ts_load_dwordx16 %1, %3, 0\n\ts_load_dwordx2 %2, %3, 128"
                 : "=&s"(k1), "=&s"(k0), "=&s"(p_gw) : "s"(__builtin_amdgcn_kernarg_segment_ptr()));
  SB();
    __builtin_amdgcn_s_setprio(3);
    if (w == 0) {
      u32 bytes;
      asm volatile("s_mov_b32 %0, .Lcode_end - .Lcode_start" : "=s"(bytes));
      const u32x4 sC = {(u32)pc, (u32)(pc >> 32), bytes, 0x00020000};
      DMA(l16, sC, bi * 1024, sb + TAB);
    }
    const u32 po = opq((pair * 256 + tid) * 2);
    const u32 ta = opq(sb + TAB + tid * 224);
    const u32 t0 = opq(sb + TAB + (wm * 128 + 4 * g4 + q4) * 224 + 8 * p4), t1 = opq(t0 + 32 * wn);
    const u32 ko = opq(sb + KD + wm * KD_CH + (4 * g4 * 36 + 16 * wn + (lane & 15)) * 2);
    asm volatile("s_waitcnt lgkmcnt(0)" : "+s"(k1), "+s"(k0), "+s"(p_gw) : "s"(d0), "s"(d1), "s"(d2));
    char* const ws = (char*)mk64(k0[12], k0[13]);
    const u32 cb2 = colblock(jj, k0[8]);
    u32 r_lar, r_aim, r_cre, r_cim, tag, t_ldt, t_dsk;
    asm volatile("global_load_ushort %0, %1, %2" PPOL : "=v"(r_lar) : "v"(po), "s"(mk64(k1[0], k1[1])));
    asm volatile("global_load_ushort %0, %1, %2" PPOL : "=v"(r_aim) : "v"(po), "s"(mk64(k1[2], k1[3])));
    asm volatile("global_load_ushort %0, %1, %2" PPOL : "=v"(r_cre) : "v"(po), "s"(mk64(k1[4], k1[5])));
    asm volatile("global_load_ushort %0, %1, %2" PPOL : "=v"(r_cim) : "v"(po), "s"(mk64(k1[6], k1[7])));
    u32 r_x[4];
    if (PPAIR) {
#pragma unroll
      for (int i = 0; i < 4; i++) asm volatile("global_load_ushort %0, %1, %2" : "=v"(r_x[i]) : "v"(po ^ (PPAIR * 512)), "s"(mk64(k1[2 * i], k1[2 * i + 1])));
    }
    asm volatile("s_load_dword %0, %3, 0\n\ts_load_dword %1, %4, %6\n\ts_load_dword %2, %5, %6"
                 : "=&s"(tag), "=&s"(t_ldt), "=&s"(t_dsk) : "s"(ws), "s"(mk64(k1[8], k1[9])), "s"(mk64(k1[10], k1[11])), "s"(pair * 4));

    ST(1);
    const __amdgpu_buffer_rsrc_t sWS = __builtin_amdgcn_make_buffer_rsrc((void*)ws, 0, WS_END, 0x00020000);
    u32 ga[9];
    {
      auto cell = [&](u32 pos) {
        const u32 hi = pos >> 8, ex = min(3u, 32u - (u32)__clz(hi));
        return sb + KD + (4 * ex + ((8 * hi) >> ex)) * 72 + ((pos & 255) >> 2);
      };
      const u32 P = 2040 - 8 * tid, a0 = cell(P), a8 = cell(P + 8);
      const u32 c0 = w == 3 ? 0x83222100u : w == 2 ? 0x88111000u : 0x88880000u, c1 = w == 3 ? 0x76543210u : w == 2 ? 0x88111000u : 0x88800000u;
      const u32 code = (w == 3 ? hh : (lane & 1)) ? c1 : c0;
#pragma unroll
      for (int k = 0; k < 8; k++) {
        const u32 n = (code >> (4 * k)) & 15u;
        ga[k + 1] = opq(n == 8 ? a8 : a0 + n * 72);
      }
      const u32 lowa = opq(P == 0 ? sb + KD + KD_ZERO : sb + KD + 7 * 72 + ((P - 8) >> 2));
      ga[0] = opq(P > 256 ? a0 : lowa);
    }
    *(LAS u32*)(hh ? sb + CNT + (lane & 15) * 4 : sb + KD + (lane & 1) * KD_CH + KD_ZERO) = 0;
    const u32 to = opq(sb + TC + tid * 16);
    const u32 ur = opq(sb + hf * 8192 + (lane & 7) * 1024 + (((lane >> 3) ^ (4 * (lane & 1))) << 4) + 4 * w);
    const u32 uw = opq(WS_UT + ((rot & 7) * 64 + (lane >> 3) * 8 + 2 * w) * UTS + rb * 128 + (lane & 7) * 16);

    asm volatile("s_waitcnt vmcnt(%7) lgkmcnt(0)" : "+v"(r_lar), "+v"(r_aim), "+v"(r_cre), "+v"(r_cim), "+s"(tag), "+s"(t_ldt), "+s"(t_dsk) : "i"(PPAIR ? 4 : 0) : "memory");
    tag += 1;
    ST(2);
    {
      const float dt = rbf(__expf(bf2f(wm ? t_ldt >> 16 : t_ldt & 0xffffu)));
      const float er = -(rbf(__expf(bf2f(r_lar))) * dt) * 1.44269504088896f;
      const float wi = (bf2f(r_aim) * dt) * 0.159154943091895f;
      auto seed = [&](float t) {
        const float m = __builtin_amdgcn_exp2f(er * t), a = __builtin_amdgcn_fractf(wi * t);
        return f32x2{m * __builtin_amdgcn_cosf(a), m * __builtin_amdgcn_sinf(a)};
      };
      auto put8 = [&](const f32x2* v, u32 off) {
        u32x4 re = {pk2(v[0].x, v[1].x), pk2(v[2].x, v[3].x), pk2(v[4].x, v[5].x), pk2(v[6].x, v[7].x)};
        u32x4 im = {pk2(v[0].y, v[1].y), pk2(v[2].y, v[3].y), pk2(v[4].y, v[5].y), pk2(v[6].y, v[7].y)};
        *(LAS u32x4*)((LAS char*)ta + off) = re;
        *(LAS u32x4*)((LAS char*)ta + 112 + off) = im;
      };
      f32x2 v[8];
      const Mul w8 = mk(seed(8.f)), w64 = mk(seed(64.f));
      v[0] = f32x2{1.f, 0.f}; v[1] = w8.b; v[2] = cmul(v[1], w8);
      const Mul w16 = mk(v[2]);
#pragma unroll
      for (int k = 3; k < 8; k++) v[k] = cmul(v[k - 2], w16);
      put8(v, 0); SB();
      v[0] = w64.b;
#pragma unroll
      for (int r = 1; r < 4; r++) {
#pragma unroll
        for (int k = (r == 1); k < 8; k++) v[k] = cmul(v[k], w64);
        put8(v, 16 * r); SB();
      }
      f32x2 s1 = seed(1.f), s256 = seed(256.f);
      s1.y = -s1.y; s256.y = -s256.y;
      const Mul v1 = mk(s1), W = mk(s256), v2c = mk(cmul(s1, v1));
      f32x2 u[8];
      u[0] = f32x2{2.f * (bf2f(r_cre) * dt), -2.f * (bf2f(r_cim) * dt)};
      u[1] = cmul(u[0], v1);
#pragma unroll
      for (int k = 2; k < 8; k++) u[k] = cmul(u[k - 2], v2c);
      put8(u, 64); SB();
      v[0] = cmul(u[0], W); v[1] = cmul(u[2], W); v[2] = cmul(u[4], W); v[3] = cmul(u[6], W);
      v[4] = cmul(v[0], W); v[5] = cmul(v[2], W); v[6] = cmul(v[4], W); v[7] = cmul(v[5], W);
      u[0] = cmul(v[6], W); u[1] = cmul(u[0], W); u[2] = cmul(u[1], W); u[3] = cmul(u[2], W); u[4] = cmul(u[3], W);
      put8(v, 80);
      u[5] = u[6] = u[7] = f32x2{0.f, 0.f};
      put8(u, 96);
      if (PPAIR) asm volatile("s_waitcnt vmcnt(0)" ::"v"(r_x[0]), "v"(r_x[1]), "v"(r_x[2]), "v"(r_x[3]));
      ST(3);
      BAR();
      ST(14);
      f32x4 ka[2] = {{(lane == 0 && wn == 0) ? bf2f(wm ? t_dsk >> 16 : t_dsk & 0xffffu) : 0.f, 0.f, 0.f, 0.f}, {0.f, 0.f, 0.f, 0.f}};
      u32x4 fv[8], f0[8], f1[8];
#pragma unroll
      for (int ks = 0; ks < 10; ks++) {
        const u32 o = ks * 16 * 224;
        if (ks < 8) { fv[ks] = ldstr(t1, o, o + 112); f0[ks] = ldstr(t0, o + 64, o + 64 + 112); f1[ks] = ldstr(t0, o + 96, o + 96 + 112); }
        if (ks >= 2) { ka[0] = MF16(f0[ks - 2], fv[ks - 2], ka[0]); ka[1] = MF16(f1[ks - 2], fv[ks - 2], ka[1]); }
        SB();
      }
#pragma unroll
      for (int rt = 0; rt < 2; rt++)
#pragma unroll
        for (int r = 0; r < 4; r += 2) {
          const u32 pr = pk2(ka[rt][r], ka[rt][r + 1]);
          *(LAS u16*)((LAS char*)ko + (16 * rt + r) * 72) = (u16)pr;
          *(LAS u16*)((LAS char*)ko + (16 * rt + r + 1) * 72) = (u16)(pr >> 16);
        }
    }
    ST(9);
    BAR();
    ST(4);
    {
      u32 xr[8];
#pragma unroll
      for (int j = 0; j < 8; j++) xr[j] = *(const LAS u32*)((LAS char*)(ur ^ ((j >> 1) << 4)) + j * 128);
#pragma unroll
      for (int c = 0; c < 2; c++) {
        u32x4 ou;
#pragma unroll
        for (int e = 0; e < 4; e++) ou[e] = __builtin_amdgcn_perm(xr[2 * e + 1], xr[2 * e], c ? 0x07060302 : 0x05040100);
        __builtin_amdgcn_raw_buffer_store_b128(ou, sWS, uw + c * UTS, 0, 16);
      }
    }
    ST(10);
    if (!spare) BAR();
    const u32 ufo = opq(WS_UF + (pair >> 5) * 128 + (lane & 31) * 4);
    const u32 ua = opq(sb + US + wm * US_STR + (256 - 16 - 128 * wn - 96 + 16 * (lane & 15) + 8 * g4) * 2);
    u32 tb[4];
#pragma unroll
    for (int j = 0; j < 4; j++) tb[j] = opq(sb + TC + (2 * wm + (lane & 1)) * TC_STR + (2046 - 16 - 128 * wn - 96 - 1792 + 8 * g4 - (lane & 14)) * 2 + 1024 * j);
    const u32 row = tid >> 3, ck = lane & 7;
    const u32 ro = opq(sb + RED + w * 6144 + l16), r1 = opq(sb + RED + (w ^ 1) * 6144 + l16), r2 = opq(sb + RED + (w ^ 2) * 6144 + l16), r3 = opq(sb + RED + (w ^ 3) * 6144 + l16);
    const u32 ao = opq(WS_AT + pair * ATS + (8 * w + g4) * 512 + (lane & 15) * 16);
    const u32 sa = opq(sb + 2 * SLOT + ck * 1024 + ((row ^ (2 * ck)) << 4));
    const u32 ato = opq(WS_AT + (32 * cb2 + 8 * w + hh) * ATS + rb * 512 + (lane & 31) * 16);
    const u32 go = opq(WS_G + rb * 512 + row * 16);
    const u32 oo = opq((64 * rb + row) * 1024 + cb2 * 128 + ck * 16);
    const u32 cw = opq(sb + CNT + 4 * w), cr = opq(sb + CNT + 4 * (lane & 3));
    const u32 aw = opq(sb + TC + (lane & 31) * 144 + (8 * w + hh) * 4), ar = opq(sb + TC + row * 144 + ck * 16);
    auto arrive = [&](u32 n) { *(volatile LAS u32*)cw = n; };
    auto meet = [&](u32 n) { LGKM0(); arrive(n); lds_wait(cr, n); };
    auto pub_u = [&]() { if (tid == 0) asm volatile("buffer_store_dword %0, %1, %2, 0 offen sc1" ::"v"(tag), "v"(WS_UF + (rot & 7) * 128 + rb * 4), "s"(sWS) : "memory"); };
    if (!spare) {
      for (int i = 2; i < UPUB; i++) BAR();
      VMCNT(0);
      BAR();
      pub_u();
      ST(5);
    } else {
      u32x4 gwv, xr[16];
      asm volatile("global_load_dwordx4 %0, %1, %2" : "=v"(gwv) : "v"(l16), "s"(p_gw));
#pragma unroll
      for (int j = 0; j < 16; j++)
        asm volatile("buffer_load_dwordx4 %0, %1, %2, %3 offen offset:%4" : "=v"(xr[j]) : "v"(l16), "s"(k0.s0123), "s"((64 * rb + 16 * w + (j & ~3)) * 1024), "i"((j & 3) * 1024));
      arrive(1);
      asm volatile("s_waitcnt vmcnt(0)" : "+v"(gwv), "+v"(xr[0]), "+v"(xr[1]), "+v"(xr[2]), "+v"(xr[3]), "+v"(xr[4]), "+v"(xr[5]), "+v"(xr[6]), "+v"(xr[7]),
                   "+v"(xr[8]), "+v"(xr[9]), "+v"(xr[10]), "+v"(xr[11]), "+v"(xr[12]), "+v"(xr[13]), "+v"(xr[14]), "+v"(xr[15])::"memory");
      meet(2);
      pub_u();
      ST(5);
      const u32 zs = sb + TC + w * TC_STR;
#pragma unroll
      for (int j = 0; j < 16; j++) {
        float z = 0.f;
#pragma unroll
        for (int e = 0; e < 4; e++) z = dot2(xr[j][e], gwv[e], z);
        *(LAS float*)((LAS char*)(zs + lane * 4) + j * 260) = z;
      }
      LGKM0();
      float zz = 0.f;
#pragma unroll
      for (int k = 0; k < 16; k++) zz += *(const LAS float*)((LAS char*)(zs + (lane & 15) * 260 + g4 * 64) + k * 4);
      zz += __shfl_xor(zz, 16); zz += __shfl_xor(zz, 32);
      const float g = rbf(1.f / (1.f + __expf(-zz)));
      if (lane < 16) __builtin_amdgcn_raw_buffer_store_b64(u32x2{pk2(g, 1.f - g), tag}, sWS, WS_G + rb * 512 + (wn * 16 + lane) * 16 + wm * 8, 0, 16);
      meet(3);
    }
    if (w < 2) *(LAS u32x4*)(w == 0 ? sb + US + hh * US_STR + (lane & 31) * 16 : sb + TC + ((lane >> 1) & 3) * TC_STR + 4096 + (lane & 1) * 16) = u32x4{0, 0, 0, 0};
    auto fetch_u = [&]() {
#pragma unroll
      for (int q = 0; q < 8; q++) {
        if (!(q & 3)) SETM0(sb, US + 512 + (q >> 2) * US_STR);
        DMAP(" sc1", l16, sWS, WS_UT + (2 * pair + (q >> 2)) * UTS, q & 3);
      }
    };
    u32 ek[2][9];
    auto exp_rd = [&]() {
#pragma unroll
      for (int i = 0; i < 9; i++) { ek[0][i] = *(const LAS u16*)ga[i]; ek[1][i] = *(const LAS u16*)((LAS char*)ga[i] + KD_CH); SB(); asm volatile("s_nop 3"); SB(); }
    };
    auto exp_wr = [&](int i) {
      const u32* e = ek[i >> 1] + (i & 1);
      *(LAS u32x4*)((LAS char*)to + i * TC_STR) = u32x4{e[7] | (e[6] << 16), e[5] | (e[4] << 16), e[3] | (e[2] << 16), e[1] | (e[0] << 16)};
    };
    bool got = w != 0;
    if (!spare) {
      if (EXPE) exp_rd();
      BAR();
      if (EXPE) { exp_wr(0); SB(); __builtin_amdgcn_s_sleep(2); SB(); exp_wr(1); }
      BAR();
#pragma unroll
      for (int i = 0; i < 2; i++) {
        if (EXPE) exp_wr(2 + i);
        if (got || i < UTRY) {
          BAR(); BAR();
        } else {
          u32 uf;
          asm volatile("buffer_load_dword %0, %1, %2, 0 offen sc1\n\ts_barrier\n\ts_barrier\n\ts_waitcnt vmcnt(0)" : "=&v"(uf) : "v"(ufo), "s"(sWS) : "memory");
          got = __all(uf == tag);
          if (got) fetch_u();
        }
      }
    }
    __builtin_amdgcn_s_setprio(CVPRIO);
    ST(21);
    if (!EXPE || spare) { exp_rd(); exp_wr(0); exp_wr(1); exp_wr(2); exp_wr(3); }
    if (!got) {
      __builtin_amdgcn_s_setprio(3);
      _Pragma("unroll 1") for (u32 spin = 0; spin < SPIN_CAP; spin++) {
        u32 uf;
        asm volatile("buffer_load_dword %0, %1, %2, 0 offen sc1\n\ts_waitcnt vmcnt(0)" : "=&v"(uf) : "v"(ufo), "s"(sWS) : "memory");
        if (__all(uf == tag)) break;
        __builtin_amdgcn_s_sleep(USLEEP);
      }
      fetch_u();
      *(volatile LAS u32*)(sb + GO + 4) = 1;
      __builtin_amdgcn_s_setprio(CVPRIO);
    }
    VMCNT(0);
    ST(6);
    meet(4);
    ST(7);

    f32x4 ca[8];
#pragma unroll
    for (int c = 0; c < 8; c++) ca[c] = f32x4{0.f, 0.f, 0.f, 0.f};
    u32x4 fu[2][8];
    u32x2 ft[2][16];
    auto cread = [&](int f, int r) {
      const int k = r < 2 ? 8 + r : r < 10 ? r - 2 : r;
      if (k < 8) fu[f & 1][k] = lds128(ua, (96 - 32 * f + 256 * k) * 2);
      else {
        const int i = (k - 8) >> 1, hl = k & 1, e = 7 - i;
        const LAS u32* t = (const LAS u32*)((LAS char*)tb[e >> 1] + (96 - 32 * f + 256 * (e & 1)) * 2 + 8 * hl);
        ft[f & 1][2 * i + hl] = u32x2{t[0], t[1]};
      }
    };
#pragma unroll
    for (int r = 0; r < 24; r++) { cread(0, r); SB(); }
#pragma unroll
    for (int f = 0; f < 4; f++) {
      int m = 0;
#pragma unroll
      for (int i = 0; i < 8; i++)
#pragma unroll
        for (int e = 0; e + i < 8; e++, m++) {
          const u32x4 tf = {ft[f & 1][2 * i][0], ft[f & 1][2 * i][1], ft[f & 1][2 * i + 1][0], ft[f & 1][2 * i + 1][1]};
          ca[i + e] = MF16(fu[f & 1][e], tf, ca[i + e]);
          if (f < 3 && m >= 4 && m < 28) cread(f + 1, m - 4);
          if (f == 3 && m == 0) arrive(5);
          SB();
        }
    }
    ST(8);
    f32x4 keep[2];
    __builtin_amdgcn_s_setprio(3);
    lds_wait(cr, 5);
#define SEND(W) { _Pragma("unroll") for (int j = 1; j < 4; j++) { *(LAS f32x4*)((LAS char*)ro + (j - 1) * 2048) = ca[2 * (W ^ j)]; *(LAS f32x4*)((LAS char*)ro + (j - 1) * 2048 + 1024) = ca[2 * (W ^ j) + 1]; } \
                  keep[0] = ca[2 * W]; keep[1] = ca[2 * W + 1]; asm volatile("; send " #W ::: "memory"); }
    if (w == 0) SEND(0) else if (w == 1) SEND(1) else if (w == 2) SEND(2) else SEND(3)
    asm volatile("" ::: "memory");
    arrive(6);
    lds_wait(cr, 6);
    const u32 sel = wm ? 0x01000302 : 0x03020100;
    f32x4 pp[2][3];
#pragma unroll
    for (int c = 0; c < 2; c++) {
      pp[c][0] = *(const LAS f32x4*)((LAS char*)r1 + c * 1024);
      pp[c][1] = *(const LAS f32x4*)((LAS char*)r2 + 2048 + c * 1024);
      pp[c][2] = *(const LAS f32x4*)((LAS char*)r3 + 4096 + c * 1024);
    }
#pragma unroll
    for (int c = 0; c < 2; c++) {
      u32 o[4];
#pragma unroll
      for (int r = 0; r < 4; r++) {
        const u32 v = pk2(add1(keep[c][r], pp[c][0][r]), add1(pp[c][1][r], pp[c][2][r]));
        o[r] = __builtin_amdgcn_perm(v, v, sel);
      }
#pragma unroll
      for (int r = 0; r < 2; r++) __builtin_amdgcn_raw_buffer_store_b128(u32x4{o[r], tag, o[r + 2], tag}, sWS, ao + c * 2048 + r * 256, 0, 16);
    }
    arrive(7);
    ST(11);
    const __amdgpu_buffer_rsrc_t sB2 = __builtin_amdgcn_make_buffer_rsrc((void*)mk64(k1[14], k1[15]), 0, D * 2, 0x00020000);
    const __amdgpu_buffer_rsrc_t sOut = __builtin_amdgcn_make_buffer_rsrc((void*)mk64(k0[14], k0[15]), 0, L * D * 2, 0x00020000);
    const u32x4 b2v = __builtin_amdgcn_raw_buffer_load_b128(sB2, ck * 16, cb2 * 128, 0);
    lds_wait<LSL>(cr + RDY - CNT, 1);
    ST(12);
    u32x4 gg, at[4];
    _Pragma("unroll 1") for (u32 spin = 0; spin < SPIN_CAP; spin++) {
      asm volatile("" ::: "memory");
#pragma unroll
      for (int j = 0; j < 4; j++) at[j] = __builtin_amdgcn_raw_buffer_load_b128(sWS, ato, 2 * j * ATS, 16);
      gg = __builtin_amdgcn_raw_buffer_load_b128(sWS, go, 0, 16);
      bool ok = gg[1] == tag && gg[3] == tag;
#pragma unroll
      for (int j = 0; j < 4; j++) ok &= at[j][1] == tag && at[j][3] == tag;
      if (__all(ok)) break;
      __builtin_amdgcn_s_sleep(RSLEEP);
    }
    __builtin_amdgcn_s_setprio(TPRIO);
    ST(13);
    lds_wait(cr, 7);
#pragma unroll
    for (int j = 0; j < 4; j++) {
      *(LAS u32*)((LAS char*)aw + j * 8) = at[j][0];
      *(LAS u32*)((LAS char*)aw + j * 8 + 32 * 144) = at[j][2];
    }
    asm volatile("" ::: "memory");
    arrive(8);
    lds_wait(cr, 8);
    ST(15);
#pragma unroll
    for (int t = 0; t < 2; t++) {
      const float g = bf2f(gg[2 * t] & 0xffffu), gm = hi2f(gg[2 * t]);
      const u32x4 av = lds128(ar, t * 32 * 144);
      float base[8];
#pragma unroll
      for (int i = 0; i < 4; i++) {
        base[2 * i] = fopq(fma1(gm, bf2f(b2v[i] & 0xffffu), mul1(g, bf2f(av[i] & 0xffffu))));
        base[2 * i + 1] = fopq(fma1(gm, hi2f(b2v[i]), mul1(g, hi2f(av[i]))));
      }
      if (t == 0) { ST(16); lds_wait<LSL>(cr + RDY - CNT, 2); } else { ST(18); asm volatile("s_barrier" ::: "memory"); ST(19); }
      f32x4 pt[4][2];
#pragma unroll
      for (int k = 0; k < 4; k++)
#pragma unroll
        for (int q = 0; q < 2; q++) pt[k][q] = *(const LAS f32x4*)((LAS char*)sa + (1 - t) * SLOT + k * 8192 + q * 512);
      u32x4 ow;
#pragma unroll
      for (int i = 0; i < 4; i++) {
        float o[2];
#pragma unroll
        for (int e = 0; e < 2; e++) {
          const int q = i >> 1, r = 2 * (i & 1) + e;
          o[e] = t ? __builtin_fmaf(gm, (pt[0][q][r] + pt[1][q][r]) + (pt[2][q][r] + pt[3][q][r]), base[2 * i + e])
                   : fma1(gm, add1(add1(pt[0][q][r], pt[1][q][r]), add1(pt[2][q][r], pt[3][q][r])), base[2 * i + e]);
        }
        ow[i] = pk2(o[0], o[1]);
      }
      __builtin_amdgcn_raw_buffer_store_b128(ow, sOut, oo + t * 32768, 0, OUTPOL);
      SB();
      if (t == 0) ST(17);
    }
    asm volatile(".Lcode_end:");
    STAMP_OUT();
    return;
  }

  u32x16 k0;
  u32x4 k1;
  asm volatile("s_load_dwordx16 %0, %2, 0\n\ts_load_dwordx4 %1, %2, 112"
               : "=&s"(k0), "=&s"(k1) : "s"(__builtin_amdgcn_kernarg_segment_ptr()));
  SB();
  const u32 o1 = opq(((spare ? 0 : cb1) * 128 + wn * 64 + (lane & 31)) * 2);
  u32 vA[4], vB[4], v2[4];
#pragma unroll
  for (int q = 0; q < 4; q++) {
    vA[q] = opq((lane >> 3) * 1024 + (((lane & 7) ^ g4 ^ (4 * (q & 1))) << 4) + q * (8192 - 1024));
    vB[q] = opq(g4 * (F * 2) + (((lane & 15) ^ (g4 << 2) ^ q) << 4) + q * (4 * F * 2 - 1024));
    v2[q] = opq((lane >> 3) * 1024 + (((lane & 7) ^ (blk << 2)) << 4) + q * (8192 - 1024));
  }
  const u32 xbase = sopq((128 * p + 32 * w) * 1024), wbase = sopq(16 * w * (F * 2) + cb1 * 256);
  const u32 m4 = sopq(sb + w * 4096), m8 = sopq(sb + w * 8192);
  asm volatile("s_waitcnt lgkmcnt(0)" : "+s"(k0), "+s"(k1) : "s"(d0), "s"(d1), "s"(d2));
  ST(14);
  const u32x4 sX = k0.s0123, sW1 = k0.s4567;
  const u32x4 sW2 = k0.s89ab;
  char* const ws = (char*)mk64(k0[12], k0[13]);
  const u32 w2base = 28 * w * 16384 + colblock(jj, sW2[0]) * 128;
  u32 tag;
  const __amdgpu_buffer_rsrc_t sWS = __builtin_amdgcn_make_buffer_rsrc((void*)ws, 0, WS_END, 0x00020000);
  auto w2piece = [&](int c, int q) {
    if ((q & 3) == 0) SETM0(m8, (c & 3) * SLOT + (q >> 2) * 4096);
    DMAQ(v2[q & 3], sW2, w2base + c * 65536 + (q >> 2) * 32768, q & 3);
  };

  if (!spare) {
    auto g1piece = [&](int g, int q) {
      if (q == 0) SETM0(m4, (g & 3) * SLOT);
      if (q == 4) SETM0(m4, (g & 3) * SLOT + 16384);
      if (q < 4) DMAP(XPOL, vA[q], sX, xbase + ((g + rot) & 7) * 128, q);
      else DMAQ(vB[q - 4], sW1, wbase + ((g + rot) & 7) * 64 * (F * 2), q - 4);
    };
    u32 r_b1a, r_b1b;
    asm volatile("s_load_dword %0, %1, 0" : "=s"(tag) : "s"(ws));
    asm volatile("global_load_ushort %0, %1, %2" : "=v"(r_b1a) : "v"(o1), "s"(mk64(k1[0], k1[1])));
    asm volatile("global_load_ushort %0, %1, %2 offset:64" : "=v"(r_b1b) : "v"(o1), "s"(mk64(k1[0], k1[1])));
#pragma unroll
    for (int i = 0; i < 12; i++) g1piece(i >> 3, i < 8 ? i : i - 4);
    ST(1);
    u32 ax[2][4], aw[2][4];
    {
      const u32 xa = sb + (64 * wm + (lane & 31)) * 128 + ((hh ^ ((lane >> 1) & 7)) << 4);
      const u32 wa = sb + 16384 + 256 * (8 * hh + q4) + 8 * (p4 & 1) + (((8 * wn + 2 * blk + (p4 >> 1)) ^ ((q4 << 2) | (2 * hh))) << 4);
#pragma unroll
      for (int k = 0; k < 4; k++) {
        ax[0][k] = opq(xa ^ (k << 5)); aw[0][k] = opq(wa ^ ((k & 1) << 4) ^ ((k >> 1) << 6));
        ax[1][k] = opq(ax[0][k] + 2 * SLOT); aw[1][k] = opq(aw[0][k] + 2 * SLOT);
      }
    }
    const u32 ho = opq(l16 + WS_H + ((4 * p + 2 * wm) * 112 + cb1 * 8 + wn * 4) * 1024), ho1 = opq(ho + 112 * 1024);
    asm volatile("s_barrier" ::: "memory");
    ST(3);
#pragma unroll
    for (int q = 0; q < 8 * LA - 12; q++) g1piece(q < 4 ? 1 : 2, q < 4 ? q : q - 4);
    asm volatile("s_waitcnt vmcnt(%3) lgkmcnt(0)" : "+v"(r_b1a), "+v"(r_b1b), "+s"(tag) : "i"(8 * LA));
    f32x16 acc[2][2];
    {
      const u32x4 one = {hh ? 0u : 0x3f80u, 0u, 0u, 0u};
      const u32x4 ba = {r_b1a, 0u, 0u, 0u}, bb = {r_b1b, 0u, 0u, 0u};
      asm volatile("s_nop 1\n\tv_mfma_f32_32x32x16_bf16 %0, %4, %6, 0\n\tv_mfma_f32_32x32x16_bf16 %1, %4, %6, 0\n\t"
                   "v_mfma_f32_32x32x16_bf16 %2, %5, %6, 0\n\tv_mfma_f32_32x32x16_bf16 %3, %5, %6, 0"
                   : "=&v"(acc[0][0]), "=&v"(acc[0][1]), "=&v"(acc[1][0]), "=&v"(acc[1][1]) : "v"(ba), "v"(bb), "v"(one));
    }
    u32x4 fx[2][2];
    s16x4 fw[2][4];
    VMCNT(8 * LA - 8);
    ST(2);
    BAR();
    ST(4);
    fx[0][0] = lds128(ax[0][0]); fw[0][0] = tr64(aw[0][0], 0); fw[0][1] = tr64(aw[0][1], 1024);
    fx[0][1] = lds128(ax[0][0], 4096); fw[0][2] = tr64(aw[0][2], 0); fw[0][3] = tr64(aw[0][3], 1024);
#pragma unroll
    for (int s = 0; s < 8; s++) {
      auto piece = [&](int q) { if (s + LA < 8) g1piece(s + LA, q); else if (s + LA - 8 < WPRE) w2piece(s + LA - 8, q); };
#pragma unroll
      for (int k = 0; k < 4; k++) {
        const int c = k & 1, n = c ^ 1, kn = (k + 1) & 3, sl = (k == 3 ? s + 1 : s) & 3, hi = sl >> 1;
        const u32 o = (sl & 1) * SLOT, ow = o + kn * 4096;
        const bool more = s < 7 || k < 3;
        MMV(acc[0][0], cat(fw[c][0], fw[c][1]), fx[c][0]);
        if (k == 3) {
          if (s < 7) VMCNT((LA == 3 && s + 2 < 8 + WPRE ? 8 : 0) + (s + LA < 8 + WPRE ? 6 : 0));
          BAR();
        }
        if (more) { fx[n][0] = lds128(ax[hi][kn], o); fw[n][0] = tr64(aw[hi][0], ow); fw[n][1] = tr64(aw[hi][1], ow + 1024); }
        MMV(acc[0][1], cat(fw[c][0], fw[c][1]), fx[c][1]);
        if (more) { fx[n][1] = lds128(ax[hi][kn], o + 4096); fw[n][2] = tr64(aw[hi][2], ow); }
        piece(2 * k);
        MMV(acc[1][0], cat(fw[c][2], fw[c][3]), fx[c][0]);
        if (more) fw[n][3] = tr64(aw[hi][3], ow + 1024);
        MMV(acc[1][1], cat(fw[c][2], fw[c][3]), fx[c][1]);
        piece(2 * k + 1);
      }
      if (s == 0) ST(8);
      if (s == 1) ST(15);
      if (s == 2) ST(16);
      if (s == 3) ST(17);
      if (s == 4) ST(19);
      if (s == 5) ST(20);
      if (s == 6) ST(21);
    }
    asm volatile("s_nop 7\n\ts_nop 7" : "+v"(acc[0][0]), "+v"(acc[0][1]), "+v"(acc[1][0]), "+v"(acc[1][1]));
    __builtin_amdgcn_s_setprio(2);
    ST(5);
#pragma unroll
    for (int u = 0; u < 2; u++)
#pragma unroll
      for (int t = 0; t < 2; t++)
#pragma unroll
        for (int s = 0; s < 2; s++) {
          u32x4 o;
#pragma unroll
          for (int e = 0; e < 4; e++) {
            const float a0 = relu(acc[u][t][8 * s + 2 * e]), a1 = relu(acc[u][t][8 * s + 2 * e + 1]);
            o[e] = EPK ? pk2(a0 * a0, a1 * a1) : pk2(mul1(a0, a0), mul1(a1, a1));
          }
          __builtin_amdgcn_raw_buffer_store_b128(o, sWS, (t ? ho1 : ho) + (2 * u + s) * 1024, 0, 16);
          SB();
        }
    tag += 1;
  } else {
    asm volatile("s_load_dword %0, %1, 0" : "=s"(tag) : "s"(ws));
    SETM0(m4, 0);
#pragma unroll
    for (int q = 0; q < 4; q++) DMAP(XPOL, vA[q], sX, xbase + (rot & 7) * 128, q);
    SETM0(m8, SLOT);
    auto pull = [&](u32 n) {
      const u32 u = n < 4 ? min(14 * xcd + 4 * n + (bi >> 3), 111u) : 28 * (bi >> 3) + 4 * PFC + n - 4;
#pragma unroll
      for (int q = 0; q < 4; q++) DMAQ(l16, sW2, u * 16384 + w * 4096, q);
    };
    __builtin_amdgcn_s_sleep(PFDELAY);
    pull(0); pull(1); pull(2);
    asm volatile("s_waitcnt vmcnt(12) lgkmcnt(0)\n\ts_barrier\n\ts_barrier" : "+s"(tag)::"memory");
    tag += 1;
    ST(2);
    lds_wait(sb + CNT + 4 * (lane & 3), 1);
    ST(8);
    _Pragma("unroll 1") for (u32 n = 3; n < NPF; n++) pull(n);
    ST(4);
#pragma unroll
    for (int i = 0; i < 8 * WPRE; i++) w2piece(i >> 3, i & 7);
    lds_wait(sb + GO + 4, 1);
  }
  SB();
  ST(6);

  u32 w2a[2][2];
  {
    const u32 a = m8 + 128 * (4 * hh + q4) + 8 * (p4 & 1) + (((2 * blk + (p4 >> 1)) ^ ((q4 >> 1) << 2)) << 4);
    w2a[0][0] = opq(a); w2a[1][0] = opq(a ^ 64); w2a[0][1] = opq(a + 2 * SLOT); w2a[1][1] = opq((a ^ 64) + 2 * SLOT);
  }
  auto w2frag = [&](int st, int v) {
    const int sl = (st >> 2) & 3;
    const u32 o = (sl & 1) * SLOT + (st & 3) * 2048;
    return ldstr(w2a[v][sl >> 1], o, o + 1024);
  };
  u32x4 w2r[12][2];
  u32 fl = 0;
  const u32 fo = opq(WS_HF + p * 128 + (((lane & 7) == 7) ? 0 : (lane & 15) * 4));
#pragma unroll
  for (int c = WPRE; c < 8; c++) {
    if (c == WPRE + PUBAT) {
      VMCNT(8 * PUBAT);
      *(volatile LAS u32*)(sb + FD + 4 * w) = 1;
      if (w == 0 && !spare) {
        lds_wait(sb + FD + 4 * (lane & 3), 1);
        if (lane == 0) asm volatile("buffer_store_dword %0, %1, %2, 0 offen sc1" ::"v"(tag), "v"(WS_HF + p * 128 + (8 * hf + jj) * 4), "s"(sWS) : "memory");
      }
      ST(7);
      __builtin_amdgcn_s_sleep(WDELAY);
    }
    if (c >= 4 && c < 7) {
      VMCNT(24);
#pragma unroll
      for (int j = 0; j < 8; j++) w2r[4 * (c - 4) + (j >> 1)][j & 1] = w2frag(4 * (c - 4) + (j >> 1), j & 1);
      LGKM0();
    }
    if (c == POLLAT) asm volatile("s_cmp_lg_u32 %3, 3\n\ts_cbranch_scc1 1f\n\tbuffer_load_dword %0, %1, %2, 0 offen sc1\n1:" : "+v"(fl) : "v"(fo), "s"(sWS), "s"(w) : "memory", "scc");
    if (c < 7) {
#pragma unroll
      for (int q = 0; q < 8; q++) w2piece(c, q);
    }
  }
  const u32 hb = sopq(WS_H + (2 * rb * 112 + 28 * w) * 1024);
  const u32 xw = opq(m8 + 2 * SLOT + l16), rdw = opq(sb + RDY + 4 * w);
  asm volatile("s_waitcnt vmcnt(%1)" : "+v"(fl) : "i"(8 * (7 - POLLAT)) : "memory");
  ST(11);
  if (w == 3) {
    _Pragma("unroll 1") for (u32 spin = 0; spin < SPIN_CAP; spin++) {
      if (__all(fl == tag)) break;
      __builtin_amdgcn_s_sleep(HSLEEP);
      asm volatile("buffer_load_dword %0, %1, %2, 0 offen sc1\n\ts_waitcnt vmcnt(0)" : "=&v"(fl) : "v"(fo), "s"(sWS) : "memory");
    }
    *(volatile LAS u32*)(sb + GO) = 1;
  } else {
    lds_wait(sb + GO, 1);
  }
  __builtin_amdgcn_s_setprio(G2PRIO);
  ST(10);
  SB();

  u32x4 hfr[56];
  auto load_h = [&](int i) {
    const int st = step(i);
    hfr[i] = __builtin_amdgcn_raw_buffer_load_b128(sWS, l16 + (st & 3) * 1024, hb + ((i / 28) * 112 + (st & ~3)) * 1024, 16);
  };
#pragma unroll
  for (int i = 0; i < HEAD; i++) load_h(i);
  SB();
  f32x16 oc[2][2] = {};
  auto send = [&](int t, int j) {
    const f32x16& s = oc[t][j >> 2];
    const int r = 4 * (j & 3);
    *(LAS f32x4*)((LAS char*)(xw ^ (j << 5)) + (1 - t) * SLOT + j * 1024) = f32x4{s[r], s[r + 1], s[r + 2], s[r + 3]};
  };
  auto signal = [&](u32 n) { asm volatile("" ::: "memory"); *(volatile LAS u32*)rdw = n; };
  u32x4 wl[56][2];
  auto fetch_w = [&](int i) { if (i < 56 && step(i) >= 12) { wl[i][0] = w2frag(step(i), 0); wl[i][1] = w2frag(step(i), 1); } };
  sfor<0, 56>([&](auto ic) {
    constexpr int i = decltype(ic)::v, t = i / 28, st = step(i);
    constexpr int D2 = 53 - TAILN;
    if (i < D2 || !TAILN) fetch_w(i + 1); else { fetch_w(2 * i - D2 + 1); fetch_w(2 * i - D2 + 2); }
    SB();
    u32x4 a0, a1;
    if (st < 12) { a0 = w2r[st < 12 ? st : 0][0]; a1 = w2r[st < 12 ? st : 0][1]; } else { a0 = wl[i][0]; a1 = wl[i][1]; }
    oc[t][0] = MF32(a0, hfr[i], oc[t][0]);
    if (i + HEAD < 56) load_h(i + HEAD);
    SB();
    if (i < 56 - TAILN) oc[t][1] = MF32(a1, hfr[i], oc[t][1]);
    if (i == ATAT) signal(1);
    if (i >= 31 && i < 39) send(0, i - 31);
    if (i == 38) signal(2);
    SB();
    if (i == 27) ST(18);
  });
#pragma unroll
  for (int i = 0; i < TAILN; i++) {
    oc[1][1] = MF32(wl[56 - TAILN + i][1], hfr[56 - TAILN + i], oc[1][1]);
    SB();
    if (i >= 1 && i < 3) { send(1, 2 * i - 2); send(1, 2 * i - 1); }
    SB();
  }
  ST(12);
#pragma unroll
  for (int j = TAILN >= 3 ? 4 : 0; j < 8; j++) send(1, j);
  BAR();
  if (b == 0 && w == 3) {
    _Pragma("unroll 1") for (u32 spin = 0; spin < SPIN_CAP; spin++) {
      asm volatile("" ::: "memory");
      const u32x4 f0 = __builtin_amdgcn_raw_buffer_load_b128(sWS, WS_UF + l16, 0, 16);
      if (__all(f0[0] == tag && f0[1] == tag && f0[2] == tag && f0[3] == tag)) break;
    }
    if (lane == 0) __builtin_amdgcn_raw_buffer_store_b32(tag, sWS, 0, 0, 16);
  }
  STAMP_OUT();
}

extern "C" void kernel_launch(void* const* d_in, const int* in_sizes, int n_in, void* d_out, int out_size,
                              void* d_ws, size_t ws_size, hipStream_t stream) {
  Args a;
  a.x = Srd{d_in[0], L * D * 2, 0x00020000}; a.w1 = Srd{d_in[8], D * F * 2, 0x00020000}; a.w2 = Srd{d_in[10], D * F * 2, 0x00020000};
  a.ws = (char*)d_ws + WSO; a.out = (u16*)d_out;
  a.lar = (const u16*)d_in[1]; a.aim = (const u16*)d_in[2]; a.cre = (const u16*)d_in[3]; a.cim = (const u16*)d_in[4];
  a.ldt = (const u16*)d_in[5]; a.dsk = (const u16*)d_in[6]; a.b1 = (const u16*)d_in[9]; a.b2 = (const u16*)d_in[11];
  a.gw = (const u16*)d_in[12];
  fused_mix<<<256, 512, 0, stream>>>(a);
}
```

```cpp
#include <hip/hip_runtime.h>
#pragma clang diagnostic ignored "-Wint-to-pointer-cast"

typedef unsigned short u16;
typedef unsigned u32;
typedef unsigned long long u64;
typedef __bf16 h16x8 __attribute__((ext_vector_type(8)));
typedef __bf16 h16x2 __attribute__((ext_vector_type(2)));
typedef short s16x4 __attribute__((ext_vector_type(4)));
typedef short s16x8 __attribute__((ext_vector_type(8)));
typedef short s16x2 __attribute__((ext_vector_type(2)));
typedef float f32x2 __attribute__((ext_vector_type(2)));
typedef float f32x4 __attribute__((ext_vector_type(4)));
typedef float f32x16 __attribute__((ext_vector_type(16)));
typedef u32 u32x2 __attribute__((ext_vector_type(2)));
typedef u32 u32x4 __attribute__((ext_vector_type(4)));
typedef u32 u32x16 __attribute__((ext_vector_type(16)));
#define LAS __attribute__((address_space(3)))
#define CAS __attribute__((address_space(4)))
#define GAS __attribute__((address_space(1)))
#define DI __device__ __forceinline__

constexpr int L = 2048, D = 512, F = 1792;
constexpr u32 WS_UF = 8192;
constexpr u32 WS_HF = 4096;
constexpr u32 WS_G = 1u << 19;
#define UTS 4096
#define ATS 16384
#define WSO 0
constexpr u32 WS_UT = 1u << 20;
constexpr u32 WS_AT = 4u << 20;
constexpr u32 WS_H = 9u << 20;
constexpr u32 WS_END = 16u << 20;
constexpr u32 WS_STAMP = 32u << 20;
constexpr u32 SLOT = 32768;
constexpr u32 TAB = 3 * SLOT;
constexpr u32 TC = 4 * SLOT;
constexpr u32 TC_STR = 4160;
constexpr u32 US = TC + 4 * TC_STR;
constexpr u32 US_STR = (256 + 2048) * 2;
constexpr u32 KD_CH = 32 * 36 * 2;
constexpr u32 LDS_BYTES = 163840;
constexpr u32 KD = LDS_BYTES - 2 * KD_CH;
constexpr u32 KD_ZERO = 34 * 2;
constexpr u32 CNT = KD - 64;
constexpr u32 FD = CNT + 16, RDY = CNT + 32, GO = CNT + 48;
constexpr u32 RED = TC;
static_assert(US + 2 * US_STR <= CNT && TAB + 256 * 224 + 16 <= CNT, "lds map");
constexpr u32 SPIN_CAP = 1u << 16;
#define EPK 0
#define WDELAY 0
#define CBO 4
#define TAILN 4
#define EXPE 0
#define LSL 4
#define TPRIO 3
#define UTRY 1
#define HEAD 12
#define WPRE 3
#define PUBAT 0
#define PPOL " nt"
#define PPOL2 ""
#define XPOL ""
#define NPF 4
#define PFDELAY 24
#define POLLAT 6
#define PFC 0
#define LA 3
#define OUTPOL 16
#define UPUB 2
#define RSLEEP 8
#define USLEEP 8
#define HSLEEP 2
#define PPAIR 4
#define ATAT 16
#define G2PRIO 0
#define CVPRIO 1

DI float bf2f(u32 v) { return __uint_as_float(v << 16); }
DI u32 pk2(float lo, float hi) { f32x2 a = {lo, hi}; return __builtin_bit_cast(u32, __builtin_convertvector(a, h16x2)); }
DI float rbf(float x) { return bf2f(pk2(x, x) & 0xffffu); }
DI float dot2(u32 a, u32 b, float c) { return __builtin_amdgcn_fdot2_f32_bf16(__builtin_bit_cast(h16x2, a), __builtin_bit_cast(h16x2, b), c, false); }
DI u32 rfl(u32 x) { return __builtin_amdgcn_readfirstlane(x); }
struct Mul { f32x2 b, n; };
DI Mul mk(f32x2 b) { return Mul{b, f32x2{-b.y, b.y}}; }
DI f32x2 cmul(f32x2 a, Mul m) { return __builtin_elementwise_fma(a.yx, m.n, a * m.b.xx); }
DI float mul1(float a, float b) { float r; asm("v_mul_f32 %0, %1, %2" : "=v"(r) : "v"(a), "v"(b)); return r; }
DI float add1(float a, float b) { float r; asm("v_add_f32 %0, %1, %2" : "=v"(r) : "v"(a), "v"(b)); return r; }
DI float fma1(float a, float b, float c) { float r; asm("v_fma_f32 %0, %1, %2, %3" : "=v"(r) : "v"(a), "v"(b), "v"(c)); return r; }
DI float hi2f(u32 v) { return __uint_as_float(v & 0xffff0000u); }
DI float fopq(float x) { asm volatile("" : "+v"(x)); return x; }
DI float relu(float x) { return __int_as_float(max(__float_as_int(x), 0)); }
DI u32 sopq(u32 x) { asm volatile("" : "+s"(x)); return x; }
DI u32 opq(u32 x) { asm volatile("" : "+v"(x)); return x; }
#define MF32(a, b, c) __builtin_amdgcn_mfma_f32_32x32x16_bf16(__builtin_bit_cast(h16x8, a), __builtin_bit_cast(h16x8, b), c, 0, 0, 0)
#define MF16(a, b, c) __builtin_amdgcn_mfma_f32_16x16x32_bf16(__builtin_bit_cast(h16x8, a), __builtin_bit_cast(h16x8, b), c, 0, 0, 0)
#define DMA(voff, srd, soff, dst) \
  asm volatile("s_mov_b32 m0, %0\n\ts_nop 0\n\tbuffer_load_dwordx4 %1, %2, %3 offen lds" ::"s"(dst), "v"(voff), "s"(srd), "s"(soff) : "memory")
#define SETM0(base, off) asm volatile("s_add_u32 m0, %0, %1\n\ts_nop 0" ::"s"(base), "i"(off) : "memory", "scc")
#define DMAP(pol, voff, srd, soff, q) asm volatile("buffer_load_dwordx4 %0, %1, %2 offen offset:%3" pol " lds" ::"v"(voff), "s"(srd), "s"(soff), "i"((q) * 1024) : "memory")
#define DMAQ(voff, srd, soff, q) DMAP("", voff, srd, soff, q)
#define MMV(acc, a, b) asm volatile("v_mfma_f32_32x32x16_bf16 %0, %1, %2, %0" : "+v"(acc) : "v"(a), "v"(b) : "memory")
#define VMCNT(n) asm volatile("s_waitcnt vmcnt(%0)" ::"i"(n) : "memory")
#define LGKM0() asm volatile("s_waitcnt lgkmcnt(0)" ::: "memory")
#define BAR() asm volatile("s_waitcnt lgkmcnt(0)\n\ts_barrier" ::: "memory")
#define SB() __builtin_amdgcn_sched_barrier(0)
#define ST(i)

template <int SL = 1> DI void lds_wait(u32 a, u32 n) {
  _Pragma("unroll 1") for (u32 spin = 0; spin < (1u << 22); spin++) {
    asm volatile("" ::: "memory");
    if (__all(*(volatile LAS u32*)a >= n)) break;
    __builtin_amdgcn_s_sleep(SL);
  }
  asm volatile("" ::: "memory");
}
template <int V> struct IC { static constexpr int v = V; };
template <int I, int N, class Fn> DI void sfor(Fn&& f) { if constexpr (I < N) { f(IC<I>{}); sfor<I + 1, N>(f); } }
constexpr int step(int i) { return i < 28 ? i : i < 32 ? i - 16 : i < 44 ? i - 32 : i - 28; }
DI u32x4 lds128(u32 a, u32 o = 0) { return *(const LAS u32x4*)((LAS char*)a + o); }
DI s16x4 tr64(u32 a, u32 o) { return __builtin_amdgcn_ds_read_tr16_b64_v4i16((LAS s16x4*)((LAS char*)a + o)); }
DI u32x4 cat(s16x4 lo, s16x4 hi) { return __builtin_bit_cast(u32x4, __builtin_shufflevector(lo, hi, 0, 1, 2, 3, 4, 5, 6, 7)); }
DI u32x4 ldstr(u32 a, u32 o0, u32 o1) { return cat(tr64(a, o0), tr64(a, o1)); }

struct Srd { const void* p; u32 bytes, flags; };
struct Args {
  Srd x, w1, w2;
  char* ws;
  u16* out;
  const u16 *lar, *aim, *cre, *cim, *ldt, *dsk, *b1, *b2, *gw;
};
DI u32 colblock(u32 jj, u32 w2lo) { return (jj + CBO - (w2lo >> 7)) & 7; }
DI u64 mk64(u32 lo, u32 hi) { return (u64)lo | ((u64)hi << 32); }
#define STAMP_OUT()

__global__ void __launch_bounds__(512, 2) fused_mix(Args) {
  __shared__ __attribute__((aligned(16))) char smem[LDS_BYTES];
  const u32 sb = (u32)(size_t)(LAS char*)smem;
  ST(0);
  u64 a_lar, a_aim, a_cre, a_cim, a_ldt, a_dsk, a_ws, a_oth;
  u32 wave, r_lar, r_aim, r_cre, r_cim, ctag, t_ldt, t_dsk;
  asm volatile(
      "s_load_dwordx2 %[lar], %[ka], 64\n\ts_load_dwordx2 %[ws], %[ka], 48\n\ts_load_dwordx2 %[aim], %[ka], 72\n\ts_load_dwordx2 %[cre], %[ka], 80\n\t"
      "s_load_dwordx2 %[cim], %[ka], 88\n\ts_load_dwordx2 %[ldt], %[ka], 96\n\ts_load_dwordx2 %[dsk], %[ka], 104\n\t"
      "v_readfirstlane_b32 %[wv], %[tid]\n\ts_getpc_b64 %[oth]\n.Lcode_start:\n\t"
      "s_lshr_b32 %[wv], %[wv], 6\n\ts_cmp_lt_u32 %[wv], 4\n\ts_cbranch_scc1 1f\n\t"
      "s_setprio 3\n\tv_and_b32 %[r3], 0xff, %[tid]\n\ts_lshr_b32 %[tg], %[b], 3\n\tv_lshlrev_b32 %[r0], 2, %[r3]\n\t"
      "v_lshl_or_b32 %[r0], %[tg], 10, %[r0]\n\tv_min_u32 %[r0], .Lcode_end - .Lcode_start - 4, %[r0]\n\tglobal_load_dword %[r0], %[r0], %[oth]\n\t"
      "s_lshl_b32 %[tg], %[wv], 3\n\ts_add_u32 %[tg], %[tg], 32\n\ts_load_dwordx2 %[oth], %[ka], %[tg]\n\t"
      "v_lshlrev_b32 %[r3], 1, %[r3]\n\tv_lshl_or_b32 %[r3], %[b], 9, %[r3]\n\ts_xor_b32 %[tl], %[b], %[pp]\n\t"
      "v_and_b32 %[r2], 63, %[tid]\n\tv_lshlrev_b32 %[r2], 3, %[r2]\n\tv_lshl_or_b32 %[r2], %[tl], 9, %[r2]\n\ts_lshl_b32 %[td], %[b], 2\n\t"
      "s_waitcnt lgkmcnt(0)\n\t"
      "global_load_ushort %[r0], %[r3], %[lar]" PPOL "\n\tglobal_load_ushort %[r1], %[r3], %[aim]" PPOL "\n\t"
      "global_load_dword %[r2], %[r2], %[oth]\n\t"
      "global_load_ushort %[r2], %[r3], %[cre]" PPOL "\n\tglobal_load_ushort %[r3], %[r3], %[cim]" PPOL "\n\t"
      "s_load_dword %[tg], %[ws], 0\n\ts_load_dword %[tl], %[ldt], %[td]\n\ts_load_dword %[td], %[dsk], %[td]\n1:"
      : [lar] "=&s"(a_lar), [aim] "=&s"(a_aim), [cre] "=&s"(a_cre), [cim] "=&s"(a_cim), [ldt] "=&s"(a_ldt), [dsk] "=&s"(a_dsk), [ws] "=&s"(a_ws),
        [oth] "=&s"(a_oth), [wv] "=&s"(wave), [tg] "=&s"(ctag), [tl] "=&s"(t_ldt), [td] "=&s"(t_dsk),
        [r0] "=&v"(r_lar), [r1] "=&v"(r_aim), [r2] "=&v"(r_cre), [r3] "=&v"(r_cim)
      : [ka] "s"(__builtin_amdgcn_kernarg_segment_ptr()), [tid] "v"(threadIdx.x), [b] "s"(blockIdx.x), [pp] "i"(PPAIR) : "scc");
  SB();
  const u32 w = wave & 3;
  const u32 b = blockIdx.x, xcd = b & 7, bi = b >> 3;
  const u32 hf = xcd & 1, p = 4 * (xcd >> 1) + (bi >> 3), jj = bi & 7;
  const bool spare = jj == 7;
  const u32 cb1 = 7 * hf + jj, rb = 2 * p + hf, pair = b;
  const u32 wm = w >> 1, wn = w & 1;
#define LANES() const u32 tid = opq(threadIdx.x) & 255, lane = tid & 63, hh = lane >> 5, blk = (lane >> 4) & 1, q4 = (lane & 15) >> 2, p4 = lane & 3, g4 = lane >> 4, l16 = opq(lane * 16)
  const u32 rot = 2 * (bi >> 3) + jj;

  if (wave >= 4) {
    u32x16 k0;
    u32x4 k1;
    asm volatile("s_load_dwordx16 %0, %2, 0\n\ts_load_dwordx4 %1, %2, 120" : "=&s"(k0), "=&s"(k1) : "s"(__builtin_amdgcn_kernarg_segment_ptr()));
    SB();
    LANES();
    const u32 ta = opq(sb + TAB + tid * 224);
    const u32 t0 = opq(sb + TAB + (wm * 128 + 4 * g4 + q4) * 224 + 8 * p4), t1 = opq(t0 + 32 * wn);
    const u32 ko = opq(sb + KD + wm * KD_CH + (4 * g4 * 36 + 16 * wn + (lane & 15)) * 2);
    char* const ws = (char*)a_ws;
    const __amdgpu_buffer_rsrc_t sWS = __builtin_amdgcn_make_buffer_rsrc((void*)ws, 0, WS_END, 0x00020000);
    u32 ga[9];
    {
      auto cell = [&](u32 pos) {
        const u32 hi = pos >> 8, ex = min(3u, 32u - (u32)__clz(hi));
        return sb + KD + (4 * ex + ((8 * hi) >> ex)) * 72 + ((pos & 255) >> 2);
      };
      const u32 P = 2040 - 8 * tid, a0 = cell(P), a8 = cell(P + 8);
      const u32 c0 = w == 3 ? 0x83222100u : w == 2 ? 0x88111000u : 0x88880000u, c1 = w == 3 ? 0x76543210u : w == 2 ? 0x88111000u : 0x88800000u;
      const u32 code = (w == 3 ? hh : (lane & 1)) ? c1 : c0;
#pragma unroll
      for (int k = 0; k < 8; k++) {
        const u32 n = (code >> (4 * k)) & 15u;
        ga[k + 1] = opq(n == 8 ? a8 : a0 + n * 72);
      }
      const u32 lowa = opq(P == 0 ? sb + KD + KD_ZERO : sb + KD + 7 * 72 + ((P - 8) >> 2));
      ga[0] = opq(P > 256 ? a0 : lowa);
    }
    *(LAS u32*)(hh ? sb + CNT + (lane & 15) * 4 : sb + KD + (lane & 1) * KD_CH + KD_ZERO) = 0;
    const u32 to = opq(sb + TC + tid * 16);
    const u32 ur = opq(sb + hf * 8192 + (lane & 7) * 1024 + (((lane >> 3) ^ (4 * (lane & 1))) << 4) + 4 * w);
    const u32 uw = opq(WS_UT + ((rot & 7) * 64 + (lane >> 3) * 8 + 2 * w) * UTS + rb * 128 + (lane & 7) * 16);

    asm volatile("s_waitcnt lgkmcnt(0)" : "+s"(ctag), "+s"(t_ldt), "+s"(t_dsk), "+s"(k0), "+s"(k1)::"memory");
    asm volatile("s_waitcnt vmcnt(0)" : "+v"(r_lar), "+v"(r_aim), "+v"(r_cre), "+v"(r_cim)::"memory");
    const u32 tag = ctag + 1, cb2 = colblock(jj, k0[8]);
    const u64 p_gw = mk64(k1[2], k1[3]);
    ST(2);
    {
      const float dt = rbf(__expf(bf2f(wm ? t_ldt >> 16 : t_ldt & 0xffffu)));
      const float er = -(rbf(__expf(bf2f(r_lar))) * dt) * 1.44269504088896f;
      const float wi = (bf2f(r_aim) * dt) * 0.159154943091895f;
      auto seed = [&](float t) {
        const float m = __builtin_amdgcn_exp2f(er * t), a = __builtin_amdgcn_fractf(wi * t);
        return f32x2{m * __builtin_amdgcn_cosf(a), m * __builtin_amdgcn_sinf(a)};
      };
      auto put8 = [&](const f32x2* v, u32 off) {
        u32x4 re = {pk2(v[0].x, v[1].x), pk2(v[2].x, v[3].x), pk2(v[4].x, v[5].x), pk2(v[6].x, v[7].x)};
        u32x4 im = {pk2(v[0].y, v[1].y), pk2(v[2].y, v[3].y), pk2(v[4].y, v[5].y), pk2(v[6].y, v[7].y)};
        *(LAS u32x4*)((LAS char*)ta + off) = re;
        *(LAS u32x4*)((LAS char*)ta + 112 + off) = im;
      };
      f32x2 v[8];
      const Mul w8 = mk(seed(8.f)), w64 = mk(seed(64.f));
      v[0] = f32x2{1.f, 0.f}; v[1] = w8.b; v[2] = cmul(v[1], w8);
      const Mul w16 = mk(v[2]);
#pragma unroll
      for (int k = 3; k < 8; k++) v[k] = cmul(v[k - 2], w16);
      put8(v, 0); SB();
      v[0] = w64.b;
#pragma unroll
      for (int r = 1; r < 4; r++) {
#pragma unroll
        for (int k = (r == 1); k < 8; k++) v[k] = cmul(v[k], w64);
        put8(v, 16 * r); SB();
      }
      f32x2 s1 = seed(1.f), s256 = seed(256.f);
      s1.y = -s1.y; s256.y = -s256.y;
      const Mul v1 = mk(s1), W = mk(s256), v2c = mk(cmul(s1, v1));
      f32x2 u[8];
      u[0] = f32x2{2.f * (bf2f(r_cre) * dt), -2.f * (bf2f(r_cim) * dt)};
      u[1] = cmul(u[0], v1);
#pragma unroll
      for (int k = 2; k < 8; k++) u[k] = cmul(u[k - 2], v2c);
      put8(u, 64); SB();
      v[0] = cmul(u[0], W); v[1] = cmul(u[2], W); v[2] = cmul(u[4], W); v[3] = cmul(u[6], W);
      v[4] = cmul(v[0], W); v[5] = cmul(v[2], W); v[6] = cmul(v[4], W); v[7] = cmul(v[5], W);
      u[0] = cmul(v[6], W); u[1] = cmul(u[0], W); u[2] = cmul(u[1], W); u[3] = cmul(u[2], W); u[4] = cmul(u[3], W);
      put8(v, 80);
      u[5] = u[6] = u[7] = f32x2{0.f, 0.f};
      put8(u, 96);
      ST(3);
      BAR();
      ST(14);
      f32x4 ka[2] = {{(lane == 0 && wn == 0) ? bf2f(wm ? t_dsk >> 16 : t_dsk & 0xffffu) : 0.f, 0.f, 0.f, 0.f}, {0.f, 0.f, 0.f, 0.f}};
      u32x4 fv[8], f0[8], f1[8];
#pragma unroll
      for (int ks = 0; ks < 10; ks++) {
        const u32 o = ks * 16 * 224;
        if (ks < 8) { fv[ks] = ldstr(t1, o, o + 112); f0[ks] = ldstr(t0, o + 64, o + 64 + 112); f1[ks] = ldstr(t0, o + 96, o + 96 + 112); }
        if (ks >= 2) { ka[0] = MF16(f0[ks - 2], fv[ks - 2], ka[0]); ka[1] = MF16(f1[ks - 2], fv[ks - 2], ka[1]); }
        SB();
      }
#pragma unroll
      for (int rt = 0; rt < 2; rt++)
#pragma unroll
        for (int r = 0; r < 4; r += 2) {
          const u32 pr = pk2(ka[rt][r], ka[rt][r + 1]);
          *(LAS u16*)((LAS char*)ko + (16 * rt + r) * 72) = (u16)pr;
          *(LAS u16*)((LAS char*)ko + (16 * rt + r + 1) * 72) = (u16)(pr >> 16);
        }
    }
    ST(9);
    BAR();
    ST(4);
    {
      u32 xr[8];
#pragma unroll
      for (int j = 0; j < 8; j++) xr[j] = *(const LAS u32*)((LAS char*)(ur ^ ((j >> 1) << 4)) + j * 128);
#pragma unroll
      for (int c = 0; c < 2; c++) {
        u32x4 ou;
#pragma unroll
        for (int e = 0; e < 4; e++) ou[e] = __builtin_amdgcn_perm(xr[2 * e + 1], xr[2 * e], c ? 0x07060302 : 0x05040100);
        __builtin_amdgcn_raw_buffer_store_b128(ou, sWS, uw + c * UTS, 0, 16);
      }
    }
    ST(10);
    if (!spare) BAR();
    const u32 ufo = opq(WS_UF + (pair >> 5) * 128 + (lane & 31) * 4);
    const u32 ua = opq(sb + US + wm * US_STR + (256 - 16 - 128 * wn - 96 + 16 * (lane & 15) + 8 * g4) * 2);
    u32 tb[4];
#pragma unroll
    for (int j = 0; j < 4; j++) tb[j] = opq(sb + TC + (2 * wm + (lane & 1)) * TC_STR + (2046 - 16 - 128 * wn - 96 - 1792 + 8 * g4 - (lane & 14)) * 2 + 1024 * j);
    const u32 row = tid >> 3, ck = lane & 7;
    const u32 ro = opq(sb + RED + w * 6144 + l16), r1 = opq(sb + RED + (w ^ 1) * 6144 + l16), r2 = opq(sb + RED + (w ^ 2) * 6144 + l16), r3 = opq(sb + RED + (w ^ 3) * 6144 + l16);
    const u32 ao = opq(WS_AT + pair * ATS + (8 * w + g4) * 512 + (lane & 15) * 16);
    const u32 sa = opq(sb + 2 * SLOT + ck * 1024 + ((row ^ (2 * ck)) << 4));
    const u32 ato = opq(WS_AT + (32 * cb2 + 8 * w + hh) * ATS + rb * 512 + (lane & 31) * 16);
    const u32 go = opq(WS_G + rb * 512 + row * 16);
    const u32 oo = opq((64 * rb + row) * 1024 + cb2 * 128 + ck * 16);
    const u32 cw = opq(sb + CNT + 4 * w), cr = opq(sb + CNT + 4 * (lane & 3));
    const u32 aw = opq(sb + TC + (lane & 31) * 144 + (8 * w + hh) * 4), ar = opq(sb + TC + row * 144 + ck * 16);
    auto arrive = [&](u32 n) { *(volatile LAS u32*)cw = n; };
    auto meet = [&](u32 n) { LGKM0(); arrive(n); lds_wait(cr, n); };
    auto pub_u = [&]() { if (tid == 0) asm volatile("buffer_store_dword %0, %1, %2, 0 offen sc1" ::"v"(tag), "v"(WS_UF + (rot & 7) * 128 + rb * 4), "s"(sWS) : "memory"); };
    if (!spare) {
      for (int i = 2; i < UPUB; i++) BAR();
      VMCNT(0);
      BAR();
      pub_u();
      ST(5);
    } else {
      u32x4 gwv, xr[16];
      asm volatile("global_load_dwordx4 %0, %1, %2" : "=v"(gwv) : "v"(l16), "s"(p_gw));
#pragma unroll
      for (int j = 0; j < 16; j++)
        asm volatile("buffer_load_dwordx4 %0, %1, %2, %3 offen offset:%4" : "=v"(xr[j]) : "v"(l16), "s"(k0.s0123), "s"((64 * rb + 16 * w + (j & ~3)) * 1024), "i"((j & 3) * 1024));
      arrive(1);
      asm volatile("s_waitcnt vmcnt(0)" : "+v"(gwv), "+v"(xr[0]), "+v"(xr[1]), "+v"(xr[2]), "+v"(xr[3]), "+v"(xr[4]), "+v"(xr[5]), "+v"(xr[6]), "+v"(xr[7]),
                   "+v"(xr[8]), "+v"(xr[9]), "+v"(xr[10]), "+v"(xr[11]), "+v"(xr[12]), "+v"(xr[13]), "+v"(xr[14]), "+v"(xr[15])::"memory");
      meet(2);
      pub_u();
      ST(5);
      const u32 zs = sb + TC + w * TC_STR;
#pragma unroll
      for (int j = 0; j < 16; j++) {
        float z = 0.f;
#pragma unroll
        for (int e = 0; e < 4; e++) z = dot2(xr[j][e], gwv[e], z);
        *(LAS float*)((LAS char*)(zs + lane * 4) + j * 260) = z;
      }
      LGKM0();
      float zz = 0.f;
#pragma unroll
      for (int k = 0; k < 16; k++) zz += *(const LAS float*)((LAS char*)(zs + (lane & 15) * 260 + g4 * 64) + k * 4);
      zz += __shfl_xor(zz, 16); zz += __shfl_xor(zz, 32);
      const float g = rbf(1.f / (1.f + __expf(-zz)));
      if (lane < 16) __builtin_amdgcn_raw_buffer_store_b64(u32x2{pk2(g, 1.f - g), tag}, sWS, WS_G + rb * 512 + (wn * 16 + lane) * 16 + wm * 8, 0, 16);
      meet(3);
    }
    if (w < 2) *(LAS u32x4*)(w == 0 ? sb + US + hh * US_STR + (lane & 31) * 16 : sb + TC + ((lane >> 1) & 3) * TC_STR + 4096 + (lane & 1) * 16) = u32x4{0, 0, 0, 0};
    auto fetch_u = [&]() {
#pragma unroll
      for (int q = 0; q < 8; q++) {
        if (!(q & 3)) SETM0(sb, US + 512 + (q >> 2) * US_STR);
        DMAP(" sc1", l16, sWS, WS_UT + (2 * pair + (q >> 2)) * UTS, q & 3);
      }
    };
    u32 ek[2][9];
    auto exp_rd = [&]() {
#pragma unroll
      for (int i = 0; i < 9; i++) { ek[0][i] = *(const LAS u16*)ga[i]; ek[1][i] = *(const LAS u16*)((LAS char*)ga[i] + KD_CH); SB(); asm volatile("s_nop 3"); SB(); }
    };
    auto exp_wr = [&](int i) {
      const u32* e = ek[i >> 1] + (i & 1);
      *(LAS u32x4*)((LAS char*)to + i * TC_STR) = u32x4{e[7] | (e[6] << 16), e[5] | (e[4] << 16), e[3] | (e[2] << 16), e[1] | (e[0] << 16)};
    };
    bool got = w != 0;
    if (!spare) {
      if (EXPE) exp_rd();
      BAR();
      if (EXPE) { exp_wr(0); SB(); __builtin_amdgcn_s_sleep(2); SB(); exp_wr(1); }
      BAR();
#pragma unroll
      for (int i = 0; i < 2; i++) {
        if (EXPE) exp_wr(2 + i);
        if (got || i < UTRY) {
          BAR(); BAR();
        } else {
          u32 uf;
          asm volatile("buffer_load_dword %0, %1, %2, 0 offen sc1\n\ts_barrier\n\ts_barrier\n\ts_waitcnt vmcnt(0)" : "=&v"(uf) : "v"(ufo), "s"(sWS) : "memory");
          got = __all(uf == tag);
          if (got) fetch_u();
        }
      }
    }
    __builtin_amdgcn_s_setprio(CVPRIO);
    ST(21);
    if (!EXPE || spare) { exp_rd(); exp_wr(0); exp_wr(1); exp_wr(2); exp_wr(3); }
    if (!got) {
      __builtin_amdgcn_s_setprio(3);
      _Pragma("unroll 1") for (u32 spin = 0; spin < SPIN_CAP; spin++) {
        u32 uf;
        asm volatile("buffer_load_dword %0, %1, %2, 0 offen sc1\n\ts_waitcnt vmcnt(0)" : "=&v"(uf) : "v"(ufo), "s"(sWS) : "memory");
        if (__all(uf == tag)) break;
        __builtin_amdgcn_s_sleep(USLEEP);
      }
      fetch_u();
      *(volatile LAS u32*)(sb + GO + 4) = 1;
      __builtin_amdgcn_s_setprio(CVPRIO);
    }
    VMCNT(0);
    ST(6);
    meet(4);
    ST(7);

    f32x4 ca[8];
#pragma unroll
    for (int c = 0; c < 8; c++) ca[c] = f32x4{0.f, 0.f, 0.f, 0.f};
    u32x4 fu[2][8];
    u32x2 ft[2][16];
    auto cread = [&](int f, int r) {
      const int k = r < 2 ? 8 + r : r < 10 ? r - 2 : r;
      if (k < 8) fu[f & 1][k] = lds128(ua, (96 - 32 * f + 256 * k) * 2);
      else {
        const int i = (k - 8) >> 1, hl = k & 1, e = 7 - i;
        const LAS u32* t = (const LAS u32*)((LAS char*)tb[e >> 1] + (96 - 32 * f + 256 * (e & 1)) * 2 + 8 * hl);
        ft[f & 1][2 * i + hl] = u32x2{t[0], t[1]};
      }
    };
#pragma unroll
    for (int r = 0; r < 24; r++) { cread(0, r); SB(); }
#pragma unroll
    for (int f = 0; f < 4; f++) {
      int m = 0;
#pragma unroll
      for (int i = 0; i < 8; i++)
#pragma unroll
        for (int e = 0; e + i < 8; e++, m++) {
          const u32x4 tf = {ft[f & 1][2 * i][0], ft[f & 1][2 * i][1], ft[f & 1][2 * i + 1][0], ft[f & 1][2 * i + 1][1]};
          ca[i + e] = MF16(fu[f & 1][e], tf, ca[i + e]);
          if (f < 3 && m >= 4 && m < 28) cread(f + 1, m - 4);
          if (f == 3 && m == 0) arrive(5);
          SB();
        }
    }
    ST(8);
    f32x4 keep[2];
    __builtin_amdgcn_s_setprio(3);
    lds_wait(cr, 5);
#define SEND(W) { _Pragma("unroll") for (int j = 1; j < 4; j++) { *(LAS f32x4*)((LAS char*)ro + (j - 1) * 2048) = ca[2 * (W ^ j)]; *(LAS f32x4*)((LAS char*)ro + (j - 1) * 2048 + 1024) = ca[2 * (W ^ j) + 1]; } \
                  keep[0] = ca[2 * W]; keep[1] = ca[2 * W + 1]; asm volatile("; send " #W ::: "memory"); }
    if (w == 0) SEND(0) else if (w == 1) SEND(1) else if (w == 2) SEND(2) else SEND(3)
    asm volatile("" ::: "memory");
    arrive(6);
    lds_wait(cr, 6);
    const u32 sel = wm ? 0x01000302 : 0x03020100;
    f32x4 pp[2][3];
#pragma unroll
    for (int c = 0; c < 2; c++) {
      pp[c][0] = *(const LAS f32x4*)((LAS char*)r1 + c * 1024);
      pp[c][1] = *(const LAS f32x4*)((LAS char*)r2 + 2048 + c * 1024);
      pp[c][2] = *(const LAS f32x4*)((LAS char*)r3 + 4096 + c * 1024);
    }
#pragma unroll
    for (int c = 0; c < 2; c++) {
      u32 o[4];
#pragma unroll
      for (int r = 0; r < 4; r++) {
        const u32 v = pk2(add1(keep[c][r], pp[c][0][r]), add1(pp[c][1][r], pp[c][2][r]));
        o[r] = __builtin_amdgcn_perm(v, v, sel);
      }
#pragma unroll
      for (int r = 0; r < 2; r++) __builtin_amdgcn_raw_buffer_store_b128(u32x4{o[r], tag, o[r + 2], tag}, sWS, ao + c * 2048 + r * 256, 0, 16);
    }
    arrive(7);
    ST(11);
    const __amdgpu_buffer_rsrc_t sB2 = __builtin_amdgcn_make_buffer_rsrc((void*)mk64(k1[0], k1[1]), 0, D * 2, 0x00020000);
    const __amdgpu_buffer_rsrc_t sOut = __builtin_amdgcn_make_buffer_rsrc((void*)mk64(k0[14], k0[15]), 0, L * D * 2, 0x00020000);
    const u32x4 b2v = __builtin_amdgcn_raw_buffer_load_b128(sB2, ck * 16, cb2 * 128, 0);
    lds_wait<LSL>(cr + RDY - CNT, 1);
    ST(12);
    u32x4 gg, at[4];
    _Pragma("unroll 1") for (u32 spin = 0; spin < SPIN_CAP; spin++) {
      asm volatile("" ::: "memory");
#pragma unroll
      for (int j = 0; j < 4; j++) at[j] = __builtin_amdgcn_raw_buffer_load_b128(sWS, ato, 2 * j * ATS, 16);
      gg = __builtin_amdgcn_raw_buffer_load_b128(sWS, go, 0, 16);
      bool ok = gg[1] == tag && gg[3] == tag;
#pragma unroll
      for (int j = 0; j < 4; j++) ok &= at[j][1] == tag && at[j][3] == tag;
      if (__all(ok)) break;
      __builtin_amdgcn_s_sleep(RSLEEP);
    }
    __builtin_amdgcn_s_setprio(TPRIO);
    ST(13);
    lds_wait(cr, 7);
#pragma unroll
    for (int j = 0; j < 4; j++) {
      *(LAS u32*)((LAS char*)aw + j * 8) = at[j][0];
      *(LAS u32*)((LAS char*)aw + j * 8 + 32 * 144) = at[j][2];
    }
    asm volatile("" ::: "memory");
    arrive(8);
    lds_wait(cr, 8);
    ST(15);
#pragma unroll
    for (int t = 0; t < 2; t++) {
      const float g = bf2f(gg[2 * t] & 0xffffu), gm = hi2f(gg[2 * t]);
      const u32x4 av = lds128(ar, t * 32 * 144);
      float base[8];
#pragma unroll
      for (int i = 0; i < 4; i++) {
        base[2 * i] = fopq(fma1(gm, bf2f(b2v[i] & 0xffffu), mul1(g, bf2f(av[i] & 0xffffu))));
        base[2 * i + 1] = fopq(fma1(gm, hi2f(b2v[i]), mul1(g, hi2f(av[i]))));
      }
      if (t == 0) { ST(16); lds_wait<LSL>(cr + RDY - CNT, 2); } else { ST(18); asm volatile("s_barrier" ::: "memory"); ST(19); }
      f32x4 pt[4][2];
#pragma unroll
      for (int k = 0; k < 4; k++)
#pragma unroll
        for (int q = 0; q < 2; q++) pt[k][q] = *(const LAS f32x4*)((LAS char*)sa + (1 - t) * SLOT + k * 8192 + q * 512);
      u32x4 ow;
#pragma unroll
      for (int i = 0; i < 4; i++) {
        float o[2];
#pragma unroll
        for (int e = 0; e < 2; e++) {
          const int q = i >> 1, r = 2 * (i & 1) + e;
          o[e] = t ? __builtin_fmaf(gm, (pt[0][q][r] + pt[1][q][r]) + (pt[2][q][r] + pt[3][q][r]), base[2 * i + e])
                   : fma1(gm, add1(add1(pt[0][q][r], pt[1][q][r]), add1(pt[2][q][r], pt[3][q][r])), base[2 * i + e]);
        }
        ow[i] = pk2(o[0], o[1]);
      }
      __builtin_amdgcn_raw_buffer_store_b128(ow, sOut, oo + t * 32768, 0, OUTPOL);
      SB();
      if (t == 0) ST(17);
    }
    asm volatile(".Lcode_end:");
    STAMP_OUT();
    return;
  }

  u32x16 k0;
  u32x4 k1;
  asm volatile("s_load_dwordx16 %0, %2, 0\n\ts_load_dwordx4 %1, %2, 112"
               : "=&s"(k0), "=&s"(k1) : "s"(__builtin_amdgcn_kernarg_segment_ptr()));
  SB();
  LANES();
  const u32 o1 = opq(((spare ? 0 : cb1) * 128 + wn * 64 + (lane & 31)) * 2);
  u32 vA[4], vB[4], v2[4];
#pragma unroll
  for (int q = 0; q < 4; q++) {
    vA[q] = opq((lane >> 3) * 1024 + (((lane & 7) ^ g4 ^ (4 * (q & 1))) << 4) + q * (8192 - 1024));
    vB[q] = opq(g4 * (F * 2) + (((lane & 15) ^ (g4 << 2) ^ q) << 4) + q * (4 * F * 2 - 1024));
    v2[q] = opq((lane >> 3) * 1024 + (((lane & 7) ^ (blk << 2)) << 4) + q * (8192 - 1024));
  }
  const u32 xbase = sopq((128 * p + 32 * w) * 1024), wbase = sopq(16 * w * (F * 2) + cb1 * 256);
  const u32 m4 = sopq(sb + w * 4096), m8 = sopq(sb + w * 8192);
  asm volatile("s_waitcnt lgkmcnt(0)" : "+s"(k0), "+s"(k1) : "s"(a_lar), "s"(a_aim), "s"(a_cre), "s"(a_cim), "s"(a_ldt), "s"(a_dsk), "s"(a_ws));
  ST(14);
  const u32x4 sX = k0.s0123, sW1 = k0.s4567;
  const u32x4 sW2 = k0.s89ab;
  char* const ws = (char*)mk64(k0[12], k0[13]);
  const u32 w2base = 28 * w * 16384 + colblock(jj, sW2[0]) * 128;
  u32 tag;
  const __amdgpu_buffer_rsrc_t sWS = __builtin_amdgcn_make_buffer_rsrc((void*)ws, 0, WS_END, 0x00020000);
  auto w2piece = [&](int c, int q) {
    if ((q & 3) == 0) SETM0(m8, (c & 3) * SLOT + (q >> 2) * 4096);
    DMAQ(v2[q & 3], sW2, w2base + c * 65536 + (q >> 2) * 32768, q & 3);
  };

  if (!spare) {
    auto g1piece = [&](int g, int q) {
      if (q == 0) SETM0(m4, (g & 3) * SLOT);
      if (q == 4) SETM0(m4, (g & 3) * SLOT + 16384);
      if (q < 4) DMAP(XPOL, vA[q], sX, xbase + ((g + rot) & 7) * 128, q);
      else DMAQ(vB[q - 4], sW1, wbase + ((g + rot) & 7) * 64 * (F * 2), q - 4);
    };
    u32 r_b1a, r_b1b;
    asm volatile("s_load_dword %0, %1, 0" : "=s"(tag) : "s"(ws));
    asm volatile("global_load_ushort %0, %1, %2" : "=v"(r_b1a) : "v"(o1), "s"(mk64(k1[0], k1[1])));
    asm volatile("global_load_ushort %0, %1, %2 offset:64" : "=v"(r_b1b) : "v"(o1), "s"(mk64(k1[0], k1[1])));
#pragma unroll
    for (int i = 0; i < 12; i++) g1piece(i >> 3, i < 8 ? i : i - 4);
    ST(1);
    u32 ax[2][4], aw[2][4];
    {
      const u32 xa = sb + (64 * wm + (lane & 31)) * 128 + ((hh ^ ((lane >> 1) & 7)) << 4);
      const u32 wa = sb + 16384 + 256 * (8 * hh + q4) + 8 * (p4 & 1) + (((8 * wn + 2 * blk + (p4 >> 1)) ^ ((q4 << 2) | (2 * hh))) << 4);
#pragma unroll
      for (int k = 0; k < 4; k++) {
        ax[0][k] = opq(xa ^ (k << 5)); aw[0][k] = opq(wa ^ ((k & 1) << 4) ^ ((k >> 1) << 6));
        ax[1][k] = opq(ax[0][k] + 2 * SLOT); aw[1][k] = opq(aw[0][k] + 2 * SLOT);
      }
    }
    const u32 ho = opq(l16 + WS_H + ((4 * p + 2 * wm) * 112 + cb1 * 8 + wn * 4) * 1024), ho1 = opq(ho + 112 * 1024);
    asm volatile("s_barrier" ::: "memory");
    ST(3);
#pragma unroll
    for (int q = 0; q < 8 * LA - 12; q++) g1piece(q < 4 ? 1 : 2, q < 4 ? q : q - 4);
    asm volatile("s_waitcnt lgkmcnt(0)" : "+s"(tag));
    asm volatile("s_waitcnt vmcnt(%2)" : "+v"(r_b1a), "+v"(r_b1b) : "i"(8 * LA));
    f32x16 acc[2][2];
    {
      const u32x4 one = {hh ? 0u : 0x3f80u, 0u, 0u, 0u};
      const u32x4 ba = {r_b1a, 0u, 0u, 0u}, bb = {r_b1b, 0u, 0u, 0u};
      asm volatile("s_nop 1\n\tv_mfma_f32_32x32x16_bf16 %0, %4, %6, 0\n\tv_mfma_f32_32x32x16_bf16 %1, %4, %6, 0\n\t"
                   "v_mfma_f32_32x32x16_bf16 %2, %5, %6, 0\n\tv_mfma_f32_32x32x16_bf16 %3, %5, %6, 0"
                   : "=&v"(acc[0][0]), "=&v"(acc[0][1]), "=&v"(acc[1][0]), "=&v"(acc[1][1]) : "v"(ba), "v"(bb), "v"(one));
    }
    u32x4 fx[2][2];
    s16x4 fw[2][4];
    VMCNT(8 * LA - 8);
    ST(2);
    BAR();
    ST(4);
    fx[0][0] = lds128(ax[0][0]); fw[0][0] = tr64(aw[0][0], 0); fw[0][1] = tr64(aw[0][1], 1024);
    fx[0][1] = lds128(ax[0][0], 4096); fw[0][2] = tr64(aw[0][2], 0); fw[0][3] = tr64(aw[0][3], 1024);
#pragma unroll
    for (int s = 0; s < 8; s++) {
      auto piece = [&](int q) { if (s + LA < 8) g1piece(s + LA, q); else if (s + LA - 8 < WPRE) w2piece(s + LA - 8, q); };
#pragma unroll
      for (int k = 0; k < 4; k++) {
        const int c = k & 1, n = c ^ 1, kn = (k + 1) & 3, sl = (k == 3 ? s + 1 : s) & 3, hi = sl >> 1;
        const u32 o = (sl & 1) * SLOT, ow = o + kn * 4096;
        const bool more = s < 7 || k < 3;
        MMV(acc[0][0], cat(fw[c][0], fw[c][1]), fx[c][0]);
        if (k == 3) {
          if (s < 7) VMCNT((LA == 3 && s + 2 < 8 + WPRE ? 8 : 0) + (s + LA < 8 + WPRE ? 6 : 0));
          BAR();
        }
        if (more) { fx[n][0] = lds128(ax[hi][kn], o); fw[n][0] = tr64(aw[hi][0], ow); fw[n][1] = tr64(aw[hi][1], ow + 1024); }
        MMV(acc[0][1], cat(fw[c][0], fw[c][1]), fx[c][1]);
        if (more) { fx[n][1] = lds128(ax[hi][kn], o + 4096); fw[n][2] = tr64(aw[hi][2], ow); }
        piece(2 * k);
        MMV(acc[1][0], cat(fw[c][2], fw[c][3]), fx[c][0]);
        if (more) fw[n][3] = tr64(aw[hi][3], ow + 1024);
        MMV(acc[1][1], cat(fw[c][2], fw[c][3]), fx[c][1]);
        piece(2 * k + 1);
      }
      if (s == 0) ST(8);
      if (s == 1) ST(15);
      if (s == 2) ST(16);
      if (s == 3) ST(17);
      if (s == 4) ST(19);
      if (s == 5) ST(20);
      if (s == 6) ST(21);
    }
    asm volatile("s_nop 7\n\ts_nop 7" : "+v"(acc[0][0]), "+v"(acc[0][1]), "+v"(acc[1][0]), "+v"(acc[1][1]));
    __builtin_amdgcn_s_setprio(2);
    ST(5);
#pragma unroll
    for (int u = 0; u < 2; u++)
#pragma unroll
      for (int t = 0; t < 2; t++)
#pragma unroll
        for (int s = 0; s < 2; s++) {
          u32x4 o;
#pragma unroll
          for (int e = 0; e < 4; e++) {
            const float a0 = relu(acc[u][t][8 * s + 2 * e]), a1 = relu(acc[u][t][8 * s + 2 * e + 1]);
            o[e] = EPK ? pk2(a0 * a0, a1 * a1) : pk2(mul1(a0, a0), mul1(a1, a1));
          }
          __builtin_amdgcn_raw_buffer_store_b128(o, sWS, (t ? ho1 : ho) + (2 * u + s) * 1024, 0, 16);
          SB();
        }
    tag += 1;
  } else {
    asm volatile("s_load_dword %0, %1, 0" : "=s"(tag) : "s"(ws));
    SETM0(m4, 0);
#pragma unroll
    for (int q = 0; q < 4; q++) DMAP(XPOL, vA[q], sX, xbase + (rot & 7) * 128, q);
    SETM0(m8, SLOT);
    auto pull = [&](u32 n) {
      const u32 u = n < 4 ? min(14 * xcd + 4 * n + (bi >> 3), 111u) : 28 * (bi >> 3) + 4 * PFC + n - 4;
#pragma unroll
      for (int q = 0; q < 4; q++) DMAQ(l16, sW2, u * 16384 + w * 4096, q);
    };
    __builtin_amdgcn_s_sleep(PFDELAY);
    pull(0); pull(1); pull(2);
    asm volatile("s_waitcnt vmcnt(12) lgkmcnt(0)\n\ts_barrier\n\ts_barrier" : "+s"(tag)::"memory");
    tag += 1;
    ST(2);
    lds_wait(sb + CNT + 4 * (lane & 3), 1);
    ST(8);
    _Pragma("unroll 1") for (u32 n = 3; n < NPF; n++) pull(n);
    ST(4);
#pragma unroll
    for (int i = 0; i < 8 * WPRE; i++) w2piece(i >> 3, i & 7);
    lds_wait(sb + GO + 4, 1);
  }
  SB();
  ST(6);

  u32 w2a[2][2];
  {
    const u32 a = m8 + 128 * (4 * hh + q4) + 8 * (p4 & 1) + (((2 * blk + (p4 >> 1)) ^ ((q4 >> 1) << 2)) << 4);
    w2a[0][0] = opq(a); w2a[1][0] = opq(a ^ 64); w2a[0][1] = opq(a + 2 * SLOT); w2a[1][1] = opq((a ^ 64) + 2 * SLOT);
  }
  auto w2frag = [&](int st, int v) {
    const int sl = (st >> 2) & 3;
    const u32 o = (sl & 1) * SLOT + (st & 3) * 2048;
    return ldstr(w2a[v][sl >> 1], o, o + 1024);
  };
  u32x4 w2r[12][2];
  u32 fl = 0;
  const u32 fo = opq(WS_HF + p * 128 + (((lane & 7) == 7) ? 0 : (lane & 15) * 4));
#pragma unroll
  for (int c = WPRE; c < 8; c++) {
    if (c == WPRE + PUBAT) {
      VMCNT(8 * PUBAT);
      *(volatile LAS u32*)(sb + FD + 4 * w) = 1;
      if (w == 0 && !spare) {
        lds_wait(sb + FD + 4 * (lane & 3), 1);
        if (lane == 0) asm volatile("buffer_store_dword %0, %1, %2, 0 offen sc1" ::"v"(tag), "v"(WS_HF + p * 128 + (8 * hf + jj) * 4), "s"(sWS) : "memory");
      }
      ST(7);
      __builtin_amdgcn_s_sleep(WDELAY);
    }
    if (c >= 4 && c < 7) {
      VMCNT(24);
#pragma unroll
      for (int j = 0; j < 8; j++) w2r[4 * (c - 4) + (j >> 1)][j & 1] = w2frag(4 * (c - 4) + (j >> 1), j & 1);
      LGKM0();
    }
    if (c == POLLAT) asm volatile("s_cmp_lg_u32 %3, 3\n\ts_cbranch_scc1 1f\n\tbuffer_load_dword %0, %1, %2, 0 offen sc1\n1:" : "+v"(fl) : "v"(fo), "s"(sWS), "s"(w) : "memory", "scc");
    if (c < 7) {
#pragma unroll
      for (int q = 0; q < 8; q++) w2piece(c, q);
    }
  }
  const u32 hb = sopq(WS_H + (2 * rb * 112 + 28 * w) * 1024);
  const u32 xw = opq(m8 + 2 * SLOT + l16), rdw = opq(sb + RDY + 4 * w);
  asm volatile("s_waitcnt vmcnt(%1)" : "+v"(fl) : "i"(8 * (7 - POLLAT)) : "memory");
  ST(11);
  if (w == 3) {
    _Pragma("unroll 1") for (u32 spin = 0; spin < SPIN_CAP; spin++) {
      if (__all(fl == tag)) break;
      __builtin_amdgcn_s_sleep(HSLEEP);
      asm volatile("buffer_load_dword %0, %1, %2, 0 offen sc1\n\ts_waitcnt vmcnt(0)" : "=&v"(fl) : "v"(fo), "s"(sWS) : "memory");
    }
    *(volatile LAS u32*)(sb + GO) = 1;
  } else {
    lds_wait(sb + GO, 1);
  }
  __builtin_amdgcn_s_setprio(G2PRIO);
  ST(10);
  SB();

  u32x4 hfr[56];
  auto load_h = [&](int i) {
    const int st = step(i);
    hfr[i] = __builtin_amdgcn_raw_buffer_load_b128(sWS, l16 + (st & 3) * 1024, hb + ((i / 28) * 112 + (st & ~3)) * 1024, 16);
  };
#pragma unroll
  for (int i = 0; i < HEAD; i++) load_h(i);
  SB();
  f32x16 oc[2][2] = {};
  auto send = [&](int t, int j) {
    const f32x16& s = oc[t][j >> 2];
    const int r = 4 * (j & 3);
    *(LAS f32x4*)((LAS char*)(xw ^ (j << 5)) + (1 - t) * SLOT + j * 1024) = f32x4{s[r], s[r + 1], s[r + 2], s[r + 3]};
  };
  auto signal = [&](u32 n) { asm volatile("" ::: "memory"); *(volatile LAS u32*)rdw = n; };
  u32x4 wl[56][2];
  auto fetch_w = [&](int i) { if (i < 56 && step(i) >= 12) { wl[i][0] = w2frag(step(i), 0); wl[i][1] = w2frag(step(i), 1); } };
  sfor<0, 56>([&](auto ic) {
    constexpr int i = decltype(ic)::v, t = i / 28, st = step(i);
    constexpr int D2 = 53 - TAILN;
    if (i < D2 || !TAILN) fetch_w(i + 1); else { fetch_w(2 * i - D2 + 1); fetch_w(2 * i - D2 + 2); }
    SB();
    u32x4 a0, a1;
    if (st < 12) { a0 = w2r[st < 12 ? st : 0][0]; a1 = w2r[st < 12 ? st : 0][1]; } else { a0 = wl[i][0]; a1 = wl[i][1]; }
    oc[t][0] = MF32(a0, hfr[i], oc[t][0]);
    if (i + HEAD < 56) load_h(i + HEAD);
    SB();
    if (i < 56 - TAILN) oc[t][1] = MF32(a1, hfr[i], oc[t][1]);
    if (i == ATAT) signal(1);
    if (i >= 31 && i < 39) send(0, i - 31);
    if (i == 38) signal(2);
    SB();
    if (i == 27) ST(18);
  });
#pragma unroll
  for (int i = 0; i < TAILN; i++) {
    oc[1][1] = MF32(wl[56 - TAILN + i][1], hfr[56 - TAILN + i], oc[1][1]);
    SB();
    if (i >= 1 && i < 3) { send(1, 2 * i - 2); send(1, 2 * i - 1); }
    SB();
  }
  ST(12);
#pragma unroll
  for (int j = TAILN >= 3 ? 4 : 0; j < 8; j++) send(1, j);
  BAR();
  if (b == 0 && w == 3) {
    _Pragma("unroll 1") for (u32 spin = 0; spin < SPIN_CAP; spin++) {
      asm volatile("" ::: "memory");
      const u32x4 f0 = __builtin_amdgcn_raw_buffer_load_b128(sWS, WS_UF + l16, 0, 16);
      if (__all(f0[0] == tag && f0[1] == tag && f0[2] == tag && f0[3] == tag)) break;
    }
    if (lane == 0) __builtin_amdgcn_raw_buffer_store_b32(tag, sWS, 0, 0, 16);
  }
  STAMP_OUT();
}

extern "C" void kernel_launch(void* const* d_in, const int* in_sizes, int n_in, void* d_out, int out_size,
                              void* d_ws, size_t ws_size, hipStream_t stream) {
  Args a;
  a.x = Srd{d_in[0], L * D * 2, 0x00020000}; a.w1 = Srd{d_in[8], D * F * 2, 0x00020000}; a.w2 = Srd{d_in[10], D * F * 2, 0x00020000};
  a.ws = (char*)d_ws + WSO; a.out = (u16*)d_out;
  a.lar = (const u16*)d_in[1]; a.aim = (const u16*)d_in[2]; a.cre = (const u16*)d_in[3]; a.cim = (const u16*)d_in[4];
  a.ldt = (const u16*)d_in[5]; a.dsk = (const u16*)d_in[6]; a.b1 = (const u16*)d_in[9]; a.b2 = (const u16*)d_in[11];
  a.gw = (const u16*)d_in[12];
  fused_mix<<<256, 512, 0, stream>>>(a);
}
```

```cpp
#define STAMP 1
#define RT 1
#define STMASK 1
#include <hip/hip_runtime.h>
#pragma clang diagnostic ignored "-Wint-to-pointer-cast"

typedef unsigned short u16;
typedef unsigned u32;
typedef unsigned long long u64;
typedef __bf16 h16x8 __attribute__((ext_vector_type(8)));
typedef __bf16 h16x2 __attribute__((ext_vector_type(2)));
typedef short s16x4 __attribute__((ext_vector_type(4)));
typedef short s16x8 __attribute__((ext_vector_type(8)));
typedef short s16x2 __attribute__((ext_vector_type(2)));
typedef float f32x2 __attribute__((ext_vector_type(2)));
typedef float f32x4 __attribute__((ext_vector_type(4)));
typedef float f32x16 __attribute__((ext_vector_type(16)));
typedef u32 u32x2 __attribute__((ext_vector_type(2)));
typedef u32 u32x4 __attribute__((ext_vector_type(4)));
typedef u32 u32x16 __attribute__((ext_vector_type(16)));
#define LAS __attribute__((address_space(3)))
#define CAS __attribute__((address_space(4)))
#define GAS __attribute__((address_space(1)))
#define DI __device__ __forceinline__

constexpr int L = 2048, D = 512, F = 1792;
constexpr u32 WS_UF = 8192;
constexpr u32 WS_HF = 4096;
constexpr u32 WS_G = 1u << 19;
#define UTS 4096
#define ATS 16384
#define WSO 0
constexpr u32 WS_UT = 1u << 20;
constexpr u32 WS_AT = 4u << 20;
constexpr u32 WS_H = 9u << 20;
constexpr u32 WS_END = 16u << 20;
constexpr u32 WS_STAMP = 32u << 20;
constexpr u32 SLOT = 32768;
constexpr u32 TAB = 3 * SLOT;
constexpr u32 TC = 4 * SLOT;
constexpr u32 TC_STR = 4160;
constexpr u32 US = TC + 4 * TC_STR;
constexpr u32 US_STR = (256 + 2048) * 2;
constexpr u32 KD_CH = 32 * 36 * 2;
constexpr u32 LDS_BYTES = 163840;
constexpr u32 KD = LDS_BYTES - 2 * KD_CH;
constexpr u32 KD_ZERO = 34 * 2;
constexpr u32 CNT = KD - 64;
constexpr u32 FD = CNT + 16, RDY = CNT + 32, GO = CNT + 48;
constexpr u32 RED = TC;
static_assert(US + 2 * US_STR <= CNT && TAB + 256 * 224 + 16 <= CNT, "lds map");
constexpr u32 SPIN_CAP = 1u << 16;
#define EPK 0
#define WDELAY 0
#define CBO 4
#define TAILN 4
#define EXPE 0
#define LSL 4
#define TPRIO 3
#define UTRY 1
#define FDELAY 0
#define PRET 4
#define HEAD 12
#define WPRE 3
#define PUBAT 0
#define PPOL " nt"
#define PPOL2 ""
#define XPOL ""
#define NPF 4
#define PFDELAY 24
#define POLLAT 6
#define PFC 0
#define LA 3
#define OUTPOL 16
#define UPUB 2
#define RSLEEP 8
#define USLEEP 8
#define HSLEEP 2
#define PPAIR 4
#define ATAT 16
#define G2PRIO 0
#define CVPRIO 1

DI float bf2f(u32 v) { return __uint_as_float(v << 16); }
DI u32 pk2(float lo, float hi) { f32x2 a = {lo, hi}; return __builtin_bit_cast(u32, __builtin_convertvector(a, h16x2)); }
DI float rbf(float x) { return bf2f(pk2(x, x) & 0xffffu); }
DI float dot2(u32 a, u32 b, float c) { return __builtin_amdgcn_fdot2_f32_bf16(__builtin_bit_cast(h16x2, a), __builtin_bit_cast(h16x2, b), c, false); }
DI u32 rfl(u32 x) { return __builtin_amdgcn_readfirstlane(x); }
struct Mul { f32x2 b, n; };
DI Mul mk(f32x2 b) { return Mul{b, f32x2{-b.y, b.y}}; }
DI f32x2 cmul(f32x2 a, Mul m) { return __builtin_elementwise_fma(a.yx, m.n, a * m.b.xx); }
DI float mul1(float a, float b) { float r; asm("v_mul_f32 %0, %1, %2" : "=v"(r) : "v"(a), "v"(b)); return r; }
DI float add1(float a, float b) { float r; asm("v_add_f32 %0, %1, %2" : "=v"(r) : "v"(a), "v"(b)); return r; }
DI float fma1(float a, float b, float c) { float r; asm("v_fma_f32 %0, %1, %2, %3" : "=v"(r) : "v"(a), "v"(b), "v"(c)); return r; }
DI float hi2f(u32 v) { return __uint_as_float(v & 0xffff0000u); }
DI float fopq(float x) { asm volatile("" : "+v"(x)); return x; }
DI float relu(float x) { return __int_as_float(max(__float_as_int(x), 0)); }
DI u32 sopq(u32 x) { asm volatile("" : "+s"(x)); return x; }
DI u32 opq(u32 x) { asm volatile("" : "+v"(x)); return x; }
#define MF32(a, b, c) __builtin_amdgcn_mfma_f32_32x32x16_bf16(__builtin_bit_cast(h16x8, a), __builtin_bit_cast(h16x8, b), c, 0, 0, 0)
#define MF16(a, b, c) __builtin_amdgcn_mfma_f32_16x16x32_bf16(__builtin_bit_cast(h16x8, a), __builtin_bit_cast(h16x8, b), c, 0, 0, 0)
#define DMA(voff, srd, soff, dst) \
  asm volatile("s_mov_b32 m0, %0\n\ts_nop 0\n\tbuffer_load_dwordx4 %1, %2, %3 offen lds" ::"s"(dst), "v"(voff), "s"(srd), "s"(soff) : "memory")
#define SETM0(base, off) asm volatile("s_add_u32 m0, %0, %1\n\ts_nop 0" ::"s"(base), "i"(off) : "memory", "scc")
#define DMAP(pol, voff, srd, soff, q) asm volatile("buffer_load_dwordx4 %0, %1, %2 offen offset:%3" pol " lds" ::"v"(voff), "s"(srd), "s"(soff), "i"((q) * 1024) : "memory")
#define DMAQ(voff, srd, soff, q) DMAP("", voff, srd, soff, q)
#define MMV(acc, a, b) asm volatile("v_mfma_f32_32x32x16_bf16 %0, %1, %2, %0" : "+v"(acc) : "v"(a), "v"(b) : "memory")
#define VMCNT(n) asm volatile("s_waitcnt vmcnt(%0)" ::"i"(n) : "memory")
#define LGKM0() asm volatile("s_waitcnt lgkmcnt(0)" ::: "memory")
#define BAR() asm volatile("s_waitcnt lgkmcnt(0)\n\ts_barrier" ::: "memory")
#define SB() __builtin_amdgcn_sched_barrier(0)
#define STI "s_memrealtime"
#define ST(i) do { if (!((STMASK >> (i)) & 1)) break; SB(); asm volatile(STI " %0\n\ts_waitcnt lgkmcnt(0)" : "=s"(stamp[(i) % 24])::"memory"); SB(); } while (0)

template <int SL = 1> DI void lds_wait(u32 a, u32 n) {
  _Pragma("unroll 1") for (u32 spin = 0; spin < (1u << 22); spin++) {
    asm volatile("" ::: "memory");
    if (__all(*(volatile LAS u32*)a >= n)) break;
    __builtin_amdgcn_s_sleep(SL);
  }
  asm volatile("" ::: "memory");
}
template <int V> struct IC { static constexpr int v = V; };
template <int I, int N, class Fn> DI void sfor(Fn&& f) { if constexpr (I < N) { f(IC<I>{}); sfor<I + 1, N>(f); } }
constexpr int step(int i) { return i < 28 ? i : i < 32 ? i - 16 : i < 44 ? i - 32 : i - 28; }
DI u32x4 lds128(u32 a, u32 o = 0) { return *(const LAS u32x4*)((LAS char*)a + o); }
DI s16x4 tr64(u32 a, u32 o) { return __builtin_amdgcn_ds_read_tr16_b64_v4i16((LAS s16x4*)((LAS char*)a + o)); }
DI u32x4 cat(s16x4 lo, s16x4 hi) { return __builtin_bit_cast(u32x4, __builtin_shufflevector(lo, hi, 0, 1, 2, 3, 4, 5, 6, 7)); }
DI u32x4 ldstr(u32 a, u32 o0, u32 o1) { return cat(tr64(a, o0), tr64(a, o1)); }

struct Srd { const void* p; u32 bytes, flags; };
struct Args {
  Srd x, w1, w2;
  char* ws;
  u16* out;
  const u16 *lar, *aim, *cre, *cim, *ldt, *dsk, *b1, *b2, *gw;
};
DI u32 colblock(u32 jj, u32 w2lo) { return (jj + CBO - (w2lo >> 7)) & 7; }
DI u64 mk64(u32 lo, u32 hi) { return (u64)lo | ((u64)hi << 32); }
#define STAMP_OUT() do { VMCNT(0); ST(23); if (b == 12 && wave == 0) { const u64 until = rt0 + 4000 + 10 * (stamp[0] - stamp[0]); for (u32 i_ = 0; i_ < (1u << 22); i_++) { if (__builtin_amdgcn_s_memrealtime() >= until) break; } } } while (0)

__global__ void __launch_bounds__(512, 2) fused_mix(Args) {
  __shared__ __attribute__((aligned(16))) char smem[LDS_BYTES];
  const u32 sb = (u32)(size_t)(LAS char*)smem;
  u64 stamp[24], rt0, rt1;
  for (int i = 0; i < 24; i++) stamp[i] = 0;
  asm volatile("s_memrealtime %0\n\ts_waitcnt lgkmcnt(0)" : "=s"(rt0)::"memory");
  ST(0);
  u32x16 k0, k1;
  u64 t64;
  u32 wave, vA[4], vB[4], r_b1a, r_b1b, ctag, tag, t_ldt, t_dsk, xbase, wbase, ts0, ts1;
  asm volatile(
      "s_load_dwordx16 s[52:67], %[ka], 64\n\t"
      "s_load_dwordx16 s[36:51], %[ka], 0\n\t"
      "v_readfirstlane_b32 %[wv], %[tid]\n\t"
      "s_getpc_b64 %[t64]\n.Lcode_start:\n\t"
      "v_and_b32 %[b0], 63, %[tid]\n\t"
      "s_lshr_b32 %[wv], %[wv], 6\n\t"
      "s_cmp_lt_u32 %[wv], 4\n\t"
      "s_cbranch_scc1 1f\n\t"
      "s_setprio 3\n\t"
      "v_and_b32 %[a3], 0xff, %[tid]\n\t"
      "s_lshr_b32 %[s0], %[b], 3\n\t"
      "v_lshlrev_b32 %[a0], 2, %[a3]\n\t"
      "v_lshl_or_b32 %[a0], %[s0], 10, %[a0]\n\t"
      "v_min_u32 %[a0], .Lcode_end - .Lcode_start - 4, %[a0]\n\t"
      "global_load_dword %[a0], %[a0], %[t64]\n\t"
      "s_lshl3_add_u32 %[s0], %[wv], 32\n\t"
      "s_load_dwordx2 %[t64], %[ka], %[s0]\n\t"
      "v_lshlrev_b32 %[a3], 1, %[a3]\n\t"
      "v_lshl_or_b32 %[a3], %[b], 9, %[a3]\n\t"
      "s_xor_b32 %[s1], %[b], %[pp]\n\t"
      "v_lshlrev_b32 %[a2], 3, %[b0]\n\t"
      "v_lshl_or_b32 %[a2], %[s1], 9, %[a2]\n\t"
      "s_lshl_b32 %[td], %[b], 2\n\t"
      "s_waitcnt lgkmcnt(0)\n\t"
      "global_load_ushort %[a0], %[a3], s[52:53]" PPOL "\n\t"
      "global_load_ushort %[a1], %[a3], s[54:55]" PPOL "\n\t"
      "global_load_dword %[a2], %[a2], %[t64]\n\t"
      "global_load_ushort %[a2], %[a3], s[56:57]" PPOL "\n\t"
      "global_load_ushort %[a3], %[a3], s[58:59]" PPOL "\n\t"
      "s_load_dword %[ct], s[48:49], 0\n\t"
      "s_load_dword %[tl], s[60:61], %[td]\n\t"
      "s_load_dword %[td], s[62:63], %[td]\n\t"
      "s_barrier\n\t"
      "s_branch 2f\n\t"
      "1:\n\t"
      "v_lshrrev_b32 %[b1], 4, %[b0]\n\t"
      "s_lshr_b32 %[s0], %[b], 6\n\t"
      "s_bfe_u32 %[s1], %[b], 0x30003\n\t"
      "v_xor_b32 %[a0], %[b0], %[b1]\n\t"
      "v_lshlrev_b32 %[a1], 7, %[b0]\n\t"
      "s_and_b32 %[xb], %[b], 6\n\t"
      "s_lshl1_add_u32 %[xb], %[xb], %[s0]\n\t"
      "v_and_b32 %[a0], 7, %[a0]\n\t"
      "v_and_b32 %[a1], 0x1c00, %[a1]\n\t"
      "s_lshl2_add_u32 %[xb], %[xb], %[wv]\n\t"
      "s_lshl_b32 %[xb], %[xb], 15\n\t"
      "v_lshl_or_b32 %[a0], %[a0], 4, %[a1]\n\t"
      "s_lshl1_add_u32 %[s0], %[s0], %[s1]\n\t"
      "v_xor_b32 %[a3], 64, %[a0]\n\t"
      "s_and_b32 %[tl], %[s0], 7\n\t"
      "v_add_u32 %[a2], 0x3800, %[a0]\n\t"
      "s_lshl_b32 %[td], %[tl], 7\n\t"
      "v_add_u32 %[a1], 0x1c00, %[a3]\n\t"
      "s_add_u32 %[td], %[td], %[xb]\n\t"
      "v_add_u32 %[a3], 0x5400, %[a3]\n\t"
      "s_lshl_b32 m0, %[wv], 12\n\t"
      "v_lshlrev_b32 %[b2], 2, %[b1]\n\t"
      "s_and_b32 %[wb], %[b], 1\n\t"
      "s_mul_i32 %[wb], %[wb], 7\n\t"
      "v_xor_b32 %[b2], %[b2], %[b0]\n\t"
      "s_add_u32 %[wb], %[wb], %[s1]\n\t"
      "v_and_b32 %[q0], 31, %[b0]\n\t"
      "v_and_b32 %[b2], 15, %[b2]\n\t"
      "v_mul_u32_u24 %[b1], 0xe00, %[b1]\n\t"
      "s_and_b32 %[ct], %[wv], 1\n\t"
      "s_lshl1_add_u32 %[ct], %[wb], %[ct]\n\t"
      "v_lshl_or_b32 %[b0], %[b2], 4, %[b1]\n\t"
      "v_lshlrev_b32 %[q0], 1, %[q0]\n\t"
      "s_lshl_b32 %[wb], %[wb], 8\n\t"
      "s_waitcnt lgkmcnt(0)\n\t"
      "s_barrier\n\t"
      "s_sleep %[fd]\n\t"
      "buffer_load_dwordx4 %[a0], s[36:39], %[td] offen offset:0" XPOL " lds\n\t"
      "buffer_load_dwordx4 %[a1], s[36:39], %[td] offen offset:1024" XPOL " lds\n\t"
      "buffer_load_dwordx4 %[a2], s[36:39], %[td] offen offset:2048" XPOL " lds\n\t"
      "buffer_load_dwordx4 %[a3], s[36:39], %[td] offen offset:3072" XPOL " lds\n\t"
      "s_load_dword %[ft], s[48:49], 0\n\t"
      "s_mul_i32 %[td], %[wv], 0xe000\n\t"
      "v_xor_b32 %[b1], 16, %[b0]\n\t"
      "v_xor_b32 %[b2], 32, %[b0]\n\t"
      "v_xor_b32 %[b3], 48, %[b0]\n\t"
      "s_add_u32 %[wb], %[wb], %[td]\n\t"
      "v_add_u32 %[b1], 0x3400, %[b1]\n\t"
      "v_add_u32 %[b2], 0x6800, %[b2]\n\t"
      "v_add_u32 %[b3], 0x9c00, %[b3]\n\t"
      "s_cmp_eq_u32 %[s1], 7\n\t"
      "s_cbranch_scc1 2f\n\t"
      "s_mul_i32 %[tl], %[tl], 0x38000\n\t"
      "s_addk_i32 m0, 0x4000\n\t"
      "s_add_u32 %[tl], %[tl], %[wb]\n\t"
      "buffer_load_dwordx4 %[b0], s[40:43], %[tl] offen offset:0 lds\n\t"
      "buffer_load_dwordx4 %[b1], s[40:43], %[tl] offen offset:1024 lds\n\t"
      "buffer_load_dwordx4 %[b2], s[40:43], %[tl] offen offset:2048 lds\n\t"
      "buffer_load_dwordx4 %[b3], s[40:43], %[tl] offen offset:3072 lds\n\t"
      "v_lshl_or_b32 %[q1], %[ct], 7, %[q0]\n\t"
      "s_add_u32 %[s0], %[s0], 1\n\t"
      "s_and_b32 %[s0], %[s0], 7\n\t"
      "s_mul_i32 %[s0], %[s0], 0x38000\n\t"
      "global_load_ushort %[q0], %[q1], s[64:65]\n\t"
      "global_load_ushort %[q1], %[q1], s[64:65] offset:64\n\t"
      "s_add_u32 m0, m0, 0x8000\n\t"
      "s_add_u32 %[s0], %[s0], %[wb]\n\t"
      "buffer_load_dwordx4 %[b0], s[40:43], %[s0] offen offset:0 lds\n\t"
      "buffer_load_dwordx4 %[b1], s[40:43], %[s0] offen offset:1024 lds\n\t"
      "buffer_load_dwordx4 %[b2], s[40:43], %[s0] offen offset:2048 lds\n\t"
      "buffer_load_dwordx4 %[b3], s[40:43], %[s0] offen offset:3072 lds\n\t"
      "2:"
      : "=&{s[36:51]}"(k0), "=&{s[52:67]}"(k1), [t64] "=&s"(t64), [wv] "=&s"(wave), [ct] "=&s"(ctag), [ft] "=&s"(tag), [tl] "=&s"(t_ldt),
        [td] "=&s"(t_dsk), [xb] "=&s"(xbase), [wb] "=&s"(wbase), [s0] "=&s"(ts0), [s1] "=&s"(ts1),
        [a0] "=&v"(vA[0]), [a1] "=&v"(vA[1]), [a2] "=&v"(vA[2]), [a3] "=&v"(vA[3]), [b0] "=&v"(vB[0]), [b1] "=&v"(vB[1]), [b2] "=&v"(vB[2]),
        [b3] "=&v"(vB[3]), [q0] "=&v"(r_b1a), [q1] "=&v"(r_b1b)
      : [ka] "s"(__builtin_amdgcn_kernarg_segment_ptr()), [tid] "v"(threadIdx.x), [b] "s"(blockIdx.x), [pp] "i"(PPAIR), [fd] "i"(FDELAY) : "scc", "memory");
  asm volatile("" : "+s"(k0), "+s"(k1), "+s"(wave), "+s"(xbase), "+s"(wbase));
  SB();
  char* const ws = (char*)mk64(k0[12], k0[13]);
  const u32 w = wave & 3;
  const u32 b = blockIdx.x, xcd = b & 7, bi = b >> 3;
  const u32 hf = xcd & 1, p = 4 * (xcd >> 1) + (bi >> 3), jj = bi & 7;
  const bool spare = jj == 7;
  const u32 cb1 = 7 * hf + jj, rb = 2 * p + hf, pair = b;
  const u32 wm = w >> 1, wn = w & 1;
#define LANES() const u32 tid = opq(threadIdx.x) & 255, lane = tid & 63, hh = lane >> 5, blk = (lane >> 4) & 1, q4 = (lane & 15) >> 2, p4 = lane & 3, g4 = lane >> 4, l16 = opq(lane * 16)
  const u32 rot = 2 * (bi >> 3) + jj;

  if (wave >= 4) {
    LANES();
    const u32 ta = opq(sb + TAB + tid * 224);
    const u32 t0 = opq(sb + TAB + (wm * 128 + 4 * g4 + q4) * 224 + 8 * p4), t1 = opq(t0 + 32 * wn);
    const u32 ko = opq(sb + KD + wm * KD_CH + (4 * g4 * 36 + 16 * wn + (lane & 15)) * 2);
    const __amdgpu_buffer_rsrc_t sWS = __builtin_amdgcn_make_buffer_rsrc((void*)ws, 0, WS_END, 0x00020000);
    u32 ga[9];
    {
      auto cell = [&](u32 pos) {
        const u32 hi = pos >> 8, ex = min(3u, 32u - (u32)__clz(hi));
        return sb + KD + (4 * ex + ((8 * hi) >> ex)) * 72 + ((pos & 255) >> 2);
      };
      const u32 P = 2040 - 8 * tid, a0 = cell(P), a8 = cell(P + 8);
      const u32 c0 = w == 3 ? 0x83222100u : w == 2 ? 0x88111000u : 0x88880000u, c1 = w == 3 ? 0x76543210u : w == 2 ? 0x88111000u : 0x88800000u;
      const u32 code = (w == 3 ? hh : (lane & 1)) ? c1 : c0;
#pragma unroll
      for (int k = 0; k < 8; k++) {
        const u32 n = (code >> (4 * k)) & 15u;
        ga[k + 1] = opq(n == 8 ? a8 : a0 + n * 72);
      }
      const u32 lowa = opq(P == 0 ? sb + KD + KD_ZERO : sb + KD + 7 * 72 + ((P - 8) >> 2));
      ga[0] = opq(P > 256 ? a0 : lowa);
    }
    *(LAS u32*)(hh ? sb + CNT + (lane & 15) * 4 : sb + KD + (lane & 1) * KD_CH + KD_ZERO) = 0;
    const u32 to = opq(sb + TC + tid * 16);
    const u32 ur = opq(sb + hf * 8192 + (lane & 7) * 1024 + (((lane >> 3) ^ (4 * (lane & 1))) << 4) + 4 * w);
    const u32 uw = opq(WS_UT + ((rot & 7) * 64 + (lane >> 3) * 8 + 2 * w) * UTS + rb * 128 + (lane & 7) * 16);

    asm volatile("s_waitcnt lgkmcnt(0)" : "+s"(ctag), "+s"(t_ldt), "+s"(t_dsk)::"memory");
    asm volatile("s_waitcnt vmcnt(0)" : "+v"(vA[0]), "+v"(vA[1]), "+v"(vA[2]), "+v"(vA[3])::"memory");
    const u32 r_lar = vA[0], r_aim = vA[1], r_cre = vA[2], r_cim = vA[3];
    const u32 tag = ctag + 1, cb2 = colblock(jj, k0[8]);
    ST(2);
    {
      const float dt = rbf(__expf(bf2f(wm ? t_ldt >> 16 : t_ldt & 0xffffu)));
      const float er = -(rbf(__expf(bf2f(r_lar))) * dt) * 1.44269504088896f;
      const float wi = (bf2f(r_aim) * dt) * 0.159154943091895f;
      auto seed = [&](float t) {
        const float m = __builtin_amdgcn_exp2f(er * t), a = __builtin_amdgcn_fractf(wi * t);
        return f32x2{m * __builtin_amdgcn_cosf(a), m * __builtin_amdgcn_sinf(a)};
      };
      auto put8 = [&](const f32x2* v, u32 off) {
        u32x4 re = {pk2(v[0].x, v[1].x), pk2(v[2].x, v[3].x), pk2(v[4].x, v[5].x), pk2(v[6].x, v[7].x)};
        u32x4 im = {pk2(v[0].y, v[1].y), pk2(v[2].y, v[3].y), pk2(v[4].y, v[5].y), pk2(v[6].y, v[7].y)};
        *(LAS u32x4*)((LAS char*)ta + off) = re;
        *(LAS u32x4*)((LAS char*)ta + 112 + off) = im;
      };
      f32x2 v[8];
      const Mul w8 = mk(seed(8.f)), w64 = mk(seed(64.f));
      v[0] = f32x2{1.f, 0.f}; v[1] = w8.b; v[2] = cmul(v[1], w8);
      const Mul w16 = mk(v[2]);
#pragma unroll
      for (int k = 3; k < 8; k++) v[k] = cmul(v[k - 2], w16);
      put8(v, 0); SB();
      v[0] = w64.b;
#pragma unroll
      for (int r = 1; r < 4; r++) {
#pragma unroll
        for (int k = (r == 1); k < 8; k++) v[k] = cmul(v[k], w64);
        put8(v, 16 * r); SB();
      }
      f32x2 s1 = seed(1.f), s256 = seed(256.f);
      s1.y = -s1.y; s256.y = -s256.y;
      const Mul v1 = mk(s1), W = mk(s256), v2c = mk(cmul(s1, v1));
      f32x2 u[8];
      u[0] = f32x2{2.f * (bf2f(r_cre) * dt), -2.f * (bf2f(r_cim) * dt)};
      u[1] = cmul(u[0], v1);
#pragma unroll
      for (int k = 2; k < 8; k++) u[k] = cmul(u[k - 2], v2c);
      put8(u, 64); SB();
      v[0] = cmul(u[0], W); v[1] = cmul(u[2], W); v[2] = cmul(u[4], W); v[3] = cmul(u[6], W);
      v[4] = cmul(v[0], W); v[5] = cmul(v[2], W); v[6] = cmul(v[4], W); v[7] = cmul(v[5], W);
      u[0] = cmul(v[6], W); u[1] = cmul(u[0], W); u[2] = cmul(u[1], W); u[3] = cmul(u[2], W); u[4] = cmul(u[3], W);
      put8(v, 80);
      u[5] = u[6] = u[7] = f32x2{0.f, 0.f};
      put8(u, 96);
      ST(3);
      BAR();
      ST(14);
      f32x4 ka[2] = {{(lane == 0 && wn == 0) ? bf2f(wm ? t_dsk >> 16 : t_dsk & 0xffffu) : 0.f, 0.f, 0.f, 0.f}, {0.f, 0.f, 0.f, 0.f}};
      u32x4 fv[8], f0[8], f1[8];
#pragma unroll
      for (int ks = 0; ks < 10; ks++) {
        const u32 o = ks * 16 * 224;
        if (ks < 8) { fv[ks] = ldstr(t1, o, o + 112); f0[ks] = ldstr(t0, o + 64, o + 64 + 112); f1[ks] = ldstr(t0, o + 96, o + 96 + 112); }
        if (ks >= 2) { ka[0] = MF16(f0[ks - 2], fv[ks - 2], ka[0]); ka[1] = MF16(f1[ks - 2], fv[ks - 2], ka[1]); }
        SB();
      }
#pragma unroll
      for (int rt = 0; rt < 2; rt++)
#pragma unroll
        for (int r = 0; r < 4; r += 2) {
          const u32 pr = pk2(ka[rt][r], ka[rt][r + 1]);
          *(LAS u16*)((LAS char*)ko + (16 * rt + r) * 72) = (u16)pr;
          *(LAS u16*)((LAS char*)ko + (16 * rt + r + 1) * 72) = (u16)(pr >> 16);
        }
    }
    ST(9);
    BAR();
    ST(4);
    {
      u32 xr[8];
#pragma unroll
      for (int j = 0; j < 8; j++) xr[j] = *(const LAS u32*)((LAS char*)(ur ^ ((j >> 1) << 4)) + j * 128);
#pragma unroll
      for (int c = 0; c < 2; c++) {
        u32x4 ou;
#pragma unroll
        for (int e = 0; e < 4; e++) ou[e] = __builtin_amdgcn_perm(xr[2 * e + 1], xr[2 * e], c ? 0x07060302 : 0x05040100);
        __builtin_amdgcn_raw_buffer_store_b128(ou, sWS, uw + c * UTS, 0, 16);
      }
    }
    ST(10);
    if (!spare) BAR();
    const u32 ufo = opq(WS_UF + (pair >> 5) * 128 + (lane & 31) * 4);
    const u32 ua = opq(sb + US + wm * US_STR + (256 - 16 - 128 * wn - 96 + 16 * (lane & 15) + 8 * g4) * 2);
    u32 tb[4];
#pragma unroll
    for (int j = 0; j < 4; j++) tb[j] = opq(sb + TC + (2 * wm + (lane & 1)) * TC_STR + (2046 - 16 - 128 * wn - 96 - 1792 + 8 * g4 - (lane & 14)) * 2 + 1024 * j);
    const u32 row = tid >> 3, ck = lane & 7;
    const u32 ro = opq(sb + RED + w * 6144 + l16), r1 = opq(sb + RED + (w ^ 1) * 6144 + l16), r2 = opq(sb + RED + (w ^ 2) * 6144 + l16), r3 = opq(sb + RED + (w ^ 3) * 6144 + l16);
    const u32 ao = opq(WS_AT + pair * ATS + (8 * w + g4) * 512 + (lane & 15) * 16);
    const u32 sa = opq(sb + 2 * SLOT + ck * 1024 + ((row ^ (2 * ck)) << 4));
    const u32 ato = opq(WS_AT + (32 * cb2 + 8 * w + hh) * ATS + rb * 512 + (lane & 31) * 16);
    const u32 go = opq(WS_G + rb * 512 + row * 16);
    const u32 oo = opq((64 * rb + row) * 1024 + cb2 * 128 + ck * 16);
    const u32 cw = opq(sb + CNT + 4 * w), cr = opq(sb + CNT + 4 * (lane & 3));
    const u32 aw = opq(sb + TC + (lane & 31) * 144 + (8 * w + hh) * 4), ar = opq(sb + TC + row * 144 + ck * 16);
    auto arrive = [&](u32 n) { *(volatile LAS u32*)cw = n; };
    auto meet = [&](u32 n) { LGKM0(); arrive(n); lds_wait(cr, n); };
    auto pub_u = [&]() { if (tid == 0) asm volatile("buffer_store_dword %0, %1, %2, 0 offen sc1" ::"v"(tag), "v"(WS_UF + (rot & 7) * 128 + rb * 4), "s"(sWS) : "memory"); };
    if (!spare) {
      for (int i = 2; i < UPUB; i++) BAR();
      VMCNT(0);
      BAR();
      pub_u();
      ST(5);
    } else {
      u32x4 gwv, xr[16];
      asm volatile("global_load_dwordx4 %0, %1, %2" : "=v"(gwv) : "v"(l16), "s"(((const CAS Args*)__builtin_amdgcn_kernarg_segment_ptr())->gw));
#pragma unroll
      for (int j = 0; j < 16; j++)
        asm volatile("buffer_load_dwordx4 %0, %1, %2, %3 offen offset:%4" : "=v"(xr[j]) : "v"(l16), "s"(k0.s0123), "s"((64 * rb + 16 * w + (j & ~3)) * 1024), "i"((j & 3) * 1024));
      arrive(1);
      asm volatile("s_waitcnt vmcnt(0)" : "+v"(gwv), "+v"(xr[0]), "+v"(xr[1]), "+v"(xr[2]), "+v"(xr[3]), "+v"(xr[4]), "+v"(xr[5]), "+v"(xr[6]), "+v"(xr[7]),
                   "+v"(xr[8]), "+v"(xr[9]), "+v"(xr[10]), "+v"(xr[11]), "+v"(xr[12]), "+v"(xr[13]), "+v"(xr[14]), "+v"(xr[15])::"memory");
      meet(2);
      pub_u();
      ST(5);
      const u32 zs = sb + TC + w * TC_STR;
#pragma unroll
      for (int j = 0; j < 16; j++) {
        float z = 0.f;
#pragma unroll
        for (int e = 0; e < 4; e++) z = dot2(xr[j][e], gwv[e], z);
        *(LAS float*)((LAS char*)(zs + lane * 4) + j * 260) = z;
      }
      LGKM0();
      float zz = 0.f;
#pragma unroll
      for (int k = 0; k < 16; k++) zz += *(const LAS float*)((LAS char*)(zs + (lane & 15) * 260 + g4 * 64) + k * 4);
      zz += __shfl_xor(zz, 16); zz += __shfl_xor(zz, 32);
      const float g = rbf(1.f / (1.f + __expf(-zz)));
      if (lane < 16) __builtin_amdgcn_raw_buffer_store_b64(u32x2{pk2(g, 1.f - g), tag}, sWS, WS_G + rb * 512 + (wn * 16 + lane) * 16 + wm * 8, 0, 16);
      meet(3);
    }
    if (w < 2) *(LAS u32x4*)(w == 0 ? sb + US + hh * US_STR + (lane & 31) * 16 : sb + TC + ((lane >> 1) & 3) * TC_STR + 4096 + (lane & 1) * 16) = u32x4{0, 0, 0, 0};
    auto fetch_u = [&]() {
#pragma unroll
      for (int q = 0; q < 8; q++) {
        if (!(q & 3)) SETM0(sb, US + 512 + (q >> 2) * US_STR);
        DMAP(" sc1", l16, sWS, WS_UT + (2 * pair + (q >> 2)) * UTS, q & 3);
      }
    };
    u32 ek[2][9];
    auto exp_rd = [&]() {
#pragma unroll
      for (int i = 0; i < 9; i++) { ek[0][i] = *(const LAS u16*)ga[i]; ek[1][i] = *(const LAS u16*)((LAS char*)ga[i] + KD_CH); SB(); asm volatile("s_nop 3"); SB(); }
    };
    auto exp_wr = [&](int i) {
      const u32* e = ek[i >> 1] + (i & 1);
      *(LAS u32x4*)((LAS char*)to + i * TC_STR) = u32x4{e[7] | (e[6] << 16), e[5] | (e[4] << 16), e[3] | (e[2] << 16), e[1] | (e[0] << 16)};
    };
    bool got = w != 0;
    if (!spare) {
      if (EXPE) exp_rd();
      BAR();
      if (EXPE) { exp_wr(0); SB(); __builtin_amdgcn_s_sleep(2); SB(); exp_wr(1); }
      BAR();
#pragma unroll
      for (int i = 0; i < 2; i++) {
        if (EXPE) exp_wr(2 + i);
        if (got || i < UTRY) {
          BAR(); BAR();
        } else {
          u32 uf;
          asm volatile("buffer_load_dword %0, %1, %2, 0 offen sc1\n\ts_barrier\n\ts_barrier\n\ts_waitcnt vmcnt(0)" : "=&v"(uf) : "v"(ufo), "s"(sWS) : "memory");
          got = __all(uf == tag);
          if (got) fetch_u();
        }
      }
    }
    __builtin_amdgcn_s_setprio(CVPRIO);
    ST(21);
    if (!EXPE || spare) { exp_rd(); exp_wr(0); exp_wr(1); exp_wr(2); exp_wr(3); }
    if (!got) {
      __builtin_amdgcn_s_setprio(3);
      _Pragma("unroll 1") for (u32 spin = 0; spin < SPIN_CAP; spin++) {
        u32 uf;
        asm volatile("buffer_load_dword %0, %1, %2, 0 offen sc1\n\ts_waitcnt vmcnt(0)" : "=&v"(uf) : "v"(ufo), "s"(sWS) : "memory");
        if (__all(uf == tag)) break;
        __builtin_amdgcn_s_sleep(USLEEP);
      }
      fetch_u();
      *(volatile LAS u32*)(sb + GO + 4) = 1;
      __builtin_amdgcn_s_setprio(CVPRIO);
    }
    VMCNT(0);
    ST(6);
    meet(4);
    ST(7);

    f32x4 ca[8];
#pragma unroll
    for (int c = 0; c < 8; c++) ca[c] = f32x4{0.f, 0.f, 0.f, 0.f};
    u32x4 fu[2][8];
    u32x2 ft[2][16];
    auto cread = [&](int f, int r) {
      const int k = r < 2 ? 8 + r : r < 10 ? r - 2 : r;
      if (k < 8) fu[f & 1][k] = lds128(ua, (96 - 32 * f + 256 * k) * 2);
      else {
        const int i = (k - 8) >> 1, hl = k & 1, e = 7 - i;
        const LAS u32* t = (const LAS u32*)((LAS char*)tb[e >> 1] + (96 - 32 * f + 256 * (e & 1)) * 2 + 8 * hl);
        ft[f & 1][2 * i + hl] = u32x2{t[0], t[1]};
      }
    };
#pragma unroll
    for (int r = 0; r < 24; r++) { cread(0, r); SB(); }
#pragma unroll
    for (int f = 0; f < 4; f++) {
      int m = 0;
#pragma unroll
      for (int i = 0; i < 8; i++)
#pragma unroll
        for (int e = 0; e + i < 8; e++, m++) {
          const u32x4 tf = {ft[f & 1][2 * i][0], ft[f & 1][2 * i][1], ft[f & 1][2 * i + 1][0], ft[f & 1][2 * i + 1][1]};
          ca[i + e] = MF16(fu[f & 1][e], tf, ca[i + e]);
          if (f < 3 && m >= 4 && m < 28) cread(f + 1, m - 4);
          if (f == 3 && m == 0) arrive(5);
          SB();
        }
    }
    ST(8);
    f32x4 keep[2];
    __builtin_amdgcn_s_setprio(3);
    lds_wait(cr, 5);
#define SEND(W) { _Pragma("unroll") for (int j = 1; j < 4; j++) { *(LAS f32x4*)((LAS char*)ro + (j - 1) * 2048) = ca[2 * (W ^ j)]; *(LAS f32x4*)((LAS char*)ro + (j - 1) * 2048 + 1024) = ca[2 * (W ^ j) + 1]; } \
                  keep[0] = ca[2 * W]; keep[1] = ca[2 * W + 1]; asm volatile("; send " #W ::: "memory"); }
    if (w == 0) SEND(0) else if (w == 1) SEND(1) else if (w == 2) SEND(2) else SEND(3)
    asm volatile("" ::: "memory");
    arrive(6);
    lds_wait(cr, 6);
    const u32 sel = wm ? 0x01000302 : 0x03020100;
    f32x4 pp[2][3];
#pragma unroll
    for (int c = 0; c < 2; c++) {
      pp[c][0] = *(const LAS f32x4*)((LAS char*)r1 + c * 1024);
      pp[c][1] = *(const LAS f32x4*)((LAS char*)r2 + 2048 + c * 1024);
      pp[c][2] = *(const LAS f32x4*)((LAS char*)r3 + 4096 + c * 1024);
    }
#pragma unroll
    for (int c = 0; c < 2; c++) {
      u32 o[4];
#pragma unroll
      for (int r = 0; r < 4; r++) {
        const u32 v = pk2(add1(keep[c][r], pp[c][0][r]), add1(pp[c][1][r], pp[c][2][r]));
        o[r] = __builtin_amdgcn_perm(v, v, sel);
      }
#pragma unroll
      for (int r = 0; r < 2; r++) __builtin_amdgcn_raw_buffer_store_b128(u32x4{o[r], tag, o[r + 2], tag}, sWS, ao + c * 2048 + r * 256, 0, 16);
    }
    arrive(7);
    ST(11);
    const __amdgpu_buffer_rsrc_t sB2 = __builtin_amdgcn_make_buffer_rsrc((void*)mk64(k1[14], k1[15]), 0, D * 2, 0x00020000);
    const __amdgpu_buffer_rsrc_t sOut = __builtin_amdgcn_make_buffer_rsrc((void*)mk64(k0[14], k0[15]), 0, L * D * 2, 0x00020000);
    const u32x4 b2v = __builtin_amdgcn_raw_buffer_load_b128(sB2, ck * 16, cb2 * 128, 0);
    lds_wait<LSL>(cr + RDY - CNT, 1);
    ST(12);
    u32x4 gg, at[4];
    _Pragma("unroll 1") for (u32 spin = 0; spin < SPIN_CAP; spin++) {
      asm volatile("" ::: "memory");
#pragma unroll
      for (int j = 0; j < 4; j++) at[j] = __builtin_amdgcn_raw_buffer_load_b128(sWS, ato, 2 * j * ATS, 16);
      gg = __builtin_amdgcn_raw_buffer_load_b128(sWS, go, 0, 16);
      bool ok = gg[1] == tag && gg[3] == tag;
#pragma unroll
      for (int j = 0; j < 4; j++) ok &= at[j][1] == tag && at[j][3] == tag;
      if (__all(ok)) break;
      __builtin_amdgcn_s_sleep(RSLEEP);
    }
    __builtin_amdgcn_s_setprio(TPRIO);
    ST(13);
    lds_wait(cr, 7);
#pragma unroll
    for (int j = 0; j < 4; j++) {
      *(LAS u32*)((LAS char*)aw + j * 8) = at[j][0];
      *(LAS u32*)((LAS char*)aw + j * 8 + 32 * 144) = at[j][2];
    }
    asm volatile("" ::: "memory");
    arrive(8);
    lds_wait(cr, 8);
    ST(15);
#pragma unroll
    for (int t = 0; t < 2; t++) {
      const float g = bf2f(gg[2 * t] & 0xffffu), gm = hi2f(gg[2 * t]);
      const u32x4 av = lds128(ar, t * 32 * 144);
      float base[8];
#pragma unroll
      for (int i = 0; i < 4; i++) {
        base[2 * i] = fopq(fma1(gm, bf2f(b2v[i] & 0xffffu), mul1(g, bf2f(av[i] & 0xffffu))));
        base[2 * i + 1] = fopq(fma1(gm, hi2f(b2v[i]), mul1(g, hi2f(av[i]))));
      }
      if (t == 0) { ST(16); lds_wait<LSL>(cr + RDY - CNT, 2); } else { ST(18); asm volatile("s_barrier" ::: "memory"); ST(19); }
      f32x4 pt[4][2];
#pragma unroll
      for (int k = 0; k < 4; k++)
#pragma unroll
        for (int q = 0; q < 2; q++) pt[k][q] = *(const LAS f32x4*)((LAS char*)sa + (1 - t) * SLOT + k * 8192 + q * 512);
      u32x4 ow;
#pragma unroll
      for (int i = 0; i < 4; i++) {
        float o[2];
#pragma unroll
        for (int e = 0; e < 2; e++) {
          const int q = i >> 1, r = 2 * (i & 1) + e;
          o[e] = t ? __builtin_fmaf(gm, (pt[0][q][r] + pt[1][q][r]) + (pt[2][q][r] + pt[3][q][r]), base[2 * i + e])
                   : fma1(gm, add1(add1(pt[0][q][r], pt[1][q][r]), add1(pt[2][q][r], pt[3][q][r])), base[2 * i + e]);
        }
        ow[i] = pk2(o[0], o[1]);
      }
      __builtin_amdgcn_raw_buffer_store_b128(ow, sOut, oo + t * 32768, 0, OUTPOL);
      SB();
      if (t == 0) ST(17);
    }
    asm volatile(".Lcode_end:");
    STAMP_OUT();
    return;
  }

  ST(1);
  LANES();
  u32 v2[4];
#pragma unroll
  for (int q = 0; q < 4; q++) v2[q] = opq((lane >> 3) * 1024 + (((lane & 7) ^ (blk << 2)) << 4) + q * (8192 - 1024));
  const u32 m4 = sopq(sb + w * 4096), m8 = sopq(sb + w * 8192);
  const u32x4 sX = k0.s0123, sW1 = k0.s4567, sW2 = k0.s89ab;
  const u32 w2base = sopq(28 * w * 16384 + colblock(jj, sW2[0]) * 128);
  asm volatile("s_waitcnt lgkmcnt(0)" : "+s"(tag));
  tag += 1;
  const __amdgpu_buffer_rsrc_t sWS = __builtin_amdgcn_make_buffer_rsrc((void*)ws, 0, WS_END, 0x00020000);
  auto w2piece = [&](int c, int q) {
    if ((q & 3) == 0) SETM0(m8, (c & 3) * SLOT + (q >> 2) * 4096);
    DMAQ(v2[q & 3], sW2, w2base + c * 65536 + (q >> 2) * 32768, q & 3);
  };

  if (!spare) {
    auto g1piece = [&](int g, int q) {
      if (q == 0) SETM0(m4, (g & 3) * SLOT);
      if (q == 4) SETM0(m4, (g & 3) * SLOT + 16384);
      if (q < 4) DMAP(XPOL, vA[q], sX, xbase + ((g + rot) & 7) * 128, q);
      else DMAQ(vB[q - 4], sW1, wbase + ((g + rot) & 7) * 64 * (F * 2), q - 4);
    };
    u32 ax[2][4], aw[2][4];
    {
      const u32 xa = sb + (64 * wm + (lane & 31)) * 128 + ((hh ^ ((lane >> 1) & 7)) << 4);
      const u32 wa = sb + 16384 + 256 * (8 * hh + q4) + 8 * (p4 & 1) + (((8 * wn + 2 * blk + (p4 >> 1)) ^ ((q4 << 2) | (2 * hh))) << 4);
#pragma unroll
      for (int k = 0; k < 4; k++) {
        ax[0][k] = opq(xa ^ (k << 5)); aw[0][k] = opq(wa ^ ((k & 1) << 4) ^ ((k >> 1) << 6));
        ax[1][k] = opq(ax[0][k] + 2 * SLOT); aw[1][k] = opq(aw[0][k] + 2 * SLOT);
      }
    }
    const u32 ho = opq(l16 + WS_H + ((4 * p + 2 * wm) * 112 + cb1 * 8 + wn * 4) * 1024), ho1 = opq(ho + 112 * 1024);
#pragma unroll
    for (int q = 0; q < 8 * LA - 12; q++) {
      if (q == PRET) { asm volatile("s_barrier" ::: "memory"); ST(3); }
      g1piece(q < 4 ? 1 : 2, q < 4 ? q : q - 4);
    }
    if (PRET >= 8 * LA - 12) { asm volatile("s_barrier" ::: "memory"); ST(3); }
    asm volatile("s_waitcnt vmcnt(%2)" : "+v"(r_b1a), "+v"(r_b1b) : "i"(8 * LA - 8));
    f32x16 acc[2][2];
    {
      const u32x4 one = {hh ? 0u : 0x3f80u, 0u, 0u, 0u};
      const u32x4 ba = {r_b1a, 0u, 0u, 0u}, bb = {r_b1b, 0u, 0u, 0u};
      asm volatile("s_nop 1\n\tv_mfma_f32_32x32x16_bf16 %0, %4, %6, 0\n\tv_mfma_f32_32x32x16_bf16 %1, %4, %6, 0\n\t"
                   "v_mfma_f32_32x32x16_bf16 %2, %5, %6, 0\n\tv_mfma_f32_32x32x16_bf16 %3, %5, %6, 0"
                   : "=&v"(acc[0][0]), "=&v"(acc[0][1]), "=&v"(acc[1][0]), "=&v"(acc[1][1]) : "v"(ba), "v"(bb), "v"(one));
    }
    u32x4 fx[2][2];
    s16x4 fw[2][4];
    ST(2);
    BAR();
    ST(4);
    fx[0][0] = lds128(ax[0][0]); fw[0][0] = tr64(aw[0][0], 0); fw[0][1] = tr64(aw[0][1], 1024);
    fx[0][1] = lds128(ax[0][0], 4096); fw[0][2] = tr64(aw[0][2], 0); fw[0][3] = tr64(aw[0][3], 1024);
#pragma unroll
    for (int s = 0; s < 8; s++) {
      auto piece = [&](int q) { if (s + LA < 8) g1piece(s + LA, q); else if (s + LA - 8 < WPRE) w2piece(s + LA - 8, q); };
#pragma unroll
      for (int k = 0; k < 4; k++) {
        const int c = k & 1, n = c ^ 1, kn = (k + 1) & 3, sl = (k == 3 ? s + 1 : s) & 3, hi = sl >> 1;
        const u32 o = (sl & 1) * SLOT, ow = o + kn * 4096;
        const bool more = s < 7 || k < 3;
        MMV(acc[0][0], cat(fw[c][0], fw[c][1]), fx[c][0]);
        if (k == 3) {
          if (s < 7) VMCNT((LA == 3 && s + 2 < 8 + WPRE ? 8 : 0) + (s + LA < 8 + WPRE ? 6 : 0));
          BAR();
        }
        if (more) { fx[n][0] = lds128(ax[hi][kn], o); fw[n][0] = tr64(aw[hi][0], ow); fw[n][1] = tr64(aw[hi][1], ow + 1024); }
        MMV(acc[0][1], cat(fw[c][0], fw[c][1]), fx[c][1]);
        if (more) { fx[n][1] = lds128(ax[hi][kn], o + 4096); fw[n][2] = tr64(aw[hi][2], ow); }
        piece(2 * k);
        MMV(acc[1][0], cat(fw[c][2], fw[c][3]), fx[c][0]);
        if (more) fw[n][3] = tr64(aw[hi][3], ow + 1024);
        MMV(acc[1][1], cat(fw[c][2], fw[c][3]), fx[c][1]);
        piece(2 * k + 1);
      }
      if (s == 0) ST(8);
      if (s == 1) ST(15);
      if (s == 2) ST(16);
      if (s == 3) ST(17);
      if (s == 4) ST(19);
      if (s == 5) ST(20);
      if (s == 6) ST(21);
    }
    asm volatile("s_nop 7\n\ts_nop 7" : "+v"(acc[0][0]), "+v"(acc[0][1]), "+v"(acc[1][0]), "+v"(acc[1][1]));
    __builtin_amdgcn_s_setprio(2);
    ST(5);
#pragma unroll
    for (int u = 0; u < 2; u++)
#pragma unroll
      for (int t = 0; t < 2; t++)
#pragma unroll
        for (int s = 0; s < 2; s++) {
          u32x4 o;
#pragma unroll
          for (int e = 0; e < 4; e++) {
            const float a0 = relu(acc[u][t][8 * s + 2 * e]), a1 = relu(acc[u][t][8 * s + 2 * e + 1]);
            o[e] = EPK ? pk2(a0 * a0, a1 * a1) : pk2(mul1(a0, a0), mul1(a1, a1));
          }
          __builtin_amdgcn_raw_buffer_store_b128(o, sWS, (t ? ho1 : ho) + (2 * u + s) * 1024, 0, 16);
          SB();
        }
  } else {
    SETM0(m8, SLOT);
    auto pull = [&](u32 n) {
      const u32 u = n < 4 ? min(14 * xcd + 4 * n + (bi >> 3), 111u) : 28 * (bi >> 3) + 4 * PFC + n - 4;
#pragma unroll
      for (int q = 0; q < 4; q++) DMAQ(l16, sW2, u * 16384 + w * 4096, q);
    };
    __builtin_amdgcn_s_sleep(PFDELAY);
    pull(0); pull(1); pull(2);
    asm volatile("s_waitcnt vmcnt(12)\n\ts_barrier\n\ts_barrier" ::: "memory");
    ST(2);
    lds_wait(sb + CNT + 4 * (lane & 3), 1);
    ST(8);
    _Pragma("unroll 1") for (u32 n = 3; n < NPF; n++) pull(n);
    ST(4);
#pragma unroll
    for (int i = 0; i < 8 * WPRE; i++) w2piece(i >> 3, i & 7);
    lds_wait(sb + GO + 4, 1);
  }
  SB();
  ST(6);

  u32 w2a[2][2];
  {
    const u32 a = m8 + 128 * (4 * hh + q4) + 8 * (p4 & 1) + (((2 * blk + (p4 >> 1)) ^ ((q4 >> 1) << 2)) << 4);
    w2a[0][0] = opq(a); w2a[1][0] = opq(a ^ 64); w2a[0][1] = opq(a + 2 * SLOT); w2a[1][1] = opq((a ^ 64) + 2 * SLOT);
  }
  auto w2frag = [&](int st, int v) {
    const int sl = (st >> 2) & 3;
    const u32 o = (sl & 1) * SLOT + (st & 3) * 2048;
    return ldstr(w2a[v][sl >> 1], o, o + 1024);
  };
  u32x4 w2r[12][2];
  u32 fl = 0;
  const u32 fo = opq(WS_HF + p * 128 + (((lane & 7) == 7) ? 0 : (lane & 15) * 4));
#pragma unroll
  for (int c = WPRE; c < 8; c++) {
    if (c == WPRE + PUBAT) {
      VMCNT(8 * PUBAT);
      *(volatile LAS u32*)(sb + FD + 4 * w) = 1;
      if (w == 0 && !spare) {
        lds_wait(sb + FD + 4 * (lane & 3), 1);
        if (lane == 0) asm volatile("buffer_store_dword %0, %1, %2, 0 offen sc1" ::"v"(tag), "v"(WS_HF + p * 128 + (8 * hf + jj) * 4), "s"(sWS) : "memory");
      }
      ST(7);
      __builtin_amdgcn_s_sleep(WDELAY);
    }
    if (c >= 4 && c < 7) {
      VMCNT(24);
#pragma unroll
      for (int j = 0; j < 8; j++) w2r[4 * (c - 4) + (j >> 1)][j & 1] = w2frag(4 * (c - 4) + (j >> 1), j & 1);
      LGKM0();
    }
    if (c == POLLAT) asm volatile("s_cmp_lg_u32 %3, 3\n\ts_cbranch_scc1 1f\n\tbuffer_load_dword %0, %1, %2, 0 offen sc1\n1:" : "+v"(fl) : "v"(fo), "s"(sWS), "s"(w) : "memory", "scc");
    if (c < 7) {
#pragma unroll
      for (int q = 0; q < 8; q++) w2piece(c, q);
    }
  }
  const u32 hb = sopq(WS_H + (2 * rb * 112 + 28 * w) * 1024);
  const u32 xw = opq(m8 + 2 * SLOT + l16), rdw = opq(sb + RDY + 4 * w);
  asm volatile("s_waitcnt vmcnt(%1)" : "+v"(fl) : "i"(8 * (7 - POLLAT)) : "memory");
  ST(11);
  if (w == 3) {
    _Pragma("unroll 1") for (u32 spin = 0; spin < SPIN_CAP; spin++) {
      if (__all(fl == tag)) break;
      __builtin_amdgcn_s_sleep(HSLEEP);
      asm volatile("buffer_load_dword %0, %1, %2, 0 offen sc1\n\ts_waitcnt vmcnt(0)" : "=&v"(fl) : "v"(fo), "s"(sWS) : "memory");
    }
    *(volatile LAS u32*)(sb + GO) = 1;
  } else {
    lds_wait(sb + GO, 1);
  }
  __builtin_amdgcn_s_setprio(G2PRIO);
  ST(10);
  SB();

  u32x4 hfr[56];
  auto load_h = [&](int i) {
    const int st = step(i);
    hfr[i] = __builtin_amdgcn_raw_buffer_load_b128(sWS, l16 + (st & 3) * 1024, hb + ((i / 28) * 112 + (st & ~3)) * 1024, 16);
  };
#pragma unroll
  for (int i = 0; i < HEAD; i++) load_h(i);
  SB();
  f32x16 oc[2][2] = {};
  auto send = [&](int t, int j) {
    const f32x16& s = oc[t][j >> 2];
    const int r = 4 * (j & 3);
    *(LAS f32x4*)((LAS char*)(xw ^ (j << 5)) + (1 - t) * SLOT + j * 1024) = f32x4{s[r], s[r + 1], s[r + 2], s[r + 3]};
  };
  auto signal = [&](u32 n) { asm volatile("" ::: "memory"); *(volatile LAS u32*)rdw = n; };
  u32x4 wl[56][2];
  auto fetch_w = [&](int i) { if (i < 56 && step(i) >= 12) { wl[i][0] = w2frag(step(i), 0); wl[i][1] = w2frag(step(i), 1); } };
  sfor<0, 56>([&](auto ic) {
    constexpr int i = decltype(ic)::v, t = i / 28, st = step(i);
    constexpr int D2 = 53 - TAILN;
    if (i < D2 || !TAILN) fetch_w(i + 1); else { fetch_w(2 * i - D2 + 1); fetch_w(2 * i - D2 + 2); }
    SB();
    u32x4 a0, a1;
    if (st < 12) { a0 = w2r[st < 12 ? st : 0][0]; a1 = w2r[st < 12 ? st : 0][1]; } else { a0 = wl[i][0]; a1 = wl[i][1]; }
    oc[t][0] = MF32(a0, hfr[i], oc[t][0]);
    if (i + HEAD < 56) load_h(i + HEAD);
    SB();
    if (i < 56 - TAILN) oc[t][1] = MF32(a1, hfr[i], oc[t][1]);
    if (i == ATAT) signal(1);
    if (i >= 31 && i < 39) send(0, i - 31);
    if (i == 38) signal(2);
    SB();
    if (i == 27) ST(18);
  });
#pragma unroll
  for (int i = 0; i < TAILN; i++) {
    oc[1][1] = MF32(wl[56 - TAILN + i][1], hfr[56 - TAILN + i], oc[1][1]);
    SB();
    if (i >= 1 && i < 3) { send(1, 2 * i - 2); send(1, 2 * i - 1); }
    SB();
  }
  ST(12);
#pragma unroll
  for (int j = TAILN >= 3 ? 4 : 0; j < 8; j++) send(1, j);
  BAR();
  if (b == 0 && w == 3) {
    _Pragma("unroll 1") for (u32 spin = 0; spin < SPIN_CAP; spin++) {
      asm volatile("" ::: "memory");
      const u32x4 f0 = __builtin_amdgcn_raw_buffer_load_b128(sWS, WS_UF + l16, 0, 16);
      if (__all(f0[0] == tag && f0[1] == tag && f0[2] == tag && f0[3] == tag)) break;
    }
    if (lane == 0) __builtin_amdgcn_raw_buffer_store_b32(tag, sWS, 0, 0, 16);
  }
  STAMP_OUT();
}

extern "C" void kernel_launch(void* const* d_in, const int* in_sizes, int n_in, void* d_out, int out_size,
                              void* d_ws, size_t ws_size, hipStream_t stream) {
  Args a;
  a.x = Srd{d_in[0], L * D * 2, 0x00020000}; a.w1 = Srd{d_in[8], D * F * 2, 0x00020000}; a.w2 = Srd{d_in[10], D * F * 2, 0x00020000};
  a.ws = (char*)d_ws + WSO; a.out = (u16*)d_out;
  a.lar = (const u16*)d_in[1]; a.aim = (const u16*)d_in[2]; a.cre = (const u16*)d_in[3]; a.cim = (const u16*)d_in[4];
  a.ldt = (const u16*)d_in[5]; a.dsk = (const u16*)d_in[6]; a.b1 = (const u16*)d_in[9]; a.b2 = (const u16*)d_in[11];
  a.gw = (const u16*)d_in[12];
  fused_mix<<<256, 512, 0, stream>>>(a);
}
```

```cpp
#define STAMP 1
#define RT 1
#define STMASK 1
#include <hip/hip_runtime.h>
#pragma clang diagnostic ignored "-Wint-to-pointer-cast"

typedef unsigned short u16;
typedef unsigned u32;
typedef unsigned long long u64;
typedef __bf16 h16x8 __attribute__((ext_vector_type(8)));
typedef __bf16 h16x2 __attribute__((ext_vector_type(2)));
typedef short s16x4 __attribute__((ext_vector_type(4)));
typedef short s16x8 __attribute__((ext_vector_type(8)));
typedef short s16x2 __attribute__((ext_vector_type(2)));
typedef float f32x2 __attribute__((ext_vector_type(2)));
typedef float f32x4 __attribute__((ext_vector_type(4)));
typedef float f32x16 __attribute__((ext_vector_type(16)));
typedef u32 u32x2 __attribute__((ext_vector_type(2)));
typedef u32 u32x4 __attribute__((ext_vector_type(4)));
typedef u32 u32x16 __attribute__((ext_vector_type(16)));
#define LAS __attribute__((address_space(3)))
#define CAS __attribute__((address_space(4)))
#define GAS __attribute__((address_space(1)))
#define DI __device__ __forceinline__

constexpr int L = 2048, D = 512, F = 1792;
constexpr u32 WS_UF = 8192;
constexpr u32 WS_HF = 4096;
constexpr u32 WS_G = 1u << 19;
#define UTS 4096
#define ATS 16384
#define WSO 0
constexpr u32 WS_UT = 1u << 20;
constexpr u32 WS_AT = 4u << 20;
constexpr u32 WS_H = 9u << 20;
constexpr u32 WS_END = 16u << 20;
constexpr u32 WS_STAMP = 32u << 20;
constexpr u32 SLOT = 32768;
constexpr u32 TAB = 3 * SLOT;
constexpr u32 TC = 4 * SLOT;
constexpr u32 TC_STR = 4160;
constexpr u32 US = TC + 4 * TC_STR;
constexpr u32 US_STR = (256 + 2048) * 2;
constexpr u32 KD_CH = 32 * 36 * 2;
constexpr u32 LDS_BYTES = 163840;
constexpr u32 KD = LDS_BYTES - 2 * KD_CH;
constexpr u32 KD_ZERO = 34 * 2;
constexpr u32 CNT = KD - 64;
constexpr u32 FD = CNT + 16, RDY = CNT + 32, GO = CNT + 48;
constexpr u32 RED = TC;
static_assert(US + 2 * US_STR <= CNT && TAB + 256 * 224 + 16 <= CNT, "lds map");
constexpr u32 SPIN_CAP = 1u << 16;
#define EPK 0
#define WDELAY 0
#define CBO 4
#define TAILN 4
#define EXPE 0
#define LSL 4
#define TPRIO 3
#define UTRY 1
#define XMAP 1
#define PRET 0
#define THR 4
#define SPWAIT 32
#define HEAD 12
#define WPRE 3
#define PUBAT 0
#define PPOL ""
#define PPOL2 ""
#define XPOL ""
#define NPF 4
#define PFDELAY 24
#define POLLAT 6
#define PFC 0
#define LA 3
#define OUTPOL 16
#define UPUB 2
#define RSLEEP 8
#define USLEEP 8
#define HSLEEP 2
#define PPAIR 4
#define ATAT 16
#define G2PRIO 0
#define CVPRIO 1

DI float bf2f(u32 v) { return __uint_as_float(v << 16); }
DI u32 pk2(float lo, float hi) { f32x2 a = {lo, hi}; return __builtin_bit_cast(u32, __builtin_convertvector(a, h16x2)); }
DI float rbf(float x) { return bf2f(pk2(x, x) & 0xffffu); }
DI float dot2(u32 a, u32 b, float c) { return __builtin_amdgcn_fdot2_f32_bf16(__builtin_bit_cast(h16x2, a), __builtin_bit_cast(h16x2, b), c, false); }
DI u32 rfl(u32 x) { return __builtin_amdgcn_readfirstlane(x); }
struct Mul { f32x2 b, n; };
DI Mul mk(f32x2 b) { return Mul{b, f32x2{-b.y, b.y}}; }
DI f32x2 cmul(f32x2 a, Mul m) { return __builtin_elementwise_fma(a.yx, m.n, a * m.b.xx); }
DI float mul1(float a, float b) { float r; asm("v_mul_f32 %0, %1, %2" : "=v"(r) : "v"(a), "v"(b)); return r; }
DI float add1(float a, float b) { float r; asm("v_add_f32 %0, %1, %2" : "=v"(r) : "v"(a), "v"(b)); return r; }
DI float fma1(float a, float b, float c) { float r; asm("v_fma_f32 %0, %1, %2, %3" : "=v"(r) : "v"(a), "v"(b), "v"(c)); return r; }
DI float hi2f(u32 v) { return __uint_as_float(v & 0xffff0000u); }
DI float fopq(float x) { asm volatile("" : "+v"(x)); return x; }
DI float relu(float x) { return __int_as_float(max(__float_as_int(x), 0)); }
DI u32 sopq(u32 x) { asm volatile("" : "+s"(x)); return x; }
DI u32 opq(u32 x) { asm volatile("" : "+v"(x)); return x; }
#define MF32(a, b, c) __builtin_amdgcn_mfma_f32_32x32x16_bf16(__builtin_bit_cast(h16x8, a), __builtin_bit_cast(h16x8, b), c, 0, 0, 0)
#define MF16(a, b, c) __builtin_amdgcn_mfma_f32_16x16x32_bf16(__builtin_bit_cast(h16x8, a), __builtin_bit_cast(h16x8, b), c, 0, 0, 0)
#define DMA(voff, srd, soff, dst) \
  asm volatile("s_mov_b32 m0, %0\n\ts_nop 0\n\tbuffer_load_dwordx4 %1, %2, %3 offen lds" ::"s"(dst), "v"(voff), "s"(srd), "s"(soff) : "memory")
#define SETM0(base, off) asm volatile("s_add_u32 m0, %0, %1\n\ts_nop 0" ::"s"(base), "i"(off) : "memory", "scc")
#define DMAP(pol, voff, srd, soff, q) asm volatile("buffer_load_dwordx4 %0, %1, %2 offen offset:%3" pol " lds" ::"v"(voff), "s"(srd), "s"(soff), "i"((q) * 1024) : "memory")
#define DMAQ(voff, srd, soff, q) DMAP("", voff, srd, soff, q)
#define MMV(acc, a, b) asm volatile("v_mfma_f32_32x32x16_bf16 %0, %1, %2, %0" : "+v"(acc) : "v"(a), "v"(b) : "memory")
#define VMCNT(n) asm volatile("s_waitcnt vmcnt(%0)" ::"i"(n) : "memory")
#define LGKM0() asm volatile("s_waitcnt lgkmcnt(0)" ::: "memory")
#define BAR() asm volatile("s_waitcnt lgkmcnt(0)\n\ts_barrier" ::: "memory")
#define SB() __builtin_amdgcn_sched_barrier(0)
#define STI "s_memrealtime"
#define ST(i) do { if (!((STMASK >> (i)) & 1)) break; SB(); asm volatile(STI " %0\n\ts_waitcnt lgkmcnt(0)" : "=s"(stamp[(i) % 24])::"memory"); SB(); } while (0)
#define STN(i) do { if (!((STMASK >> (i)) & 1)) break; SB(); asm volatile(STI " %0" : "=s"(stamp[(i) % 24])::"memory"); SB(); } while (0)

template <int SL = 1> DI void lds_wait(u32 a, u32 n) {
  _Pragma("unroll 1") for (u32 spin = 0; spin < (1u << 22); spin++) {
    asm volatile("" ::: "memory");
    if (__all(*(volatile LAS u32*)a >= n)) break;
    __builtin_amdgcn_s_sleep(SL);
  }
  asm volatile("" ::: "memory");
}
template <int V> struct IC { static constexpr int v = V; };
template <int I, int N, class Fn> DI void sfor(Fn&& f) { if constexpr (I < N) { f(IC<I>{}); sfor<I + 1, N>(f); } }
constexpr int step(int i) { return i < 28 ? i : i < 32 ? i - 16 : i < 44 ? i - 32 : i - 28; }
DI u32x4 lds128(u32 a, u32 o = 0) { return *(const LAS u32x4*)((LAS char*)a + o); }
DI s16x4 tr64(u32 a, u32 o) { return __builtin_amdgcn_ds_read_tr16_b64_v4i16((LAS s16x4*)((LAS char*)a + o)); }
DI u32x4 cat(s16x4 lo, s16x4 hi) { return __builtin_bit_cast(u32x4, __builtin_shufflevector(lo, hi, 0, 1, 2, 3, 4, 5, 6, 7)); }
DI u32x4 ldstr(u32 a, u32 o0, u32 o1) { return cat(tr64(a, o0), tr64(a, o1)); }

struct Srd { const void* p; u32 bytes, flags; };
struct Args {
  Srd x, w1, w2;
  char* ws;
  u16* out;
  const u16 *lar, *aim, *cre, *cim, *ldt, *dsk, *b1, *b2, *gw;
};
DI u32 colblock(u32 jj, u32 w2lo) { return (jj + CBO - (w2lo >> 7)) & 7; }
DI u64 mk64(u32 lo, u32 hi) { return (u64)lo | ((u64)hi << 32); }
#define STAMP_OUT() do { VMCNT(0); ST(23); if (b == 8 && wave == 0) { const u64 until = rt0 + 4000 + 10 * (stamp[0] - stamp[0]); for (u32 i_ = 0; i_ < (1u << 22); i_++) { if (__builtin_amdgcn_s_memrealtime() >= until) break; } } } while (0)

__global__ void __launch_bounds__(512, 2) fused_mix(Args) {
  __shared__ __attribute__((aligned(16))) char smem[LDS_BYTES];
  const u32 sb = (u32)(size_t)(LAS char*)smem;
  u64 stamp[24], rt0, rt1;
  for (int i = 0; i < 24; i++) stamp[i] = 0;
  asm volatile("s_memrealtime %0\n\ts_waitcnt lgkmcnt(0)" : "=s"(rt0)::"memory");
  ST(0);
  u64 a_lar, a_aim, a_cre, a_cim, a_ldt, a_dsk, a_ws, a_oth;
  u32 wave, r_lar, r_aim, r_cre, r_cim, ctag, t_ldt, t_dsk;
  asm volatile(
      "s_load_dwordx2 %[lar], %[ka], 64\n\ts_load_dwordx2 %[ws], %[ka], 48\n\ts_load_dwordx2 %[aim], %[ka], 72\n\ts_load_dwordx2 %[cre], %[ka], 80\n\t"
      "s_load_dwordx2 %[cim], %[ka], 88\n\ts_load_dwordx2 %[ldt], %[ka], 96\n\ts_load_dwordx2 %[dsk], %[ka], 104\n\t"
      "v_readfirstlane_b32 %[wv], %[tid]\n\ts_getpc_b64 %[oth]\n.Lcode_start:\n\t"
      "s_lshr_b32 %[wv], %[wv], 6\n\ts_cmp_lt_u32 %[wv], 4\n\ts_cbranch_scc1 1f\n\t"
      "s_setprio 3\n\tv_and_b32 %[r3], 0xff, %[tid]\n\ts_lshr_b32 %[tg], %[b], 3\n\tv_lshlrev_b32 %[r0], 2, %[r3]\n\t"
      "v_lshl_or_b32 %[r0], %[tg], 10, %[r0]\n\tv_min_u32 %[r0], .Lcode_end - .Lcode_start - 4, %[r0]\n\tglobal_load_dword %[r0], %[r0], %[oth]\n\t"
      "s_lshl_b32 %[tg], %[wv], 3\n\ts_add_u32 %[tg], %[tg], 32\n\ts_load_dwordx2 %[oth], %[ka], %[tg]\n\t"
      "v_lshlrev_b32 %[r3], 1, %[r3]\n\tv_lshl_or_b32 %[r3], %[b], 9, %[r3]\n\ts_xor_b32 %[tl], %[b], %[pp]\n\t"
      "v_and_b32 %[r2], 63, %[tid]\n\tv_lshlrev_b32 %[r2], 3, %[r2]\n\tv_lshl_or_b32 %[r2], %[tl], 9, %[r2]\n\ts_lshl_b32 %[td], %[b], 2\n\t"
      "s_waitcnt lgkmcnt(0)\n\t"
      "global_load_ushort %[r0], %[r3], %[lar]" PPOL "\n\tglobal_load_ushort %[r1], %[r3], %[aim]" PPOL "\n\t"
      "global_load_dword %[r2], %[r2], %[oth]\n\t"
      "global_load_ushort %[r2], %[r3], %[cre]" PPOL "\n\tglobal_load_ushort %[r3], %[r3], %[cim]" PPOL "\n\t"
      "s_load_dword %[tg], %[ws], 0\n\ts_load_dword %[tl], %[ldt], %[td]\n\ts_load_dword %[td], %[dsk], %[td]\n1:"
      : [lar] "=&s"(a_lar), [aim] "=&s"(a_aim), [cre] "=&s"(a_cre), [cim] "=&s"(a_cim), [ldt] "=&s"(a_ldt), [dsk] "=&s"(a_dsk), [ws] "=&s"(a_ws),
        [oth] "=&s"(a_oth), [wv] "=&s"(wave), [tg] "=&s"(ctag), [tl] "=&s"(t_ldt), [td] "=&s"(t_dsk),
        [r0] "=&v"(r_lar), [r1] "=&v"(r_aim), [r2] "=&v"(r_cre), [r3] "=&v"(r_cim)
      : [ka] "s"(__builtin_amdgcn_kernarg_segment_ptr()), [tid] "v"(threadIdx.x), [b] "s"(blockIdx.x), [pp] "i"(PPAIR) : "scc");
  SB();
  const u32 w = wave & 3;
  const u32 b = blockIdx.x, xcd = b & 7, bi = b >> 3;
  const u32 hf = XMAP ? (xcd ^ (xcd >> 2)) & 1 : xcd & 1, p = 4 * (XMAP ? xcd & 3 : xcd >> 1) + (bi >> 3), jj = bi & 7;
  const bool spare = jj == 7;
  const u32 cb1 = 7 * hf + jj, rb = 2 * p + hf, pair = b;
  const u32 wm = w >> 1, wn = w & 1;
#define LANES() const u32 tid = opq(threadIdx.x) & 255, lane = tid & 63, hh = lane >> 5, blk = (lane >> 4) & 1, q4 = (lane & 15) >> 2, p4 = lane & 3, g4 = lane >> 4, l16 = opq(lane * 16)
  const u32 rot = 2 * (bi >> 3) + jj;

  if (wave >= 4) {
    u32x16 k0;
    u32x4 k1;
    asm volatile("s_load_dwordx16 %0, %2, 0\n\ts_load_dwordx4 %1, %2, 120" : "=&s"(k0), "=&s"(k1) : "s"(__builtin_amdgcn_kernarg_segment_ptr()));
    SB();
    LANES();
    const u32 ta = opq(sb + TAB + tid * 224);
    const u32 t0 = opq(sb + TAB + (wm * 128 + 4 * g4 + q4) * 224 + 8 * p4), t1 = opq(t0 + 32 * wn);
    const u32 ko = opq(sb + KD + wm * KD_CH + (4 * g4 * 36 + 16 * wn + (lane & 15)) * 2);
    char* const ws = (char*)a_ws;
    const __amdgpu_buffer_rsrc_t sWS = __builtin_amdgcn_make_buffer_rsrc((void*)ws, 0, WS_END, 0x00020000);
    u32 ga[9];
    {
      auto cell = [&](u32 pos) {
        const u32 hi = pos >> 8, ex = min(3u, 32u - (u32)__clz(hi));
        return sb + KD + (4 * ex + ((8 * hi) >> ex)) * 72 + ((pos & 255) >> 2);
      };
      const u32 P = 2040 - 8 * tid, a0 = cell(P), a8 = cell(P + 8);
      const u32 c0 = w == 3 ? 0x83222100u : w == 2 ? 0x88111000u : 0x88880000u, c1 = w == 3 ? 0x76543210u : w == 2 ? 0x88111000u : 0x88800000u;
      const u32 code = (w == 3 ? hh : (lane & 1)) ? c1 : c0;
#pragma unroll
      for (int k = 0; k < 8; k++) {
        const u32 n = (code >> (4 * k)) & 15u;
        ga[k + 1] = opq(n == 8 ? a8 : a0 + n * 72);
      }
      const u32 lowa = opq(P == 0 ? sb + KD + KD_ZERO : sb + KD + 7 * 72 + ((P - 8) >> 2));
      ga[0] = opq(P > 256 ? a0 : lowa);
    }
    *(LAS u32*)(hh ? sb + CNT + (lane & 15) * 4 : sb + KD + (lane & 1) * KD_CH + KD_ZERO) = 0;
    const u32 to = opq(sb + TC + tid * 16);
    const u32 ur = opq(sb + hf * 8192 + (lane & 7) * 1024 + (((lane >> 3) ^ (4 * (lane & 1))) << 4) + 4 * w);
    const u32 uw = opq(WS_UT + ((rot & 7) * 64 + (lane >> 3) * 8 + 2 * w) * UTS + rb * 128 + (lane & 7) * 16);

    STN(1);
    asm volatile("s_waitcnt lgkmcnt(0)" : "+s"(ctag), "+s"(t_ldt), "+s"(t_dsk), "+s"(k0), "+s"(k1)::"memory");
    asm volatile("s_waitcnt vmcnt(0)" : "+v"(r_lar), "+v"(r_aim), "+v"(r_cre), "+v"(r_cim)::"memory");
    const u32 tag = ctag + 1, cb2 = colblock(jj, k0[8]);
    ST(2);
    {
      const float dt = rbf(__expf(bf2f(wm ? t_ldt >> 16 : t_ldt & 0xffffu)));
      const float er = -(rbf(__expf(bf2f(r_lar))) * dt) * 1.44269504088896f;
      const float wi = (bf2f(r_aim) * dt) * 0.159154943091895f;
      auto seed = [&](float t) {
        const float m = __builtin_amdgcn_exp2f(er * t), a = __builtin_amdgcn_fractf(wi * t);
        return f32x2{m * __builtin_amdgcn_cosf(a), m * __builtin_amdgcn_sinf(a)};
      };
      auto put8 = [&](const f32x2* v, u32 off) {
        u32x4 re = {pk2(v[0].x, v[1].x), pk2(v[2].x, v[3].x), pk2(v[4].x, v[5].x), pk2(v[6].x, v[7].x)};
        u32x4 im = {pk2(v[0].y, v[1].y), pk2(v[2].y, v[3].y), pk2(v[4].y, v[5].y), pk2(v[6].y, v[7].y)};
        *(LAS u32x4*)((LAS char*)ta + off) = re;
        *(LAS u32x4*)((LAS char*)ta + 112 + off) = im;
      };
      f32x2 v[8];
      const Mul w8 = mk(seed(8.f)), w64 = mk(seed(64.f));
      v[0] = f32x2{1.f, 0.f}; v[1] = w8.b; v[2] = cmul(v[1], w8);
      const Mul w16 = mk(v[2]);
#pragma unroll
      for (int k = 3; k < 8; k++) v[k] = cmul(v[k - 2], w16);
      put8(v, 0); SB();
      v[0] = w64.b;
#pragma unroll
      for (int r = 1; r < 4; r++) {
#pragma unroll
        for (int k = (r == 1); k < 8; k++) v[k] = cmul(v[k], w64);
        put8(v, 16 * r); SB();
      }
      f32x2 s1 = seed(1.f), s256 = seed(256.f);
      s1.y = -s1.y; s256.y = -s256.y;
      const Mul v1 = mk(s1), W = mk(s256), v2c = mk(cmul(s1, v1));
      f32x2 u[8];
      u[0] = f32x2{2.f * (bf2f(r_cre) * dt), -2.f * (bf2f(r_cim) * dt)};
      u[1] = cmul(u[0], v1);
#pragma unroll
      for (int k = 2; k < 8; k++) u[k] = cmul(u[k - 2], v2c);
      put8(u, 64); SB();
      v[0] = cmul(u[0], W); v[1] = cmul(u[2], W); v[2] = cmul(u[4], W); v[3] = cmul(u[6], W);
      v[4] = cmul(v[0], W); v[5] = cmul(v[2], W); v[6] = cmul(v[4], W); v[7] = cmul(v[5], W);
      u[0] = cmul(v[6], W); u[1] = cmul(u[0], W); u[2] = cmul(u[1], W); u[3] = cmul(u[2], W); u[4] = cmul(u[3], W);
      put8(v, 80);
      u[5] = u[6] = u[7] = f32x2{0.f, 0.f};
      put8(u, 96);
      ST(3);
      BAR();
      ST(14);
      f32x4 ka[2] = {{(lane == 0 && wn == 0) ? bf2f(wm ? t_dsk >> 16 : t_dsk & 0xffffu) : 0.f, 0.f, 0.f, 0.f}, {0.f, 0.f, 0.f, 0.f}};
      u32x4 fv[8], f0[8], f1[8];
#pragma unroll
      for (int ks = 0; ks < 10; ks++) {
        const u32 o = ks * 16 * 224;
        if (ks < 8) { fv[ks] = ldstr(t1, o, o + 112); f0[ks] = ldstr(t0, o + 64, o + 64 + 112); f1[ks] = ldstr(t0, o + 96, o + 96 + 112); }
        if (ks >= 2) { ka[0] = MF16(f0[ks - 2], fv[ks - 2], ka[0]); ka[1] = MF16(f1[ks - 2], fv[ks - 2], ka[1]); }
        SB();
      }
#pragma unroll
      for (int rt = 0; rt < 2; rt++)
#pragma unroll
        for (int r = 0; r < 4; r += 2) {
          const u32 pr = pk2(ka[rt][r], ka[rt][r + 1]);
          *(LAS u16*)((LAS char*)ko + (16 * rt + r) * 72) = (u16)pr;
          *(LAS u16*)((LAS char*)ko + (16 * rt + r + 1) * 72) = (u16)(pr >> 16);
        }
    }
    ST(9);
    BAR();
    ST(4);
    {
      u32 xr[8];
#pragma unroll
      for (int j = 0; j < 8; j++) xr[j] = *(const LAS u32*)((LAS char*)(ur ^ ((j >> 1) << 4)) + j * 128);
#pragma unroll
      for (int c = 0; c < 2; c++) {
        u32x4 ou;
#pragma unroll
        for (int e = 0; e < 4; e++) ou[e] = __builtin_amdgcn_perm(xr[2 * e + 1], xr[2 * e], c ? 0x07060302 : 0x05040100);
        __builtin_amdgcn_raw_buffer_store_b128(ou, sWS, uw + c * UTS, 0, 16);
      }
    }
    ST(10);
    if (!spare) BAR();
    const u32 ufo = opq(WS_UF + (pair >> 5) * 128 + (lane & 31) * 4);
    const u32 ua = opq(sb + US + wm * US_STR + (256 - 16 - 128 * wn - 96 + 16 * (lane & 15) + 8 * g4) * 2);
    u32 tb[4];
#pragma unroll
    for (int j = 0; j < 4; j++) tb[j] = opq(sb + TC + (2 * wm + (lane & 1)) * TC_STR + (2046 - 16 - 128 * wn - 96 - 1792 + 8 * g4 - (lane & 14)) * 2 + 1024 * j);
    const u32 cw = opq(sb + CNT + 4 * w), cr = opq(sb + CNT + 4 * (lane & 3));
    auto arrive = [&](u32 n) { *(volatile LAS u32*)cw = n; };
    auto meet = [&](u32 n) { LGKM0(); arrive(n); lds_wait(cr, n); };
    if (spare) arrive(1);
    VMCNT(0);
    if (!spare) BAR();
    else {
      meet(2);
      if (tid == 0) asm volatile("buffer_store_dword %0, %1, %2, 0 offen sc1" ::"v"(tag), "v"(WS_UF + (rot & 7) * 128 + rb * 4), "s"(sWS) : "memory");
    }
    ST(5);
    const u32 row = tid >> 3, ck = lane & 7;
    const u32 ro = opq(sb + RED + w * 6144 + l16), r1 = opq(sb + RED + (w ^ 1) * 6144 + l16), r2 = opq(sb + RED + (w ^ 2) * 6144 + l16), r3 = opq(sb + RED + (w ^ 3) * 6144 + l16);
    const u32 ao = opq(WS_AT + pair * ATS + (8 * w + g4) * 512 + (lane & 15) * 16);
    const u32 sa = opq(sb + 2 * SLOT + ck * 1024 + ((row ^ (2 * ck)) << 4));
    if (!spare) BAR();
    const u32 ato = opq(WS_AT + (32 * cb2 + 8 * w + hh) * ATS + rb * 512 + (lane & 31) * 16);
    const u32 go = opq(WS_G + rb * 512 + row * 16);
    const u32 oo = opq((64 * rb + row) * 1024 + cb2 * 128 + ck * 16);
    const u32 aw = opq(sb + TC + (lane & 31) * 144 + (8 * w + hh) * 4), ar = opq(sb + TC + row * 144 + ck * 16);
    const u32 zo = opq(wn ? sb + US + hh * US_STR + (lane & 31) * 16 : sb + TC + ((lane >> 1) & 3) * TC_STR + 4096 + (lane & 1) * 16);
    auto fetch_u = [&]() {
#pragma unroll
      for (int q = 0; q < 8; q++) {
        if (!(q & 3)) SETM0(sb, US + 512 + (q >> 2) * US_STR);
        DMAP(" sc1", l16, sWS, WS_UT + (2 * pair + (q >> 2)) * UTS, q & 3);
      }
    };
    bool got = w != 0;
    if (!spare) {
      BAR();
#pragma unroll
      for (int i = 0; i < 2; i++) {
        if (got || i < UTRY) {
          BAR(); BAR();
        } else {
          u32 uf;
          asm volatile("buffer_load_dword %0, %1, %2, 0 offen sc1\n\ts_barrier\n\ts_barrier\n\ts_waitcnt vmcnt(0)" : "=&v"(uf) : "v"(ufo), "s"(sWS) : "memory");
          got = __all(uf == tag);
          if (got) fetch_u();
        }
      }
    }
    __builtin_amdgcn_s_setprio(CVPRIO);
    ST(21);
    {
      u32 ek[2][9];
#pragma unroll
      for (int i = 0; i < 9; i++) { ek[0][i] = *(const LAS u16*)ga[i]; ek[1][i] = *(const LAS u16*)((LAS char*)ga[i] + KD_CH); }
      *(LAS u32x4*)zo = u32x4{0, 0, 0, 0};
#pragma unroll
      for (int i = 0; i < 4; i++) {
        const u32* e = ek[i >> 1] + (i & 1);
        *(LAS u32x4*)((LAS char*)to + i * TC_STR) = u32x4{e[7] | (e[6] << 16), e[5] | (e[4] << 16), e[3] | (e[2] << 16), e[1] | (e[0] << 16)};
      }
    }
    if (!got) {
      __builtin_amdgcn_s_setprio(3);
      if (spare) __builtin_amdgcn_s_sleep(SPWAIT);
      _Pragma("unroll 1") for (u32 spin = 0; spin < SPIN_CAP; spin++) {
        u32 uf;
        asm volatile("buffer_load_dword %0, %1, %2, 0 offen sc1\n\ts_waitcnt vmcnt(0)" : "=&v"(uf) : "v"(ufo), "s"(sWS) : "memory");
        if (__all(uf == tag)) break;
        __builtin_amdgcn_s_sleep(USLEEP);
      }
      fetch_u();
      __builtin_amdgcn_s_setprio(CVPRIO);
    }
    VMCNT(0);
    ST(6);
    meet(4);
    ST(7);

    f32x4 ca[8];
#pragma unroll
    for (int c = 0; c < 8; c++) ca[c] = f32x4{0.f, 0.f, 0.f, 0.f};
    u32x4 fu[2][8];
    u32x2 ft[2][16];
    auto cread = [&](int f, int r) {
      const int k = r < 2 ? 8 + r : r < 10 ? r - 2 : r;
      if (k < 8) fu[f & 1][k] = lds128(ua, (96 - 32 * f + 256 * k) * 2);
      else {
        const int i = (k - 8) >> 1, hl = k & 1, e = 7 - i;
        const LAS u32* t = (const LAS u32*)((LAS char*)tb[e >> 1] + (96 - 32 * f + 256 * (e & 1)) * 2 + 8 * hl);
        ft[f & 1][2 * i + hl] = u32x2{t[0], t[1]};
      }
    };
#pragma unroll
    for (int r = 0; r < 24; r++) { cread(0, r); SB(); }
#pragma unroll
    for (int f = 0; f < 4; f++) {
      int m = 0;
#pragma unroll
      for (int i = 0; i < 8; i++)
#pragma unroll
        for (int e = 0; e + i < 8; e++, m++) {
          const u32x4 tf = {ft[f & 1][2 * i][0], ft[f & 1][2 * i][1], ft[f & 1][2 * i + 1][0], ft[f & 1][2 * i + 1][1]};
          ca[i + e] = MF16(fu[f & 1][e], tf, ca[i + e]);
          if (f < 3 && m >= 4 && m < 28) cread(f + 1, m - 4);
          if (f == 3 && m == 0) arrive(5);
          SB();
        }
    }
    ST(8);
    f32x4 keep[2];
    __builtin_amdgcn_s_setprio(3);
    lds_wait(cr, 5);
#define SEND(W) { _Pragma("unroll") for (int j = 1; j < 4; j++) { *(LAS f32x4*)((LAS char*)ro + (j - 1) * 2048) = ca[2 * (W ^ j)]; *(LAS f32x4*)((LAS char*)ro + (j - 1) * 2048 + 1024) = ca[2 * (W ^ j) + 1]; } \
                  keep[0] = ca[2 * W]; keep[1] = ca[2 * W + 1]; asm volatile("; send " #W ::: "memory"); }
    if (w == 0) SEND(0) else if (w == 1) SEND(1) else if (w == 2) SEND(2) else SEND(3)
    asm volatile("" ::: "memory");
    arrive(6);
    lds_wait(cr, 6);
    const u32 sel = wm ? 0x01000302 : 0x03020100;
    f32x4 pp[2][3];
#pragma unroll
    for (int c = 0; c < 2; c++) {
      pp[c][0] = *(const LAS f32x4*)((LAS char*)r1 + c * 1024);
      pp[c][1] = *(const LAS f32x4*)((LAS char*)r2 + 2048 + c * 1024);
      pp[c][2] = *(const LAS f32x4*)((LAS char*)r3 + 4096 + c * 1024);
    }
#pragma unroll
    for (int c = 0; c < 2; c++) {
      u32 o[4];
#pragma unroll
      for (int r = 0; r < 4; r++) {
        const u32 v = pk2(add1(keep[c][r], pp[c][0][r]), add1(pp[c][1][r], pp[c][2][r]));
        o[r] = __builtin_amdgcn_perm(v, v, sel);
      }
#pragma unroll
      for (int r = 0; r < 2; r++) __builtin_amdgcn_raw_buffer_store_b128(u32x4{o[r], tag, o[r + 2], tag}, sWS, ao + c * 2048 + r * 256, 0, 16);
    }
    arrive(7);
    ST(11);
    const __amdgpu_buffer_rsrc_t sB2 = __builtin_amdgcn_make_buffer_rsrc((void*)mk64(k1[0], k1[1]), 0, D * 2, 0x00020000);
    const __amdgpu_buffer_rsrc_t sOut = __builtin_amdgcn_make_buffer_rsrc((void*)mk64(k0[14], k0[15]), 0, L * D * 2, 0x00020000);
    const u32x4 b2v = __builtin_amdgcn_raw_buffer_load_b128(sB2, ck * 16, cb2 * 128, 0);
    lds_wait<LSL>(cr + RDY - CNT, 1);
    ST(12);
    u32x4 gg, at[4];
    _Pragma("unroll 1") for (u32 spin = 0; spin < SPIN_CAP; spin++) {
      asm volatile("" ::: "memory");
#pragma unroll
      for (int j = 0; j < 4; j++) at[j] = __builtin_amdgcn_raw_buffer_load_b128(sWS, ato, 2 * j * ATS, 16);
      gg = __builtin_amdgcn_raw_buffer_load_b128(sWS, go, 0, 16);
      bool ok = gg[1] == tag && gg[3] == tag;
#pragma unroll
      for (int j = 0; j < 4; j++) ok &= at[j][1] == tag && at[j][3] == tag;
      if (__all(ok)) break;
      __builtin_amdgcn_s_sleep(RSLEEP);
    }
    __builtin_amdgcn_s_setprio(TPRIO);
    ST(13);
    lds_wait(cr, 7);
#pragma unroll
    for (int j = 0; j < 4; j++) {
      *(LAS u32*)((LAS char*)aw + j * 8) = at[j][0];
      *(LAS u32*)((LAS char*)aw + j * 8 + 32 * 144) = at[j][2];
    }
    asm volatile("" ::: "memory");
    arrive(8);
    lds_wait(cr, 8);
    ST(15);
#pragma unroll
    for (int t = 0; t < 2; t++) {
      const float g = bf2f(gg[2 * t] & 0xffffu), gm = hi2f(gg[2 * t]);
      const u32x4 av = lds128(ar, t * 32 * 144);
      float base[8];
#pragma unroll
      for (int i = 0; i < 4; i++) {
        base[2 * i] = fopq(fma1(gm, bf2f(b2v[i] & 0xffffu), mul1(g, bf2f(av[i] & 0xffffu))));
        base[2 * i + 1] = fopq(fma1(gm, hi2f(b2v[i]), mul1(g, hi2f(av[i]))));
      }
      if (t == 0) { ST(16); lds_wait<LSL>(cr + RDY - CNT, 2); } else { ST(18); asm volatile("s_barrier" ::: "memory"); ST(19); }
      f32x4 pt[4][2];
#pragma unroll
      for (int k = 0; k < 4; k++)
#pragma unroll
        for (int q = 0; q < 2; q++) pt[k][q] = *(const LAS f32x4*)((LAS char*)sa + (1 - t) * SLOT + k * 8192 + q * 512);
      u32x4 ow;
#pragma unroll
      for (int i = 0; i < 4; i++) {
        float o[2];
#pragma unroll
        for (int e = 0; e < 2; e++) {
          const int q = i >> 1, r = 2 * (i & 1) + e;
          o[e] = t ? __builtin_fmaf(gm, (pt[0][q][r] + pt[1][q][r]) + (pt[2][q][r] + pt[3][q][r]), base[2 * i + e])
                   : fma1(gm, add1(add1(pt[0][q][r], pt[1][q][r]), add1(pt[2][q][r], pt[3][q][r])), base[2 * i + e]);
        }
        ow[i] = pk2(o[0], o[1]);
      }
      __builtin_amdgcn_raw_buffer_store_b128(ow, sOut, oo + t * 32768, 0, OUTPOL);
      SB();
      if (t == 0) ST(17);
    }
    asm volatile(".Lcode_end:");
    STAMP_OUT();
    return;
  }

  u32x16 k0;
  u32x4 k1;
  asm volatile("s_load_dwordx16 %0, %2, 0\n\ts_load_dwordx4 %1, %2, 112"
               : "=&s"(k0), "=&s"(k1) : "s"(__builtin_amdgcn_kernarg_segment_ptr()));
  SB();
  LANES();
  const u32 o1 = opq(((spare ? 0 : cb1) * 128 + wn * 64 + (lane & 31)) * 2);
  u32 vA[4], vB[4], v2[4];
#pragma unroll
  for (int q = 0; q < 4; q++) {
    vA[q] = opq((lane >> 3) * 1024 + (((lane & 7) ^ g4 ^ (4 * (q & 1))) << 4) + q * (8192 - 1024));
    vB[q] = opq(g4 * (F * 2) + (((lane & 15) ^ (g4 << 2) ^ q) << 4) + q * (4 * F * 2 - 1024));
    v2[q] = opq((lane >> 3) * 1024 + (((lane & 7) ^ (blk << 2)) << 4) + q * (8192 - 1024));
  }
  const u32 xbase = sopq((128 * p + 32 * w) * 1024), wbase = sopq(16 * w * (F * 2) + cb1 * 256);
  const u32 m4 = sopq(sb + w * 4096), m8 = sopq(sb + w * 8192);
  asm volatile("s_waitcnt lgkmcnt(0)" : "+s"(k0), "+s"(k1) : "s"(a_lar), "s"(a_aim), "s"(a_cre), "s"(a_cim), "s"(a_ldt), "s"(a_dsk), "s"(a_ws));
  ST(14);
  const u32x4 sX = k0.s0123, sW1 = k0.s4567;
  const u32x4 sW2 = k0.s89ab;
  char* const ws = (char*)mk64(k0[12], k0[13]);
  const u32 w2base = 28 * w * 16384 + colblock(jj, sW2[0]) * 128;
  u32 tag;
  const __amdgpu_buffer_rsrc_t sWS = __builtin_amdgcn_make_buffer_rsrc((void*)ws, 0, WS_END, 0x00020000);
  auto w2piece = [&](int c, int q) {
    if ((q & 3) == 0) SETM0(m8, (c & 3) * SLOT + (q >> 2) * 4096);
    DMAQ(v2[q & 3], sW2, w2base + c * 65536 + (q >> 2) * 32768, q & 3);
  };

  if (!spare) {
    auto g1piece = [&](int g, int q) {
      if (q == 0) SETM0(m4, (g & 3) * SLOT);
      if (q == 4) SETM0(m4, (g & 3) * SLOT + 16384);
      if (q < 4) DMAP(XPOL, vA[q], sX, xbase + ((g + rot) & 7) * 128, q);
      else DMAQ(vB[q - 4], sW1, wbase + ((g + rot) & 7) * 64 * (F * 2), q - 4);
    };
    u32 r_b1a, r_b1b;
    asm volatile("s_load_dword %0, %1, 0" : "=s"(tag) : "s"(ws));
    asm volatile("global_load_ushort %0, %1, %2" : "=v"(r_b1a) : "v"(o1), "s"(mk64(k1[0], k1[1])));
    asm volatile("global_load_ushort %0, %1, %2 offset:64" : "=v"(r_b1b) : "v"(o1), "s"(mk64(k1[0], k1[1])));
#pragma unroll
    for (int i = 0; i < 12; i++) g1piece(i >> 3, i < 8 ? i : i - 4);
    ST(1);
    u32 ax[2][4], aw[2][4];
    {
      const u32 xa = sb + (64 * wm + (lane & 31)) * 128 + ((hh ^ ((lane >> 1) & 7)) << 4);
      const u32 wa = sb + 16384 + 256 * (8 * hh + q4) + 8 * (p4 & 1) + (((8 * wn + 2 * blk + (p4 >> 1)) ^ ((q4 << 2) | (2 * hh))) << 4);
#pragma unroll
      for (int k = 0; k < 4; k++) {
        ax[0][k] = opq(xa ^ (k << 5)); aw[0][k] = opq(wa ^ ((k & 1) << 4) ^ ((k >> 1) << 6));
        ax[1][k] = opq(ax[0][k] + 2 * SLOT); aw[1][k] = opq(aw[0][k] + 2 * SLOT);
      }
    }
    const u32 ho = opq(l16 + WS_H + ((4 * p + 2 * wm) * 112 + cb1 * 8 + wn * 4) * 1024), ho1 = opq(ho + 112 * 1024);
    asm volatile("s_waitcnt lgkmcnt(0)" : "+s"(tag));
    const u32 vtag = opq(tag + 1), vuf = opq(WS_UF + (rot & 7) * 128 + rb * 4);
#pragma unroll
    for (int q = 0; q < 8 * LA - 12; q++) {
      if (q == PRET) { asm volatile("s_barrier" ::: "memory"); ST(3); }
      g1piece(q < 4 ? 1 : 2, q < 4 ? q : q - 4);
    }
    if (PRET >= 8 * LA - 12) { asm volatile("s_barrier" ::: "memory"); ST(3); }
    asm volatile("s_waitcnt vmcnt(%2)" : "+v"(r_b1a), "+v"(r_b1b) : "i"(8 * LA));
    f32x16 acc[2][2];
    {
      const u32x4 one = {hh ? 0u : 0x3f80u, 0u, 0u, 0u};
      const u32x4 ba = {r_b1a, 0u, 0u, 0u}, bb = {r_b1b, 0u, 0u, 0u};
      asm volatile("s_nop 1\n\tv_mfma_f32_32x32x16_bf16 %0, %4, %6, 0\n\tv_mfma_f32_32x32x16_bf16 %1, %4, %6, 0\n\t"
                   "v_mfma_f32_32x32x16_bf16 %2, %5, %6, 0\n\tv_mfma_f32_32x32x16_bf16 %3, %5, %6, 0"
                   : "=&v"(acc[0][0]), "=&v"(acc[0][1]), "=&v"(acc[1][0]), "=&v"(acc[1][1]) : "v"(ba), "v"(bb), "v"(one));
    }
    u32x4 fx[2][2];
    s16x4 fw[2][4];
    VMCNT(8 * LA - 8);
    ST(2);
    BAR();
    ST(4);
    fx[0][0] = lds128(ax[0][0]); fw[0][0] = tr64(aw[0][0], 0); fw[0][1] = tr64(aw[0][1], 1024);
    fx[0][1] = lds128(ax[0][0], 4096); fw[0][2] = tr64(aw[0][2], 0); fw[0][3] = tr64(aw[0][3], 1024);
#pragma unroll
    for (int s = 0; s < 8; s++) {
      auto piece = [&](int q) { if (s + LA < 8) g1piece(s + LA, q); else if (s + LA - 8 < WPRE) w2piece(s + LA - 8, q); };
#pragma unroll
      for (int k = 0; k < 4; k++) {
        const int c = k & 1, n = c ^ 1, kn = (k + 1) & 3, sl = (k == 3 ? s + 1 : s) & 3, hi = sl >> 1;
        const u32 o = (sl & 1) * SLOT, ow = o + kn * 4096;
        const bool more = s < 7 || k < 3;
        MMV(acc[0][0], cat(fw[c][0], fw[c][1]), fx[c][0]);
        if (k == 3) {
          if (s < 7) VMCNT((LA == 3 && s + 2 < 8 + WPRE ? 8 : 0) + (s + LA < 8 + WPRE ? 6 : 0));
          BAR();
        }
        if (more) { fx[n][0] = lds128(ax[hi][kn], o); fw[n][0] = tr64(aw[hi][0], ow); fw[n][1] = tr64(aw[hi][1], ow + 1024); }
        MMV(acc[0][1], cat(fw[c][0], fw[c][1]), fx[c][1]);
        if (more) { fx[n][1] = lds128(ax[hi][kn], o + 4096); fw[n][2] = tr64(aw[hi][2], ow); }
        piece(2 * k);
        MMV(acc[1][0], cat(fw[c][2], fw[c][3]), fx[c][0]);
        if (more) fw[n][3] = tr64(aw[hi][3], ow + 1024);
        MMV(acc[1][1], cat(fw[c][2], fw[c][3]), fx[c][1]);
        piece(2 * k + 1);
      }
      if (s == 1)
        asm volatile("s_cmp_lg_u32 %3, 0\n\ts_cbranch_scc1 1f\n\ts_mov_b64 exec, 1\n\tbuffer_store_dword %0, %1, %2, 0 offen sc1\n\ts_mov_b64 exec, -1\n1:"
                     ::"v"(vtag), "v"(vuf), "s"(sWS), "s"(w) : "memory", "scc");
      if (s == 0) ST(8);
      if (s == 1) ST(15);
      if (s == 2) ST(16);
      if (s == 3) ST(17);
      if (s == 4) ST(19);
      if (s == 5) ST(20);
      if (s == 6) ST(21);
    }
    asm volatile("s_nop 7\n\ts_nop 7" : "+v"(acc[0][0]), "+v"(acc[0][1]), "+v"(acc[1][0]), "+v"(acc[1][1]));
    __builtin_amdgcn_s_setprio(2);
    ST(5);
#pragma unroll
    for (int u = 0; u < 2; u++)
#pragma unroll
      for (int t = 0; t < 2; t++)
#pragma unroll
        for (int s = 0; s < 2; s++) {
          u32x4 o;
#pragma unroll
          for (int e = 0; e < 4; e++) {
            const float a0 = relu(acc[u][t][8 * s + 2 * e]), a1 = relu(acc[u][t][8 * s + 2 * e + 1]);
            o[e] = EPK ? pk2(a0 * a0, a1 * a1) : pk2(mul1(a0, a0), mul1(a1, a1));
          }
          __builtin_amdgcn_raw_buffer_store_b128(o, sWS, (t ? ho1 : ho) + (2 * u + s) * 1024, 0, 16);
          SB();
        }
    tag += 1;
  } else {
    asm volatile("s_load_dword %0, %1, 0" : "=s"(tag) : "s"(ws));
    SETM0(m4, 0);
#pragma unroll
    for (int q = 0; q < 4; q++) DMAP(XPOL, vA[q], sX, xbase + (rot & 7) * 128, q);
    u32x4 gwv, xr[16];
    asm volatile("global_load_dwordx4 %0, %1, %2" : "=v"(gwv) : "v"(l16), "s"(((const CAS Args*)__builtin_amdgcn_kernarg_segment_ptr())->gw));
#pragma unroll
    for (int j = 0; j < 16; j++) {
      if (j == 7) asm volatile("s_barrier" ::: "memory");
      asm volatile("buffer_load_dwordx4 %0, %1, %2, %3 offen offset:%4" : "=v"(xr[j]) : "v"(l16), "s"(sX), "s"((64 * rb + 16 * w + (j & ~3)) * 1024), "i"((j & 3) * 1024));
    }
    asm volatile("s_waitcnt vmcnt(17) lgkmcnt(0)\n\ts_barrier" : "+s"(tag)::"memory");
    tag += 1;
    ST(2);
    asm volatile("s_waitcnt vmcnt(0)" : "+v"(gwv), "+v"(xr[0]), "+v"(xr[1]), "+v"(xr[2]), "+v"(xr[3]), "+v"(xr[4]), "+v"(xr[5]), "+v"(xr[6]), "+v"(xr[7]),
                 "+v"(xr[8]), "+v"(xr[9]), "+v"(xr[10]), "+v"(xr[11]), "+v"(xr[12]), "+v"(xr[13]), "+v"(xr[14]), "+v"(xr[15])::"memory");
    SETM0(m8, SLOT);
    auto pull = [&](u32 n) {
#pragma unroll
      for (int q = 0; q < 4; q++) DMAQ(l16, sW2, min(14 * xcd + 4 * n + (bi >> 3), 111u) * 16384 + w * 4096, q);
    };
    pull(0); pull(1);
    ST(8);
    const u32 zs = m8 + 2 * SLOT;
#pragma unroll
    for (int j = 0; j < 16; j++) {
      float z = 0.f;
#pragma unroll
      for (int e = 0; e < 4; e++) z = dot2(xr[j][e], gwv[e], z);
      *(LAS float*)((LAS char*)(zs + lane * 4) + j * 272) = z;
    }
    VMCNT(4); pull(2);
    LGKM0();
    f32x4 z4 = {};
#pragma unroll
    for (int k = 0; k < 4; k++) z4 += *(const LAS f32x4*)((LAS char*)(zs + (lane & 15) * 272 + g4 * 64) + k * 16);
    float zz = (z4[0] + z4[1]) + (z4[2] + z4[3]);
    zz += __shfl_xor(zz, 16); zz += __shfl_xor(zz, 32);
    const float g = rbf(1.f / (1.f + __expf(-zz)));
    VMCNT(4); pull(3);
    if (lane < 16) __builtin_amdgcn_raw_buffer_store_b64(u32x2{pk2(g, 1.f - g), tag}, sWS, WS_G + rb * 512 + (wn * 16 + lane) * 16 + wm * 8, 0, 16);
    lds_wait(sb + CNT + 4 * (lane & 3), 1);
    ST(4);
#pragma unroll
    for (int i = 0; i < 8 * WPRE; i++) { if (!(i & 3)) VMCNT(THR); w2piece(i >> 3, i & 7); }
  }
  SB();
  ST(6);

  u32 w2a[2][2];
  {
    const u32 a = m8 + 128 * (4 * hh + q4) + 8 * (p4 & 1) + (((2 * blk + (p4 >> 1)) ^ ((q4 >> 1) << 2)) << 4);
    w2a[0][0] = opq(a); w2a[1][0] = opq(a ^ 64); w2a[0][1] = opq(a + 2 * SLOT); w2a[1][1] = opq((a ^ 64) + 2 * SLOT);
  }
  auto w2frag = [&](int st, int v) {
    const int sl = (st >> 2) & 3;
    const u32 o = (sl & 1) * SLOT + (st & 3) * 2048;
    return ldstr(w2a[v][sl >> 1], o, o + 1024);
  };
  u32x4 w2r[12][2];
  u32 fl = 0;
  const u32 fo = opq(WS_HF + p * 128 + (((lane & 7) == 7) ? 0 : (lane & 15) * 4));
#pragma unroll
  for (int c = WPRE; c < 8; c++) {
    if (c == WPRE + PUBAT) {
      VMCNT(8 * PUBAT);
      *(volatile LAS u32*)(sb + FD + 4 * w) = 1;
      if (w == 0 && !spare) {
        lds_wait(sb + FD + 4 * (lane & 3), 1);
        if (lane == 0) asm volatile("buffer_store_dword %0, %1, %2, 0 offen sc1" ::"v"(tag), "v"(WS_HF + p * 128 + (8 * hf + jj) * 4), "s"(sWS) : "memory");
      }
      ST(7);
      __builtin_amdgcn_s_sleep(WDELAY);
    }
    if (c >= 4 && c < 7) {
      VMCNT(24);
#pragma unroll
      for (int j = 0; j < 8; j++) w2r[4 * (c - 4) + (j >> 1)][j & 1] = w2frag(4 * (c - 4) + (j >> 1), j & 1);
      LGKM0();
    }
    if (c == POLLAT) asm volatile("s_cmp_lg_u32 %3, 3\n\ts_cbranch_scc1 1f\n\tbuffer_load_dword %0, %1, %2, 0 offen sc1\n1:" : "+v"(fl) : "v"(fo), "s"(sWS), "s"(w) : "memory", "scc");
    if (c < 7) {
#pragma unroll
      for (int q = 0; q < 8; q++) {
        if (!(q & 3)) asm volatile("s_cmp_lg_u32 %0, 7\n\ts_cbranch_scc1 1f\n\ts_waitcnt vmcnt(%1)\n1:" ::"s"(jj), "i"(THR) : "memory", "scc");
        w2piece(c, q);
      }
    }
  }
  const u32 hb = sopq(WS_H + (2 * rb * 112 + 28 * w) * 1024);
  const u32 xw = opq(m8 + 2 * SLOT + l16), rdw = opq(sb + RDY + 4 * w);
  asm volatile("s_waitcnt vmcnt(%1)" : "+v"(fl) : "i"(8 * (7 - POLLAT)) : "memory");
  ST(11);
  if (w == 3) {
    _Pragma("unroll 1") for (u32 spin = 0; spin < SPIN_CAP; spin++) {
      if (__all(fl == tag)) break;
      __builtin_amdgcn_s_sleep(HSLEEP);
      asm volatile("buffer_load_dword %0, %1, %2, 0 offen sc1\n\ts_waitcnt vmcnt(0)" : "=&v"(fl) : "v"(fo), "s"(sWS) : "memory");
    }
    *(volatile LAS u32*)(sb + GO) = 1;
  } else {
    lds_wait(sb + GO, 1);
  }
  __builtin_amdgcn_s_setprio(G2PRIO);
  ST(10);
  SB();

  u32x4 hfr[56];
  auto load_h = [&](int i) {
    const int st = step(i);
    hfr[i] = __builtin_amdgcn_raw_buffer_load_b128(sWS, l16 + (st & 3) * 1024, hb + ((i / 28) * 112 + (st & ~3)) * 1024, 16);
  };
#pragma unroll
  for (int i = 0; i < HEAD; i++) load_h(i);
  SB();
  f32x16 oc[2][2] = {};
  auto send = [&](int t, int j) {
    const f32x16& s = oc[t][j >> 2];
    const int r = 4 * (j & 3);
    *(LAS f32x4*)((LAS char*)(xw ^ (j << 5)) + (1 - t) * SLOT + j * 1024) = f32x4{s[r], s[r + 1], s[r + 2], s[r + 3]};
  };
  auto signal = [&](u32 n) { asm volatile("" ::: "memory"); *(volatile LAS u32*)rdw = n; };
  u32x4 wl[56][2];
  auto fetch_w = [&](int i) { if (i < 56 && step(i) >= 12) { wl[i][0] = w2frag(step(i), 0); wl[i][1] = w2frag(step(i), 1); } };
  sfor<0, 56>([&](auto ic) {
    constexpr int i = decltype(ic)::v, t = i / 28, st = step(i);
    constexpr int D2 = 53 - TAILN;
    if (i < D2 || !TAILN) fetch_w(i + 1); else { fetch_w(2 * i - D2 + 1); fetch_w(2 * i - D2 + 2); }
    SB();
    u32x4 a0, a1;
    if (st < 12) { a0 = w2r[st < 12 ? st : 0][0]; a1 = w2r[st < 12 ? st : 0][1]; } else { a0 = wl[i][0]; a1 = wl[i][1]; }
    oc[t][0] = MF32(a0, hfr[i], oc[t][0]);
    if (i + HEAD < 56) load_h(i + HEAD);
    SB();
    if (i < 56 - TAILN) oc[t][1] = MF32(a1, hfr[i], oc[t][1]);
    if (i == ATAT) signal(1);
    if (i >= 31 && i < 39) send(0, i - 31);
    if (i == 38) signal(2);
    SB();
    if (i == 27) ST(18);
  });
#pragma unroll
  for (int i = 0; i < TAILN; i++) {
    oc[1][1] = MF32(wl[56 - TAILN + i][1], hfr[56 - TAILN + i], oc[1][1]);
    SB();
    if (i >= 1 && i < 3) { send(1, 2 * i - 2); send(1, 2 * i - 1); }
    SB();
  }
  ST(12);
#pragma unroll
  for (int j = TAILN >= 3 ? 4 : 0; j < 8; j++) send(1, j);
  BAR();
  if (b == 0 && w == 3) {
    _Pragma("unroll 1") for (u32 spin = 0; spin < SPIN_CAP; spin++) {
      asm volatile("" ::: "memory");
      const u32x4 f0 = __builtin_amdgcn_raw_buffer_load_b128(sWS, WS_UF + l16, 0, 16);
      if (__all(f0[0] == tag && f0[1] == tag && f0[2] == tag && f0[3] == tag)) break;
    }
    if (lane == 0) __builtin_amdgcn_raw_buffer_store_b32(tag, sWS, 0, 0, 16);
  }
  STAMP_OUT();
}

extern "C" void kernel_launch(void* const* d_in, const int* in_sizes, int n_in, void* d_out, int out_size,
                              void* d_ws, size_t ws_size, hipStream_t stream) {
  Args a;
  a.x = Srd{d_in[0], L * D * 2, 0x00020000}; a.w1 = Srd{d_in[8], D * F * 2, 0x00020000}; a.w2 = Srd{d_in[10], D * F * 2, 0x00020000};
  a.ws = (char*)d_ws + WSO; a.out = (u16*)d_out;
  a.lar = (const u16*)d_in[1]; a.aim = (const u16*)d_in[2]; a.cre = (const u16*)d_in[3]; a.cim = (const u16*)d_in[4];
  a.ldt = (const u16*)d_in[5]; a.dsk = (const u16*)d_in[6]; a.b1 = (const u16*)d_in[9]; a.b2 = (const u16*)d_in[11];
  a.gw = (const u16*)d_in[12];
  fused_mix<<<256, 512, 0, stream>>>(a);
}
```

```cpp
#include <hip/hip_runtime.h>
#pragma clang diagnostic ignored "-Wint-to-pointer-cast"

typedef unsigned short u16;
typedef unsigned u32;
typedef unsigned long long u64;
typedef __bf16 h16x8 __attribute__((ext_vector_type(8)));
typedef __bf16 h16x2 __attribute__((ext_vector_type(2)));
typedef short s16x4 __attribute__((ext_vector_type(4)));
typedef short s16x8 __attribute__((ext_vector_type(8)));
typedef short s16x2 __attribute__((ext_vector_type(2)));
typedef float f32x2 __attribute__((ext_vector_type(2)));
typedef float f32x4 __attribute__((ext_vector_type(4)));
typedef float f32x16 __attribute__((ext_vector_type(16)));
typedef u32 u32x2 __attribute__((ext_vector_type(2)));
typedef u32 u32x4 __attribute__((ext_vector_type(4)));
typedef u32 u32x16 __attribute__((ext_vector_type(16)));
#define LAS __attribute__((address_space(3)))
#define CAS __attribute__((address_space(4)))
#define GAS __attribute__((address_space(1)))
#define DI __device__ __forceinline__

constexpr int L = 2048, D = 512, F = 1792;
constexpr u32 WS_UF = 8192;
constexpr u32 WS_HF = 4096;
constexpr u32 WS_G = 1u << 19;
#define UTS 4096
#define ATS 16384
#define WSO 0
constexpr u32 WS_UT = 1u << 20;
constexpr u32 WS_AT = 4u << 20;
constexpr u32 WS_H = 9u << 20;
constexpr u32 WS_END = 16u << 20;
constexpr u32 WS_STAMP = 32u << 20;
constexpr u32 SLOT = 32768;
constexpr u32 TAB = 3 * SLOT;
constexpr u32 TC = 4 * SLOT;
constexpr u32 TC_STR = 4160;
constexpr u32 US = TC + 4 * TC_STR;
constexpr u32 US_STR = (256 + 2048) * 2;
constexpr u32 KD_CH = 32 * 36 * 2;
constexpr u32 LDS_BYTES = 163840;
constexpr u32 KD = LDS_BYTES - 2 * KD_CH;
constexpr u32 KD_ZERO = 34 * 2;
constexpr u32 CNT = KD - 64;
constexpr u32 FD = CNT + 16, RDY = CNT + 32, GO = CNT + 48;
constexpr u32 RED = TC;
static_assert(US + 2 * US_STR <= CNT && TAB + 256 * 224 + 16 <= CNT, "lds map");
constexpr u32 SPIN_CAP = 1u << 16;
#define EPK 0
#define WDELAY 0
#define CBO 4
#define TAILN 4
#define EXPE 0
#define LSL 4
#define TPRIO 3
#define UTRY 1
#define XMAP 1
#define PRET 0
#define THR 4
#define SPWAIT 32
#define HEAD 12
#define WPRE 3
#define PUBAT 0
#define PPOL ""
#define PPOL2 ""
#define XPOL ""
#define NPF 4
#define PFDELAY 24
#define POLLAT 6
#define PFC 0
#define LA 3
#define OUTPOL 16
#define UPUB 2
#define RSLEEP 8
#define USLEEP 8
#define HSLEEP 2
#define PPAIR 4
#define ATAT 16
#define G2PRIO 0
#define CVPRIO 1

DI float bf2f(u32 v) { return __uint_as_float(v << 16); }
DI u32 pk2(float lo, float hi) { f32x2 a = {lo, hi}; return __builtin_bit_cast(u32, __builtin_convertvector(a, h16x2)); }
DI float rbf(float x) { return bf2f(pk2(x, x) & 0xffffu); }
DI float dot2(u32 a, u32 b, float c) { return __builtin_amdgcn_fdot2_f32_bf16(__builtin_bit_cast(h16x2, a), __builtin_bit_cast(h16x2, b), c, false); }
DI u32 rfl(u32 x) { return __builtin_amdgcn_readfirstlane(x); }
struct Mul { f32x2 b, n; };
DI Mul mk(f32x2 b) { return Mul{b, f32x2{-b.y, b.y}}; }
DI f32x2 cmul(f32x2 a, Mul m) { return __builtin_elementwise_fma(a.yx, m.n, a * m.b.xx); }
DI float mul1(float a, float b) { float r; asm("v_mul_f32 %0, %1, %2" : "=v"(r) : "v"(a), "v"(b)); return r; }
DI float add1(float a, float b) { float r; asm("v_add_f32 %0, %1, %2" : "=v"(r) : "v"(a), "v"(b)); return r; }
DI float fma1(float a, float b, float c) { float r; asm("v_fma_f32 %0, %1, %2, %3" : "=v"(r) : "v"(a), "v"(b), "v"(c)); return r; }
DI float hi2f(u32 v) { return __uint_as_float(v & 0xffff0000u); }
DI float fopq(float x) { asm volatile("" : "+v"(x)); return x; }
DI float relu(float x) { return __int_as_float(max(__float_as_int(x), 0)); }
DI u32 sopq(u32 x) { asm volatile("" : "+s"(x)); return x; }
DI u32 opq(u32 x) { asm volatile("" : "+v"(x)); return x; }
#define MF32(a, b, c) __builtin_amdgcn_mfma_f32_32x32x16_bf16(__builtin_bit_cast(h16x8, a), __builtin_bit_cast(h16x8, b), c, 0, 0, 0)
#define MF16(a, b, c) __builtin_amdgcn_mfma_f32_16x16x32_bf16(__builtin_bit_cast(h16x8, a), __builtin_bit_cast(h16x8, b), c, 0, 0, 0)
#define DMA(voff, srd, soff, dst) \
  asm volatile("s_mov_b32 m0, %0\n\ts_nop 0\n\tbuffer_load_dwordx4 %1, %2, %3 offen lds" ::"s"(dst), "v"(voff), "s"(srd), "s"(soff) : "memory")
#define SETM0(base, off) asm volatile("s_add_u32 m0, %0, %1\n\ts_nop 0" ::"s"(base), "i"(off) : "memory", "scc")
#define DMAP(pol, voff, srd, soff, q) asm volatile("buffer_load_dwordx4 %0, %1, %2 offen offset:%3" pol " lds" ::"v"(voff), "s"(srd), "s"(soff), "i"((q) * 1024) : "memory")
#define DMAQ(voff, srd, soff, q) DMAP("", voff, srd, soff, q)
#define MMV(acc, a, b) asm volatile("v_mfma_f32_32x32x16_bf16 %0, %1, %2, %0" : "+v"(acc) : "v"(a), "v"(b) : "memory")
#define VMCNT(n) asm volatile("s_waitcnt vmcnt(%0)" ::"i"(n) : "memory")
#define LGKM0() asm volatile("s_waitcnt lgkmcnt(0)" ::: "memory")
#define BAR() asm volatile("s_waitcnt lgkmcnt(0)\n\ts_barrier" ::: "memory")
#define SB() __builtin_amdgcn_sched_barrier(0)
#define ST(i)
#define STN(i)

template <int SL = 1> DI void lds_wait(u32 a, u32 n) {
  _Pragma("unroll 1") for (u32 spin = 0; spin < (1u << 22); spin++) {
    asm volatile("" ::: "memory");
    if (__all(*(volatile LAS u32*)a >= n)) break;
    __builtin_amdgcn_s_sleep(SL);
  }
  asm volatile("" ::: "memory");
}
template <int V> struct IC { static constexpr int v = V; };
template <int I, int N, class Fn> DI void sfor(Fn&& f) { if constexpr (I < N) { f(IC<I>{}); sfor<I + 1, N>(f); } }
constexpr int step(int i) { return i < 28 ? i : i < 32 ? i - 16 : i < 44 ? i - 32 : i - 28; }
DI u32x4 lds128(u32 a, u32 o = 0) { return *(const LAS u32x4*)((LAS char*)a + o); }
DI s16x4 tr64(u32 a, u32 o) { return __builtin_amdgcn_ds_read_tr16_b64_v4i16((LAS s16x4*)((LAS char*)a + o)); }
DI u32x4 cat(s16x4 lo, s16x4 hi) { return __builtin_bit_cast(u32x4, __builtin_shufflevector(lo, hi, 0, 1, 2, 3, 4, 5, 6, 7)); }
DI u32x4 ldstr(u32 a, u32 o0, u32 o1) { return cat(tr64(a, o0), tr64(a, o1)); }

struct Srd { const void* p; u32 bytes, flags; };
struct Args {
  Srd x, w1, w2;
  char* ws;
  u16* out;
  const u16 *lar, *aim, *cre, *cim, *ldt, *dsk, *b1, *b2, *gw;
};
DI u32 colblock(u32 jj, u32 w2lo) { return (jj + CBO - (w2lo >> 7)) & 7; }
DI u64 mk64(u32 lo, u32 hi) { return (u64)lo | ((u64)hi << 32); }
#define STAMP_OUT()

__global__ void __launch_bounds__(512, 2) fused_mix(Args) {
  __shared__ __attribute__((aligned(16))) char smem[LDS_BYTES];
  const u32 sb = (u32)(size_t)(LAS char*)smem;
  ST(0);
  u64 a_lar, a_aim, a_cre, a_cim, a_ldt, a_dsk, a_ws, a_oth;
  u32 wave, r_lar, r_aim, r_cre, r_cim, ctag, t_ldt, t_dsk;
  asm volatile(
      "s_load_dwordx2 %[lar], %[ka], 64\n\ts_load_dwordx2 %[ws], %[ka], 48\n\ts_load_dwordx2 %[aim], %[ka], 72\n\ts_load_dwordx2 %[cre], %[ka], 80\n\t"
      "s_load_dwordx2 %[cim], %[ka], 88\n\ts_load_dwordx2 %[ldt], %[ka], 96\n\ts_load_dwordx2 %[dsk], %[ka], 104\n\t"
      "v_readfirstlane_b32 %[wv], %[tid]\n\ts_getpc_b64 %[oth]\n.Lcode_start:\n\t"
      "s_lshr_b32 %[wv], %[wv], 6\n\ts_cmp_lt_u32 %[wv], 4\n\ts_cbranch_scc1 1f\n\t"
      "s_setprio 3\n\tv_and_b32 %[r3], 0xff, %[tid]\n\ts_lshr_b32 %[tg], %[b], 3\n\tv_lshlrev_b32 %[r0], 2, %[r3]\n\t"
      "v_lshl_or_b32 %[r0], %[tg], 10, %[r0]\n\tv_min_u32 %[r0], .Lcode_end - .Lcode_start - 4, %[r0]\n\tglobal_load_dword %[r0], %[r0], %[oth]\n\t"
      "s_lshl_b32 %[tg], %[wv], 3\n\ts_add_u32 %[tg], %[tg], 32\n\ts_load_dwordx2 %[oth], %[ka], %[tg]\n\t"
      "v_lshlrev_b32 %[r3], 1, %[r3]\n\tv_lshl_or_b32 %[r3], %[b], 9, %[r3]\n\ts_xor_b32 %[tl], %[b], %[pp]\n\t"
      "v_and_b32 %[r2], 63, %[tid]\n\tv_lshlrev_b32 %[r2], 3, %[r2]\n\tv_lshl_or_b32 %[r2], %[tl], 9, %[r2]\n\ts_lshl_b32 %[td], %[b], 2\n\t"
      "s_waitcnt lgkmcnt(0)\n\t"
      "global_load_ushort %[r0], %[r3], %[lar]" PPOL "\n\tglobal_load_ushort %[r1], %[r3], %[aim]" PPOL "\n\t"
      "global_load_dword %[r2], %[r2], %[oth]\n\t"
      "global_load_ushort %[r2], %[r3], %[cre]" PPOL "\n\tglobal_load_ushort %[r3], %[r3], %[cim]" PPOL "\n\t"
      "s_load_dword %[tg], %[ws], 0\n\ts_load_dword %[tl], %[ldt], %[td]\n\ts_load_dword %[td], %[dsk], %[td]\n1:"
      : [lar] "=&s"(a_lar), [aim] "=&s"(a_aim), [cre] "=&s"(a_cre), [cim] "=&s"(a_cim), [ldt] "=&s"(a_ldt), [dsk] "=&s"(a_dsk), [ws] "=&s"(a_ws),
        [oth] "=&s"(a_oth), [wv] "=&s"(wave), [tg] "=&s"(ctag), [tl] "=&s"(t_ldt), [td] "=&s"(t_dsk),
        [r0] "=&v"(r_lar), [r1] "=&v"(r_aim), [r2] "=&v"(r_cre), [r3] "=&v"(r_cim)
      : [ka] "s"(__builtin_amdgcn_kernarg_segment_ptr()), [tid] "v"(threadIdx.x), [b] "s"(blockIdx.x), [pp] "i"(PPAIR) : "scc");
  SB();
  const u32 w = wave & 3;
  const u32 b = blockIdx.x, xcd = b & 7, bi = b >> 3;
  const u32 hf = XMAP ? (xcd ^ (xcd >> 2)) & 1 : xcd & 1, p = 4 * (XMAP ? xcd & 3 : xcd >> 1) + (bi >> 3), jj = bi & 7;
  const bool spare = jj == 7;
  const u32 cb1 = 7 * hf + jj, rb = 2 * p + hf, pair = b;
  const u32 wm = w >> 1, wn = w & 1;
#define LANES() const u32 tid = opq(threadIdx.x) & 255, lane = tid & 63, hh = lane >> 5, blk = (lane >> 4) & 1, q4 = (lane & 15) >> 2, p4 = lane & 3, g4 = lane >> 4, l16 = opq(lane * 16)
  const u32 rot = 2 * (bi >> 3) + jj;

  if (wave >= 4) {
    u32x16 k0;
    u32x4 k1;
    asm volatile("s_load_dwordx16 %0, %2, 0\n\ts_load_dwordx4 %1, %2, 120" : "=&s"(k0), "=&s"(k1) : "s"(__builtin_amdgcn_kernarg_segment_ptr()));
    SB();
    LANES();
    const u32 ta = opq(sb + TAB + tid * 224);
    const u32 t0 = opq(sb + TAB + (wm * 128 + 4 * g4 + q4) * 224 + 8 * p4), t1 = opq(t0 + 32 * wn);
    const u32 ko = opq(sb + KD + wm * KD_CH + (4 * g4 * 36 + 16 * wn + (lane & 15)) * 2);
    char* const ws = (char*)a_ws;
    const __amdgpu_buffer_rsrc_t sWS = __builtin_amdgcn_make_buffer_rsrc((void*)ws, 0, WS_END, 0x00020000);
    u32 ga[9];
    {
      auto cell = [&](u32 pos) {
        const u32 hi = pos >> 8, ex = min(3u, 32u - (u32)__clz(hi));
        return sb + KD + (4 * ex + ((8 * hi) >> ex)) * 72 + ((pos & 255) >> 2);
      };
      const u32 P = 2040 - 8 * tid, a0 = cell(P), a8 = cell(P + 8);
      const u32 c0 = w == 3 ? 0x83222100u : w == 2 ? 0x88111000u : 0x88880000u, c1 = w == 3 ? 0x76543210u : w == 2 ? 0x88111000u : 0x88800000u;
      const u32 code = (w == 3 ? hh : (lane & 1)) ? c1 : c0;
#pragma unroll
      for (int k = 0; k < 8; k++) {
        const u32 n = (code >> (4 * k)) & 15u;
        ga[k + 1] = opq(n == 8 ? a8 : a0 + n * 72);
      }
      const u32 lowa = opq(P == 0 ? sb + KD + KD_ZERO : sb + KD + 7 * 72 + ((P - 8) >> 2));
      ga[0] = opq(P > 256 ? a0 : lowa);
    }
    *(LAS u32*)(hh ? sb + CNT + (lane & 15) * 4 : sb + KD + (lane & 1) * KD_CH + KD_ZERO) = 0;
    const u32 to = opq(sb + TC + tid * 16);
    const u32 ur = opq(sb + hf * 8192 + (lane & 7) * 1024 + (((lane >> 3) ^ (4 * (lane & 1))) << 4) + 4 * w);
    const u32 uw = opq(WS_UT + ((rot & 7) * 64 + (lane >> 3) * 8 + 2 * w) * UTS + rb * 128 + (lane & 7) * 16);

    STN(1);
    asm volatile("s_waitcnt lgkmcnt(0)" : "+s"(ctag), "+s"(t_ldt), "+s"(t_dsk), "+s"(k0), "+s"(k1)::"memory");
    asm volatile("s_waitcnt vmcnt(0)" : "+v"(r_lar), "+v"(r_aim), "+v"(r_cre), "+v"(r_cim)::"memory");
    const u32 tag = ctag + 1, cb2 = colblock(jj, k0[8]);
    ST(2);
    {
      const float dt = rbf(__expf(bf2f(wm ? t_ldt >> 16 : t_ldt & 0xffffu)));
      const float er = -(rbf(__expf(bf2f(r_lar))) * dt) * 1.44269504088896f;
      const float wi = (bf2f(r_aim) * dt) * 0.159154943091895f;
      auto seed = [&](float t) {
        const float m = __builtin_amdgcn_exp2f(er * t), a = __builtin_amdgcn_fractf(wi * t);
        return f32x2{m * __builtin_amdgcn_cosf(a), m * __builtin_amdgcn_sinf(a)};
      };
      auto put8 = [&](const f32x2* v, u32 off) {
        u32x4 re = {pk2(v[0].x, v[1].x), pk2(v[2].x, v[3].x), pk2(v[4].x, v[5].x), pk2(v[6].x, v[7].x)};
        u32x4 im = {pk2(v[0].y, v[1].y), pk2(v[2].y, v[3].y), pk2(v[4].y, v[5].y), pk2(v[6].y, v[7].y)};
        *(LAS u32x4*)((LAS char*)ta + off) = re;
        *(LAS u32x4*)((LAS char*)ta + 112 + off) = im;
      };
      f32x2 v[8];
      const Mul w8 = mk(seed(8.f)), w64 = mk(seed(64.f));
      v[0] = f32x2{1.f, 0.f}; v[1] = w8.b; v[2] = cmul(v[1], w8);
      const Mul w16 = mk(v[2]);
#pragma unroll
      for (int k = 3; k < 8; k++) v[k] = cmul(v[k - 2], w16);
      put8(v, 0); SB();
      v[0] = w64.b;
#pragma unroll
      for (int r = 1; r < 4; r++) {
#pragma unroll
        for (int k = (r == 1); k < 8; k++) v[k] = cmul(v[k], w64);
        put8(v, 16 * r); SB();
      }
      f32x2 s1 = seed(1.f), s256 = seed(256.f);
      s1.y = -s1.y; s256.y = -s256.y;
      const Mul v1 = mk(s1), W = mk(s256), v2c = mk(cmul(s1, v1));
      f32x2 u[8];
      u[0] = f32x2{2.f * (bf2f(r_cre) * dt), -2.f * (bf2f(r_cim) * dt)};
      u[1] = cmul(u[0], v1);
#pragma unroll
      for (int k = 2; k < 8; k++) u[k] = cmul(u[k - 2], v2c);
      put8(u, 64); SB();
      v[0] = cmul(u[0], W); v[1] = cmul(u[2], W); v[2] = cmul(u[4], W); v[3] = cmul(u[6], W);
      v[4] = cmul(v[0], W); v[5] = cmul(v[2], W); v[6] = cmul(v[4], W); v[7] = cmul(v[5], W);
      u[0] = cmul(v[6], W); u[1] = cmul(u[0], W); u[2] = cmul(u[1], W); u[3] = cmul(u[2], W); u[4] = cmul(u[3], W);
      put8(v, 80);
      u[5] = u[6] = u[7] = f32x2{0.f, 0.f};
      put8(u, 96);
      ST(3);
      BAR();
      ST(14);
      f32x4 ka[2] = {{(lane == 0 && wn == 0) ? bf2f(wm ? t_dsk >> 16 : t_dsk & 0xffffu) : 0.f, 0.f, 0.f, 0.f}, {0.f, 0.f, 0.f, 0.f}};
      u32x4 fv[8], f0[8], f1[8];
#pragma unroll
      for (int ks = 0; ks < 10; ks++) {
        const u32 o = ks * 16 * 224;
        if (ks < 8) { fv[ks] = ldstr(t1, o, o + 112); f0[ks] = ldstr(t0, o + 64, o + 64 + 112); f1[ks] = ldstr(t0, o + 96, o + 96 + 112); }
        if (ks >= 2) { ka[0] = MF16(f0[ks - 2], fv[ks - 2], ka[0]); ka[1] = MF16(f1[ks - 2], fv[ks - 2], ka[1]); }
        SB();
      }
#pragma unroll
      for (int rt = 0; rt < 2; rt++)
#pragma unroll
        for (int r = 0; r < 4; r += 2) {
          const u32 pr = pk2(ka[rt][r], ka[rt][r + 1]);
          *(LAS u16*)((LAS char*)ko + (16 * rt + r) * 72) = (u16)pr;
          *(LAS u16*)((LAS char*)ko + (16 * rt + r + 1) * 72) = (u16)(pr >> 16);
        }
    }
    ST(9);
    BAR();
    ST(4);
    {
      u32 xr[8];
#pragma unroll
      for (int j = 0; j < 8; j++) xr[j] = *(const LAS u32*)((LAS char*)(ur ^ ((j >> 1) << 4)) + j * 128);
#pragma unroll
      for (int c = 0; c < 2; c++) {
        u32x4 ou;
#pragma unroll
        for (int e = 0; e < 4; e++) ou[e] = __builtin_amdgcn_perm(xr[2 * e + 1], xr[2 * e], c ? 0x07060302 : 0x05040100);
        __builtin_amdgcn_raw_buffer_store_b128(ou, sWS, uw + c * UTS, 0, 16);
      }
    }
    ST(10);
    if (!spare) BAR();
    const u32 ufo = opq(WS_UF + (pair >> 5) * 128 + (lane & 31) * 4);
    const u32 ua = opq(sb + US + wm * US_STR + (256 - 16 - 128 * wn - 96 + 16 * (lane & 15) + 8 * g4) * 2);
    u32 tb[4];
#pragma unroll
    for (int j = 0; j < 4; j++) tb[j] = opq(sb + TC + (2 * wm + (lane & 1)) * TC_STR + (2046 - 16 - 128 * wn - 96 - 1792 + 8 * g4 - (lane & 14)) * 2 + 1024 * j);
    const u32 cw = opq(sb + CNT + 4 * w), cr = opq(sb + CNT + 4 * (lane & 3));
    auto arrive = [&](u32 n) { *(volatile LAS u32*)cw = n; };
    auto meet = [&](u32 n) { LGKM0(); arrive(n); lds_wait(cr, n); };
    if (spare) arrive(1);
    VMCNT(0);
    if (!spare) BAR();
    else {
      meet(2);
      if (tid == 0) asm volatile("buffer_store_dword %0, %1, %2, 0 offen sc1" ::"v"(tag), "v"(WS_UF + (rot & 7) * 128 + rb * 4), "s"(sWS) : "memory");
    }
    ST(5);
    const u32 row = tid >> 3, ck = lane & 7;
    const u32 ro = opq(sb + RED + w * 6144 + l16), r1 = opq(sb + RED + (w ^ 1) * 6144 + l16), r2 = opq(sb + RED + (w ^ 2) * 6144 + l16), r3 = opq(sb + RED + (w ^ 3) * 6144 + l16);
    const u32 ao = opq(WS_AT + pair * ATS + (8 * w + g4) * 512 + (lane & 15) * 16);
    const u32 sa = opq(sb + 2 * SLOT + ck * 1024 + ((row ^ (2 * ck)) << 4));
    if (!spare) BAR();
    const u32 ato = opq(WS_AT + (32 * cb2 + 8 * w + hh) * ATS + rb * 512 + (lane & 31) * 16);
    const u32 go = opq(WS_G + rb * 512 + row * 16);
    const u32 oo = opq((64 * rb + row) * 1024 + cb2 * 128 + ck * 16);
    const u32 aw = opq(sb + TC + (lane & 31) * 144 + (8 * w + hh) * 4), ar = opq(sb + TC + row * 144 + ck * 16);
    const u32 zo = opq(wn ? sb + US + hh * US_STR + (lane & 31) * 16 : sb + TC + ((lane >> 1) & 3) * TC_STR + 4096 + (lane & 1) * 16);
    auto fetch_u = [&]() {
#pragma unroll
      for (int q = 0; q < 8; q++) {
        if (!(q & 3)) SETM0(sb, US + 512 + (q >> 2) * US_STR);
        DMAP(" sc1", l16, sWS, WS_UT + (2 * pair + (q >> 2)) * UTS, q & 3);
      }
    };
    bool got = w != 0;
    if (!spare) {
      BAR();
#pragma unroll
      for (int i = 0; i < 2; i++) {
        if (got || i < UTRY) {
          BAR(); BAR();
        } else {
          u32 uf;
          asm volatile("buffer_load_dword %0, %1, %2, 0 offen sc1\n\ts_barrier\n\ts_barrier\n\ts_waitcnt vmcnt(0)" : "=&v"(uf) : "v"(ufo), "s"(sWS) : "memory");
          got = __all(uf == tag);
          if (got) fetch_u();
        }
      }
    }
    __builtin_amdgcn_s_setprio(CVPRIO);
    ST(21);
    {
      u32 ek[2][9];
#pragma unroll
      for (int i = 0; i < 9; i++) { ek[0][i] = *(const LAS u16*)ga[i]; ek[1][i] = *(const LAS u16*)((LAS char*)ga[i] + KD_CH); }
      *(LAS u32x4*)zo = u32x4{0, 0, 0, 0};
#pragma unroll
      for (int i = 0; i < 4; i++) {
        const u32* e = ek[i >> 1] + (i & 1);
        *(LAS u32x4*)((LAS char*)to + i * TC_STR) = u32x4{e[7] | (e[6] << 16), e[5] | (e[4] << 16), e[3] | (e[2] << 16), e[1] | (e[0] << 16)};
      }
    }
    if (!got) {
      __builtin_amdgcn_s_setprio(3);
      if (spare) __builtin_amdgcn_s_sleep(SPWAIT);
      _Pragma("unroll 1") for (u32 spin = 0; spin < SPIN_CAP; spin++) {
        u32 uf;
        asm volatile("buffer_load_dword %0, %1, %2, 0 offen sc1\n\ts_waitcnt vmcnt(0)" : "=&v"(uf) : "v"(ufo), "s"(sWS) : "memory");
        if (__all(uf == tag)) break;
        __builtin_amdgcn_s_sleep(USLEEP);
      }
      fetch_u();
      __builtin_amdgcn_s_setprio(CVPRIO);
    }
    VMCNT(0);
    ST(6);
    meet(4);
    ST(7);

    f32x4 ca[8];
#pragma unroll
    for (int c = 0; c < 8; c++) ca[c] = f32x4{0.f, 0.f, 0.f, 0.f};
    u32x4 fu[2][8];
    u32x2 ft[2][16];
    auto cread = [&](int f, int r) {
      const int k = r < 2 ? 8 + r : r < 10 ? r - 2 : r;
      if (k < 8) fu[f & 1][k] = lds128(ua, (96 - 32 * f + 256 * k) * 2);
      else {
        const int i = (k - 8) >> 1, hl = k & 1, e = 7 - i;
        const LAS u32* t = (const LAS u32*)((LAS char*)tb[e >> 1] + (96 - 32 * f + 256 * (e & 1)) * 2 + 8 * hl);
        ft[f & 1][2 * i + hl] = u32x2{t[0], t[1]};
      }
    };
#pragma unroll
    for (int r = 0; r < 24; r++) { cread(0, r); SB(); }
#pragma unroll
    for (int f = 0; f < 4; f++) {
      int m = 0;
#pragma unroll
      for (int i = 0; i < 8; i++)
#pragma unroll
        for (int e = 0; e + i < 8; e++, m++) {
          const u32x4 tf = {ft[f & 1][2 * i][0], ft[f & 1][2 * i][1], ft[f & 1][2 * i + 1][0], ft[f & 1][2 * i + 1][1]};
          ca[i + e] = MF16(fu[f & 1][e], tf, ca[i + e]);
          if (f < 3 && m >= 4 && m < 28) cread(f + 1, m - 4);
          if (f == 3 && m == 0) arrive(5);
          SB();
        }
    }
    ST(8);
    f32x4 keep[2];
    __builtin_amdgcn_s_setprio(3);
    lds_wait(cr, 5);
#define SEND(W) { _Pragma("unroll") for (int j = 1; j < 4; j++) { *(LAS f32x4*)((LAS char*)ro + (j - 1) * 2048) = ca[2 * (W ^ j)]; *(LAS f32x4*)((LAS char*)ro + (j - 1) * 2048 + 1024) = ca[2 * (W ^ j) + 1]; } \
                  keep[0] = ca[2 * W]; keep[1] = ca[2 * W + 1]; asm volatile("; send " #W ::: "memory"); }
    if (w == 0) SEND(0) else if (w == 1) SEND(1) else if (w == 2) SEND(2) else SEND(3)
    asm volatile("" ::: "memory");
    arrive(6);
    lds_wait(cr, 6);
    const u32 sel = wm ? 0x01000302 : 0x03020100;
    f32x4 pp[2][3];
#pragma unroll
    for (int c = 0; c < 2; c++) {
      pp[c][0] = *(const LAS f32x4*)((LAS char*)r1 + c * 1024);
      pp[c][1] = *(const LAS f32x4*)((LAS char*)r2 + 2048 + c * 1024);
      pp[c][2] = *(const LAS f32x4*)((LAS char*)r3 + 4096 + c * 1024);
    }
#pragma unroll
    for (int c = 0; c < 2; c++) {
      u32 o[4];
#pragma unroll
      for (int r = 0; r < 4; r++) {
        const u32 v = pk2(add1(keep[c][r], pp[c][0][r]), add1(pp[c][1][r], pp[c][2][r]));
        o[r] = __builtin_amdgcn_perm(v, v, sel);
      }
#pragma unroll
      for (int r = 0; r < 2; r++) __builtin_amdgcn_raw_buffer_store_b128(u32x4{o[r], tag, o[r + 2], tag}, sWS, ao + c * 2048 + r * 256, 0, 16);
    }
    arrive(7);
    ST(11);
    const __amdgpu_buffer_rsrc_t sB2 = __builtin_amdgcn_make_buffer_rsrc((void*)mk64(k1[0], k1[1]), 0, D * 2, 0x00020000);
    const __amdgpu_buffer_rsrc_t sOut = __builtin_amdgcn_make_buffer_rsrc((void*)mk64(k0[14], k0[15]), 0, L * D * 2, 0x00020000);
    const u32x4 b2v = __builtin_amdgcn_raw_buffer_load_b128(sB2, ck * 16, cb2 * 128, 0);
    lds_wait<LSL>(cr + RDY - CNT, 1);
    ST(12);
    u32x4 gg, at[4];
    _Pragma("unroll 1") for (u32 spin = 0; spin < SPIN_CAP; spin++) {
      asm volatile("" ::: "memory");
#pragma unroll
      for (int j = 0; j < 4; j++) at[j] = __builtin_amdgcn_raw_buffer_load_b128(sWS, ato, 2 * j * ATS, 16);
      gg = __builtin_amdgcn_raw_buffer_load_b128(sWS, go, 0, 16);
      bool ok = gg[1] == tag && gg[3] == tag;
#pragma unroll
      for (int j = 0; j < 4; j++) ok &= at[j][1] == tag && at[j][3] == tag;
      if (__all(ok)) break;
      __builtin_amdgcn_s_sleep(RSLEEP);
    }
    __builtin_amdgcn_s_setprio(TPRIO);
    ST(13);
    lds_wait(cr, 7);
#pragma unroll
    for (int j = 0; j < 4; j++) {
      *(LAS u32*)((LAS char*)aw + j * 8) = at[j][0];
      *(LAS u32*)((LAS char*)aw + j * 8 + 32 * 144) = at[j][2];
    }
    asm volatile("" ::: "memory");
    arrive(8);
    lds_wait(cr, 8);
    ST(15);
#pragma unroll
    for (int t = 0; t < 2; t++) {
      const float g = bf2f(gg[2 * t] & 0xffffu), gm = hi2f(gg[2 * t]);
      const u32x4 av = lds128(ar, t * 32 * 144);
      float base[8];
#pragma unroll
      for (int i = 0; i < 4; i++) {
        base[2 * i] = fopq(fma1(gm, bf2f(b2v[i] & 0xffffu), mul1(g, bf2f(av[i] & 0xffffu))));
        base[2 * i + 1] = fopq(fma1(gm, hi2f(b2v[i]), mul1(g, hi2f(av[i]))));
      }
      if (t == 0) { ST(16); lds_wait<LSL>(cr + RDY - CNT, 2); } else { ST(18); asm volatile("s_barrier" ::: "memory"); ST(19); }
      f32x4 pt[4][2];
#pragma unroll
      for (int k = 0; k < 4; k++)
#pragma unroll
        for (int q = 0; q < 2; q++) pt[k][q] = *(const LAS f32x4*)((LAS char*)sa + (1 - t) * SLOT + k * 8192 + q * 512);
      u32x4 ow;
#pragma unroll
      for (int i = 0; i < 4; i++) {
        float o[2];
#pragma unroll
        for (int e = 0; e < 2; e++) {
          const int q = i >> 1, r = 2 * (i & 1) + e;
          o[e] = t ? __builtin_fmaf(gm, (pt[0][q][r] + pt[1][q][r]) + (pt[2][q][r] + pt[3][q][r]), base[2 * i + e])
                   : fma1(gm, add1(add1(pt[0][q][r], pt[1][q][r]), add1(pt[2][q][r], pt[3][q][r])), base[2 * i + e]);
        }
        ow[i] = pk2(o[0], o[1]);
      }
      __builtin_amdgcn_raw_buffer_store_b128(ow, sOut, oo + t * 32768, 0, OUTPOL);
      SB();
      if (t == 0) ST(17);
    }
    asm volatile(".Lcode_end:");
    STAMP_OUT();
    return;
  }

  u32x16 k0;
  u32x4 k1;
  asm volatile("s_load_dwordx16 %0, %2, 0\n\ts_load_dwordx4 %1, %2, 112"
               : "=&s"(k0), "=&s"(k1) : "s"(__builtin_amdgcn_kernarg_segment_ptr()));
  SB();
  LANES();
  const u32 o1 = opq(((spare ? 0 : cb1) * 128 + wn * 64 + (lane & 31)) * 2);
  u32 vA[4], vB[4], v2[4];
#pragma unroll
  for (int q = 0; q < 4; q++) {
    vA[q] = opq((lane >> 3) * 1024 + (((lane & 7) ^ g4 ^ (4 * (q & 1))) << 4) + q * (8192 - 1024));
    vB[q] = opq(g4 * (F * 2) + (((lane & 15) ^ (g4 << 2) ^ q) << 4) + q * (4 * F * 2 - 1024));
    v2[q] = opq((lane >> 3) * 1024 + (((lane & 7) ^ (blk << 2)) << 4) + q * (8192 - 1024));
  }
  const u32 xbase = sopq((128 * p + 32 * w) * 1024), wbase = sopq(16 * w * (F * 2) + cb1 * 256);
  const u32 m4 = sopq(sb + w * 4096), m8 = sopq(sb + w * 8192);
  asm volatile("s_waitcnt lgkmcnt(0)" : "+s"(k0), "+s"(k1) : "s"(a_lar), "s"(a_aim), "s"(a_cre), "s"(a_cim), "s"(a_ldt), "s"(a_dsk), "s"(a_ws));
  ST(14);
  const u32x4 sX = k0.s0123, sW1 = k0.s4567;
  const u32x4 sW2 = k0.s89ab;
  char* const ws = (char*)mk64(k0[12], k0[13]);
  const u32 w2base = 28 * w * 16384 + colblock(jj, sW2[0]) * 128;
  u32 tag;
  const __amdgpu_buffer_rsrc_t sWS = __builtin_amdgcn_make_buffer_rsrc((void*)ws, 0, WS_END, 0x00020000);
  auto w2piece = [&](int c, int q) {
    if ((q & 3) == 0) SETM0(m8, (c & 3) * SLOT + (q >> 2) * 4096);
    DMAQ(v2[q & 3], sW2, w2base + c * 65536 + (q >> 2) * 32768, q & 3);
  };

  if (!spare) {
    auto g1piece = [&](int g, int q) {
      if (q == 0) SETM0(m4, (g & 3) * SLOT);
      if (q == 4) SETM0(m4, (g & 3) * SLOT + 16384);
      if (q < 4) DMAP(XPOL, vA[q], sX, xbase + ((g + rot) & 7) * 128, q);
      else DMAQ(vB[q - 4], sW1, wbase + ((g + rot) & 7) * 64 * (F * 2), q - 4);
    };
    u32 r_b1a, r_b1b;
    asm volatile("s_load_dword %0, %1, 0" : "=s"(tag) : "s"(ws));
    asm volatile("global_load_ushort %0, %1, %2" : "=v"(r_b1a) : "v"(o1), "s"(mk64(k1[0], k1[1])));
    asm volatile("global_load_ushort %0, %1, %2 offset:64" : "=v"(r_b1b) : "v"(o1), "s"(mk64(k1[0], k1[1])));
#pragma unroll
    for (int i = 0; i < 12; i++) g1piece(i >> 3, i < 8 ? i : i - 4);
    ST(1);
    u32 ax[2][4], aw[2][4];
    {
      const u32 xa = sb + (64 * wm + (lane & 31)) * 128 + ((hh ^ ((lane >> 1) & 7)) << 4);
      const u32 wa = sb + 16384 + 256 * (8 * hh + q4) + 8 * (p4 & 1) + (((8 * wn + 2 * blk + (p4 >> 1)) ^ ((q4 << 2) | (2 * hh))) << 4);
#pragma unroll
      for (int k = 0; k < 4; k++) {
        ax[0][k] = opq(xa ^ (k << 5)); aw[0][k] = opq(wa ^ ((k & 1) << 4) ^ ((k >> 1) << 6));
        ax[1][k] = opq(ax[0][k] + 2 * SLOT); aw[1][k] = opq(aw[0][k] + 2 * SLOT);
      }
    }
    const u32 ho = opq(l16 + WS_H + ((4 * p + 2 * wm) * 112 + cb1 * 8 + wn * 4) * 1024), ho1 = opq(ho + 112 * 1024);
    asm volatile("s_waitcnt lgkmcnt(0)" : "+s"(tag));
    const u32 vtag = opq(tag + 1), vuf = opq(WS_UF + (rot & 7) * 128 + rb * 4);
#pragma unroll
    for (int q = 0; q < 8 * LA - 12; q++) {
      if (q == PRET) { asm volatile("s_barrier" ::: "memory"); ST(3); }
      g1piece(q < 4 ? 1 : 2, q < 4 ? q : q - 4);
    }
    if (PRET >= 8 * LA - 12) { asm volatile("s_barrier" ::: "memory"); ST(3); }
    asm volatile("s_waitcnt vmcnt(%2)" : "+v"(r_b1a), "+v"(r_b1b) : "i"(8 * LA));
    f32x16 acc[2][2];
    {
      const u32x4 one = {hh ? 0u : 0x3f80u, 0u, 0u, 0u};
      const u32x4 ba = {r_b1a, 0u, 0u, 0u}, bb = {r_b1b, 0u, 0u, 0u};
      asm volatile("s_nop 1\n\tv_mfma_f32_32x32x16_bf16 %0, %4, %6, 0\n\tv_mfma_f32_32x32x16_bf16 %1, %4, %6, 0\n\t"
                   "v_mfma_f32_32x32x16_bf16 %2, %5, %6, 0\n\tv_mfma_f32_32x32x16_bf16 %3, %5, %6, 0"
                   : "=&v"(acc[0][0]), "=&v"(acc[0][1]), "=&v"(acc[1][0]), "=&v"(acc[1][1]) : "v"(ba), "v"(bb), "v"(one));
    }
    u32x4 fx[2][2];
    s16x4 fw[2][4];
    VMCNT(8 * LA - 8);
    ST(2);
    BAR();
    ST(4);
    fx[0][0] = lds128(ax[0][0]); fw[0][0] = tr64(aw[0][0], 0); fw[0][1] = tr64(aw[0][1], 1024);
    fx[0][1] = lds128(ax[0][0], 4096); fw[0][2] = tr64(aw[0][2], 0); fw[0][3] = tr64(aw[0][3], 1024);
#pragma unroll
    for (int s = 0; s < 8; s++) {
      auto piece = [&](int q) { if (s + LA < 8) g1piece(s + LA, q); else if (s + LA - 8 < WPRE) w2piece(s + LA - 8, q); };
#pragma unroll
      for (int k = 0; k < 4; k++) {
        const int c = k & 1, n = c ^ 1, kn = (k + 1) & 3, sl = (k == 3 ? s + 1 : s) & 3, hi = sl >> 1;
        const u32 o = (sl & 1) * SLOT, ow = o + kn * 4096;
        const bool more = s < 7 || k < 3;
        MMV(acc[0][0], cat(fw[c][0], fw[c][1]), fx[c][0]);
        if (k == 3) {
          if (s < 7) VMCNT((LA == 3 && s + 2 < 8 + WPRE ? 8 : 0) + (s + LA < 8 + WPRE ? 6 : 0));
          BAR();
        }
        if (more) { fx[n][0] = lds128(ax[hi][kn], o); fw[n][0] = tr64(aw[hi][0], ow); fw[n][1] = tr64(aw[hi][1], ow + 1024); }
        MMV(acc[0][1], cat(fw[c][0], fw[c][1]), fx[c][1]);
        if (more) { fx[n][1] = lds128(ax[hi][kn], o + 4096); fw[n][2] = tr64(aw[hi][2], ow); }
        piece(2 * k);
        MMV(acc[1][0], cat(fw[c][2], fw[c][3]), fx[c][0]);
        if (more) fw[n][3] = tr64(aw[hi][3], ow + 1024);
        MMV(acc[1][1], cat(fw[c][2], fw[c][3]), fx[c][1]);
        piece(2 * k + 1);
      }
      if (s == 1)
        asm volatile("s_cmp_lg_u32 %3, 0\n\ts_cbranch_scc1 1f\n\ts_mov_b64 exec, 1\n\tbuffer_store_dword %0, %1, %2, 0 offen sc1\n\ts_mov_b64 exec, -1\n1:"
                     ::"v"(vtag), "v"(vuf), "s"(sWS), "s"(w) : "memory", "scc");
      if (s == 0) ST(8);
      if (s == 1) ST(15);
      if (s == 2) ST(16);
      if (s == 3) ST(17);
      if (s == 4) ST(19);
      if (s == 5) ST(20);
      if (s == 6) ST(21);
    }
    asm volatile("s_nop 7\n\ts_nop 7" : "+v"(acc[0][0]), "+v"(acc[0][1]), "+v"(acc[1][0]), "+v"(acc[1][1]));
    __builtin_amdgcn_s_setprio(2);
    ST(5);
#pragma unroll
    for (int u = 0; u < 2; u++)
#pragma unroll
      for (int t = 0; t < 2; t++)
#pragma unroll
        for (int s = 0; s < 2; s++) {
          u32x4 o;
#pragma unroll
          for (int e = 0; e < 4; e++) {
            const float a0 = relu(acc[u][t][8 * s + 2 * e]), a1 = relu(acc[u][t][8 * s + 2 * e + 1]);
            o[e] = EPK ? pk2(a0 * a0, a1 * a1) : pk2(mul1(a0, a0), mul1(a1, a1));
          }
          __builtin_amdgcn_raw_buffer_store_b128(o, sWS, (t ? ho1 : ho) + (2 * u + s) * 1024, 0, 16);
          SB();
        }
    tag += 1;
  } else {
    asm volatile("s_load_dword %0, %1, 0" : "=s"(tag) : "s"(ws));
    SETM0(m4, 0);
#pragma unroll
    for (int q = 0; q < 4; q++) DMAP(XPOL, vA[q], sX, xbase + (rot & 7) * 128, q);
    u32x4 gwv, xr[16];
    asm volatile("global_load_dwordx4 %0, %1, %2" : "=v"(gwv) : "v"(l16), "s"(((const CAS Args*)__builtin_amdgcn_kernarg_segment_ptr())->gw));
#pragma unroll
    for (int j = 0; j < 16; j++) {
      if (j == 7) asm volatile("s_barrier" ::: "memory");
      asm volatile("buffer_load_dwordx4 %0, %1, %2, %3 offen offset:%4" : "=v"(xr[j]) : "v"(l16), "s"(sX), "s"((64 * rb + 16 * w + (j & ~3)) * 1024), "i"((j & 3) * 1024));
    }
    asm volatile("s_waitcnt vmcnt(17) lgkmcnt(0)\n\ts_barrier" : "+s"(tag)::"memory");
    tag += 1;
    ST(2);
    asm volatile("s_waitcnt vmcnt(0)" : "+v"(gwv), "+v"(xr[0]), "+v"(xr[1]), "+v"(xr[2]), "+v"(xr[3]), "+v"(xr[4]), "+v"(xr[5]), "+v"(xr[6]), "+v"(xr[7]),
                 "+v"(xr[8]), "+v"(xr[9]), "+v"(xr[10]), "+v"(xr[11]), "+v"(xr[12]), "+v"(xr[13]), "+v"(xr[14]), "+v"(xr[15])::"memory");
    SETM0(m8, SLOT);
    auto pull = [&](u32 n) {
#pragma unroll
      for (int q = 0; q < 4; q++) DMAQ(l16, sW2, min(14 * xcd + 4 * n + (bi >> 3), 111u) * 16384 + w * 4096, q);
    };
    pull(0); pull(1);
    ST(8);
    const u32 zs = m8 + 2 * SLOT;
#pragma unroll
    for (int j = 0; j < 16; j++) {
      float z = 0.f;
#pragma unroll
      for (int e = 0; e < 4; e++) z = dot2(xr[j][e], gwv[e], z);
      *(LAS float*)((LAS char*)(zs + lane * 4) + j * 272) = z;
    }
    VMCNT(4); pull(2);
    LGKM0();
    f32x4 z4 = {};
#pragma unroll
    for (int k = 0; k < 4; k++) z4 += *(const LAS f32x4*)((LAS char*)(zs + (lane & 15) * 272 + g4 * 64) + k * 16);
    float zz = (z4[0] + z4[1]) + (z4[2] + z4[3]);
    zz += __shfl_xor(zz, 16); zz += __shfl_xor(zz, 32);
    const float g = rbf(1.f / (1.f + __expf(-zz)));
    VMCNT(4); pull(3);
    if (lane < 16) __builtin_amdgcn_raw_buffer_store_b64(u32x2{pk2(g, 1.f - g), tag}, sWS, WS_G + rb * 512 + (wn * 16 + lane) * 16 + wm * 8, 0, 16);
    lds_wait(sb + CNT + 4 * (lane & 3), 1);
    ST(4);
#pragma unroll
    for (int i = 0; i < 8 * WPRE; i++) { if (!(i & 3)) VMCNT(THR); w2piece(i >> 3, i & 7); }
  }
  SB();
  ST(6);

  u32 w2a[2][2];
  {
    const u32 a = m8 + 128 * (4 * hh + q4) + 8 * (p4 & 1) + (((2 * blk + (p4 >> 1)) ^ ((q4 >> 1) << 2)) << 4);
    w2a[0][0] = opq(a); w2a[1][0] = opq(a ^ 64); w2a[0][1] = opq(a + 2 * SLOT); w2a[1][1] = opq((a ^ 64) + 2 * SLOT);
  }
  auto w2frag = [&](int st, int v) {
    const int sl = (st >> 2) & 3;
    const u32 o = (sl & 1) * SLOT + (st & 3) * 2048;
    return ldstr(w2a[v][sl >> 1], o, o + 1024);
  };
  u32x4 w2r[12][2];
  u32 fl = 0;
  const u32 fo = opq(WS_HF + p * 128 + (((lane & 7) == 7) ? 0 : (lane & 15) * 4));
#pragma unroll
  for (int c = WPRE; c < 8; c++) {
    if (c == WPRE + PUBAT) {
      VMCNT(8 * PUBAT);
      *(volatile LAS u32*)(sb + FD + 4 * w) = 1;
      if (w == 0 && !spare) {
        lds_wait(sb + FD + 4 * (lane & 3), 1);
        if (lane == 0) asm volatile("buffer_store_dword %0, %1, %2, 0 offen sc1" ::"v"(tag), "v"(WS_HF + p * 128 + (8 * hf + jj) * 4), "s"(sWS) : "memory");
      }
      ST(7);
      __builtin_amdgcn_s_sleep(WDELAY);
    }
    if (c >= 4 && c < 7) {
      VMCNT(24);
#pragma unroll
      for (int j = 0; j < 8; j++) w2r[4 * (c - 4) + (j >> 1)][j & 1] = w2frag(4 * (c - 4) + (j >> 1), j & 1);
      LGKM0();
    }
    if (c == POLLAT) asm volatile("s_cmp_lg_u32 %3, 3\n\ts_cbranch_scc1 1f\n\tbuffer_load_dword %0, %1, %2, 0 offen sc1\n1:" : "+v"(fl) : "v"(fo), "s"(sWS), "s"(w) : "memory", "scc");
    if (c < 7) {
#pragma unroll
      for (int q = 0; q < 8; q++) {
        if (!(q & 3)) asm volatile("s_cmp_lg_u32 %0, 7\n\ts_cbranch_scc1 1f\n\ts_waitcnt vmcnt(%1)\n1:" ::"s"(jj), "i"(THR) : "memory", "scc");
        w2piece(c, q);
      }
    }
  }
  const u32 hb = sopq(WS_H + (2 * rb * 112 + 28 * w) * 1024);
  const u32 xw = opq(m8 + 2 * SLOT + l16), rdw = opq(sb + RDY + 4 * w);
  asm volatile("s_waitcnt vmcnt(%1)" : "+v"(fl) : "i"(8 * (7 - POLLAT)) : "memory");
  ST(11);
  if (w == 3) {
    _Pragma("unroll 1") for (u32 spin = 0; spin < SPIN_CAP; spin++) {
      if (__all(fl == tag)) break;
      __builtin_amdgcn_s_sleep(HSLEEP);
      asm volatile("buffer_load_dword %0, %1, %2, 0 offen sc1\n\ts_waitcnt vmcnt(0)" : "=&v"(fl) : "v"(fo), "s"(sWS) : "memory");
    }
    *(volatile LAS u32*)(sb + GO) = 1;
  } else {
    lds_wait(sb + GO, 1);
  }
  __builtin_amdgcn_s_setprio(G2PRIO);
  ST(10);
  SB();

  u32x4 hfr[56];
  auto load_h = [&](int i) {
    const int st = step(i);
    hfr[i] = __builtin_amdgcn_raw_buffer_load_b128(sWS, l16 + (st & 3) * 1024, hb + ((i / 28) * 112 + (st & ~3)) * 1024, 16);
  };
#pragma unroll
  for (int i = 0; i < HEAD; i++) load_h(i);
  SB();
  f32x16 oc[2][2] = {};
  auto send = [&](int t, int j) {
    const f32x16& s = oc[t][j >> 2];
    const int r = 4 * (j & 3);
    *(LAS f32x4*)((LAS char*)(xw ^ (j << 5)) + (1 - t) * SLOT + j * 1024) = f32x4{s[r], s[r + 1], s[r + 2], s[r + 3]};
  };
  auto signal = [&](u32 n) { asm volatile("" ::: "memory"); *(volatile LAS u32*)rdw = n; };
  u32x4 wl[56][2];
  auto fetch_w = [&](int i) { if (i < 56 && step(i) >= 12) { wl[i][0] = w2frag(step(i), 0); wl[i][1] = w2frag(step(i), 1); } };
  sfor<0, 56>([&](auto ic) {
    constexpr int i = decltype(ic)::v, t = i / 28, st = step(i);
    constexpr int D2 = 53 - TAILN;
    if (i < D2 || !TAILN) fetch_w(i + 1); else { fetch_w(2 * i - D2 + 1); fetch_w(2 * i - D2 + 2); }
    SB();
    u32x4 a0, a1;
    if (st < 12) { a0 = w2r[st < 12 ? st : 0][0]; a1 = w2r[st < 12 ? st : 0][1]; } else { a0 = wl[i][0]; a1 = wl[i][1]; }
    oc[t][0] = MF32(a0, hfr[i], oc[t][0]);
    if (i + HEAD < 56) load_h(i + HEAD);
    SB();
    if (i < 56 - TAILN) oc[t][1] = MF32(a1, hfr[i], oc[t][1]);
    if (i == ATAT) signal(1);
    if (i >= 31 && i < 39) send(0, i - 31);
    if (i == 38) signal(2);
    SB();
    if (i == 27) ST(18);
  });
#pragma unroll
  for (int i = 0; i < TAILN; i++) {
    oc[1][1] = MF32(wl[56 - TAILN + i][1], hfr[56 - TAILN + i], oc[1][1]);
    SB();
    if (i >= 1 && i < 3) { send(1, 2 * i - 2); send(1, 2 * i - 1); }
    SB();
  }
  ST(12);
#pragma unroll
  for (int j = TAILN >= 3 ? 4 : 0; j < 8; j++) send(1, j);
  BAR();
  if (b == 0 && w == 3) {
    _Pragma("unroll 1") for (u32 spin = 0; spin < SPIN_CAP; spin++) {
      asm volatile("" ::: "memory");
      const u32x4 f0 = __builtin_amdgcn_raw_buffer_load_b128(sWS, WS_UF + l16, 0, 16);
      if (__all(f0[0] == tag && f0[1] == tag && f0[2] == tag && f0[3] == tag)) break;
    }
    if (lane == 0) __builtin_amdgcn_raw_buffer_store_b32(tag, sWS, 0, 0, 16);
  }
  STAMP_OUT();
}

extern "C" void kernel_launch(void* const* d_in, const int* in_sizes, int n_in, void* d_out, int out_size,
                              void* d_ws, size_t ws_size, hipStream_t stream) {
  Args a;
  a.x = Srd{d_in[0], L * D * 2, 0x00020000}; a.w1 = Srd{d_in[8], D * F * 2, 0x00020000}; a.w2 = Srd{d_in[10], D * F * 2, 0x00020000};
  a.ws = (char*)d_ws + WSO; a.out = (u16*)d_out;
  a.lar = (const u16*)d_in[1]; a.aim = (const u16*)d_in[2]; a.cre = (const u16*)d_in[3]; a.cim = (const u16*)d_in[4];
  a.ldt = (const u16*)d_in[5]; a.dsk = (const u16*)d_in[6]; a.b1 = (const u16*)d_in[9]; a.b2 = (const u16*)d_in[11];
  a.gw = (const u16*)d_in[12];
  fused_mix<<<256, 512, 0, stream>>>(a);
}
```

```cpp
#define STAMP 1
#define RT 1
#define STMASK 1
#include <hip/hip_runtime.h>
#pragma clang diagnostic ignored "-Wint-to-pointer-cast"

typedef unsigned short u16;
typedef unsigned u32;
typedef unsigned long long u64;
typedef __bf16 h16x8 __attribute__((ext_vector_type(8)));
typedef __bf16 h16x2 __attribute__((ext_vector_type(2)));
typedef short s16x4 __attribute__((ext_vector_type(4)));
typedef short s16x8 __attribute__((ext_vector_type(8)));
typedef short s16x2 __attribute__((ext_vector_type(2)));
typedef float f32x2 __attribute__((ext_vector_type(2)));
typedef float f32x4 __attribute__((ext_vector_type(4)));
typedef float f32x16 __attribute__((ext_vector_type(16)));
typedef u32 u32x2 __attribute__((ext_vector_type(2)));
typedef u32 u32x4 __attribute__((ext_vector_type(4)));
typedef u32 u32x16 __attribute__((ext_vector_type(16)));
#define LAS __attribute__((address_space(3)))
#define CAS __attribute__((address_space(4)))
#define GAS __attribute__((address_space(1)))
#define DI __device__ __forceinline__

constexpr int L = 2048, D = 512, F = 1792;
constexpr u32 WS_UF = 8192;
constexpr u32 WS_HF = 4096;
constexpr u32 WS_G = 1u << 19;
#define UTS 4096
#define ATS 16384
#define WSO 0
constexpr u32 WS_UT = 1u << 20;
constexpr u32 WS_AT = 4u << 20;
constexpr u32 WS_H = 9u << 20;
constexpr u32 WS_END = 16u << 20;
constexpr u32 WS_STAMP = 32u << 20;
constexpr u32 SLOT = 32768;
constexpr u32 TAB = 3 * SLOT;
constexpr u32 TC = 4 * SLOT;
constexpr u32 TC_STR = 4160;
constexpr u32 US = TC + 4 * TC_STR;
constexpr u32 US_STR = (256 + 2048) * 2;
constexpr u32 KD_CH = 32 * 36 * 2;
constexpr u32 LDS_BYTES = 163840;
constexpr u32 KD = LDS_BYTES - 2 * KD_CH;
constexpr u32 KD_ZERO = 34 * 2;
constexpr u32 CNT = KD - 64;
constexpr u32 FD = CNT + 16, RDY = CNT + 32, GO = CNT + 48;
constexpr u32 RED = TC;
static_assert(US + 2 * US_STR <= CNT && TAB + 256 * 224 + 16 <= CNT, "lds map");
constexpr u32 SPIN_CAP = 1u << 16;
#define EPK 0
#define WDELAY 0
#define CBO 4
#define TAILN 4
#define EXPE 0
#define LSL 4
#define TPRIO 3
#define UTRY 1
#define XMAP 1
#define PRET 0
#define THR 4
#define SPWAIT 32
#define HEAD 12
#define WPRE 3
#define PUBAT 0
#define PPOL ""
#define PPOL2 ""
#define XPOL ""
#define NPF 4
#define PFDELAY 24
#define POLLAT 6
#define PFC 0
#define LA 3
#define OUTPOL 16
#define UPUB 2
#define RSLEEP 8
#define USLEEP 8
#define HSLEEP 2
#define PPAIR 4
#define ATAT 16
#define G2PRIO 0
#define CVPRIO 1

DI float bf2f(u32 v) { return __uint_as_float(v << 16); }
DI u32 pk2(float lo, float hi) { f32x2 a = {lo, hi}; return __builtin_bit_cast(u32, __builtin_convertvector(a, h16x2)); }
DI float rbf(float x) { return bf2f(pk2(x, x) & 0xffffu); }
DI float dot2(u32 a, u32 b, float c) { return __builtin_amdgcn_fdot2_f32_bf16(__builtin_bit_cast(h16x2, a), __builtin_bit_cast(h16x2, b), c, false); }
DI u32 rfl(u32 x) { return __builtin_amdgcn_readfirstlane(x); }
struct Mul { f32x2 b, n; };
DI Mul mk(f32x2 b) { return Mul{b, f32x2{-b.y, b.y}}; }
DI f32x2 cmul(f32x2 a, Mul m) { return __builtin_elementwise_fma(a.yx, m.n, a * m.b.xx); }
DI float mul1(float a, float b) { float r; asm("v_mul_f32 %0, %1, %2" : "=v"(r) : "v"(a), "v"(b)); return r; }
DI float add1(float a, float b) { float r; asm("v_add_f32 %0, %1, %2" : "=v"(r) : "v"(a), "v"(b)); return r; }
DI float fma1(float a, float b, float c) { float r; asm("v_fma_f32 %0, %1, %2, %3" : "=v"(r) : "v"(a), "v"(b), "v"(c)); return r; }
DI float hi2f(u32 v) { return __uint_as_float(v & 0xffff0000u); }
DI float fopq(float x) { asm volatile("" : "+v"(x)); return x; }
DI float relu(float x) { return __int_as_float(max(__float_as_int(x), 0)); }
DI u32 sopq(u32 x) { asm volatile("" : "+s"(x)); return x; }
DI u32 opq(u32 x) { asm volatile("" : "+v"(x)); return x; }
#define MF32(a, b, c) __builtin_amdgcn_mfma_f32_32x32x16_bf16(__builtin_bit_cast(h16x8, a), __builtin_bit_cast(h16x8, b), c, 0, 0, 0)
#define MF16(a, b, c) __builtin_amdgcn_mfma_f32_16x16x32_bf16(__builtin_bit_cast(h16x8, a), __builtin_bit_cast(h16x8, b), c, 0, 0, 0)
#define DMA(voff, srd, soff, dst) \
  asm volatile("s_mov_b32 m0, %0\n\ts_nop 0\n\tbuffer_load_dwordx4 %1, %2, %3 offen lds" ::"s"(dst), "v"(voff), "s"(srd), "s"(soff) : "memory")
#define SETM0(base, off) asm volatile("s_add_u32 m0, %0, %1\n\ts_nop 0" ::"s"(base), "i"(off) : "memory", "scc")
#define DMAP(pol, voff, srd, soff, q) asm volatile("buffer_load_dwordx4 %0, %1, %2 offen offset:%3" pol " lds" ::"v"(voff), "s"(srd), "s"(soff), "i"((q) * 1024) : "memory")
#define DMAQ(voff, srd, soff, q) DMAP("", voff, srd, soff, q)
#define MMV(acc, a, b) asm volatile("v_mfma_f32_32x32x16_bf16 %0, %1, %2, %0" : "+v"(acc) : "v"(a), "v"(b) : "memory")
#define VMCNT(n) asm volatile("s_waitcnt vmcnt(%0)" ::"i"(n) : "memory")
#define LGKM0() asm volatile("s_waitcnt lgkmcnt(0)" ::: "memory")
#define BAR() asm volatile("s_waitcnt lgkmcnt(0)\n\ts_barrier" ::: "memory")
#define SB() __builtin_amdgcn_sched_barrier(0)
#define STI "s_memrealtime"
#define ST(i) do { if (!((STMASK >> (i)) & 1)) break; SB(); asm volatile(STI " %0\n\ts_waitcnt lgkmcnt(0)" : "=s"(stamp[(i) % 24])::"memory"); SB(); } while (0)
#define STN(i) do { if (!((STMASK >> (i)) & 1)) break; SB(); asm volatile(STI " %0" : "=s"(stamp[(i) % 24])::"memory"); SB(); } while (0)

template <int SL = 1> DI void lds_wait(u32 a, u32 n) {
  _Pragma("unroll 1") for (u32 spin = 0; spin < (1u << 22); spin++) {
    asm volatile("" ::: "memory");
    if (__all(*(volatile LAS u32*)a >= n)) break;
    __builtin_amdgcn_s_sleep(SL);
  }
  asm volatile("" ::: "memory");
}
template <int V> struct IC { static constexpr int v = V; };
template <int I, int N, class Fn> DI void sfor(Fn&& f) { if constexpr (I < N) { f(IC<I>{}); sfor<I + 1, N>(f); } }
constexpr int step(int i) { return i < 28 ? i : i < 32 ? i - 16 : i < 44 ? i - 32 : i - 28; }
DI u32x4 lds128(u32 a, u32 o = 0) { return *(const LAS u32x4*)((LAS char*)a + o); }
DI s16x4 tr64(u32 a, u32 o) { return __builtin_amdgcn_ds_read_tr16_b64_v4i16((LAS s16x4*)((LAS char*)a + o)); }
DI u32x4 cat(s16x4 lo, s16x4 hi) { return __builtin_bit_cast(u32x4, __builtin_shufflevector(lo, hi, 0, 1, 2, 3, 4, 5, 6, 7)); }
DI u32x4 ldstr(u32 a, u32 o0, u32 o1) { return cat(tr64(a, o0), tr64(a, o1)); }

struct Srd { const void* p; u32 bytes, flags; };
struct Args {
  Srd x, w1, w2;
  char* ws;
  u16* out;
  const u16 *lar, *aim, *cre, *cim, *ldt, *dsk, *b1, *b2, *gw;
};
DI u32 colblock(u32 jj, u32 w2lo) { return (jj + CBO - (w2lo >> 7)) & 7; }
DI u64 mk64(u32 lo, u32 hi) { return (u64)lo | ((u64)hi << 32); }
#define STAMP_OUT() do { VMCNT(0); ST(23); \
  const __amdgpu_buffer_rsrc_t sS = __builtin_amdgcn_make_buffer_rsrc((void*)(ws + WS_STAMP), 0, 65536, 0x00020000); \
  if (lane == 0) __builtin_amdgcn_raw_buffer_store_b128(u32x4{(u32)rt0, tag, (u32)stamp[0], tag}, sS, (b * 8 + wave) * 16, 0, 16); \
  if (b == 8 && wave == 4) { \
    int lo = 0, r = 0; \
    for (u32 i_ = 0; i_ < (1u << 22); i_++) { if (__builtin_amdgcn_s_memrealtime() >= rt0 + 2500) break; } \
    for (u32 sp_ = 0; sp_ < 1000; sp_++) { bool ok = true; lo = 1 << 30; int mx = -(1 << 30), mn = 1 << 30, sm = 0, cn = 0; \
      for (int j = 0; j < 32; j++) { asm volatile("" ::: "memory"); const u32 id = j * 64 + lane; const u32x4 v = __builtin_amdgcn_raw_buffer_load_b128(sS, id * 16, 0, 16); \
        ok &= v[1] == tag && v[3] == tag; const int s0 = (int)(v[0] - (u32)rt0), s1 = (int)(v[2] - (u32)rt0); lo = min(lo, s0); \
        if (2 == 2 || (((id >> 2) & 1) == 2 && ((id >> 6) & 7) != 7)) { mx = max(mx, s1); mn = min(mn, s1); sm += s1; cn++; } } \
      for (int o = 1; o < 64; o <<= 1) { lo = min(lo, __shfl_xor(lo, o)); mx = max(mx, __shfl_xor(mx, o)); mn = min(mn, __shfl_xor(mn, o)); sm += __shfl_xor(sm, o); cn += __shfl_xor(cn, o); } \
      r = 10 * (0); if (__all(ok)) break; } \
    const u64 until = rt0 + (long long)(int)rfl(lo) + 5000 + (long long)(int)rfl(r); \
    for (u32 i_ = 0; i_ < (1u << 22); i_++) { if (__builtin_amdgcn_s_memrealtime() >= until) break; } } } while (0)

__global__ void __launch_bounds__(512, 2) fused_mix(Args) {
  __shared__ __attribute__((aligned(16))) char smem[LDS_BYTES];
  const u32 sb = (u32)(size_t)(LAS char*)smem;
  u64 stamp[24], rt0, rt1;
  for (int i = 0; i < 24; i++) stamp[i] = 0;
  asm volatile("s_memrealtime %0\n\ts_waitcnt lgkmcnt(0)" : "=s"(rt0)::"memory");
  ST(0);
  u64 a_lar, a_aim, a_cre, a_cim, a_ldt, a_dsk, a_ws, a_oth;
  u32 wave, r_lar, r_aim, r_cre, r_cim, ctag, t_ldt, t_dsk;
  asm volatile(
      "s_load_dwordx2 %[lar], %[ka], 64\n\ts_load_dwordx2 %[ws], %[ka], 48\n\ts_load_dwordx2 %[aim], %[ka], 72\n\ts_load_dwordx2 %[cre], %[ka], 80\n\t"
      "s_load_dwordx2 %[cim], %[ka], 88\n\ts_load_dwordx2 %[ldt], %[ka], 96\n\ts_load_dwordx2 %[dsk], %[ka], 104\n\t"
      "v_readfirstlane_b32 %[wv], %[tid]\n\ts_getpc_b64 %[oth]\n.Lcode_start:\n\t"
      "s_lshr_b32 %[wv], %[wv], 6\n\ts_cmp_lt_u32 %[wv], 4\n\ts_cbranch_scc1 1f\n\t"
      "s_setprio 3\n\tv_and_b32 %[r3], 0xff, %[tid]\n\ts_lshr_b32 %[tg], %[b], 3\n\tv_lshlrev_b32 %[r0], 2, %[r3]\n\t"
      "v_lshl_or_b32 %[r0], %[tg], 10, %[r0]\n\tv_min_u32 %[r0], .Lcode_end - .Lcode_start - 4, %[r0]\n\tglobal_load_dword %[r0], %[r0], %[oth]\n\t"
      "s_lshl_b32 %[tg], %[wv], 3\n\ts_add_u32 %[tg], %[tg], 32\n\ts_load_dwordx2 %[oth], %[ka], %[tg]\n\t"
      "v_lshlrev_b32 %[r3], 1, %[r3]\n\tv_lshl_or_b32 %[r3], %[b], 9, %[r3]\n\ts_xor_b32 %[tl], %[b], %[pp]\n\t"
      "v_and_b32 %[r2], 63, %[tid]\n\tv_lshlrev_b32 %[r2], 3, %[r2]\n\tv_lshl_or_b32 %[r2], %[tl], 9, %[r2]\n\ts_lshl_b32 %[td], %[b], 2\n\t"
      "s_waitcnt lgkmcnt(0)\n\t"
      "global_load_ushort %[r0], %[r3], %[lar]" PPOL "\n\tglobal_load_ushort %[r1], %[r3], %[aim]" PPOL "\n\t"
      "global_load_dword %[r2], %[r2], %[oth]\n\t"
      "global_load_ushort %[r2], %[r3], %[cre]" PPOL "\n\tglobal_load_ushort %[r3], %[r3], %[cim]" PPOL "\n\t"
      "s_load_dword %[tg], %[ws], 0\n\ts_load_dword %[tl], %[ldt], %[td]\n\ts_load_dword %[td], %[dsk], %[td]\n1:"
      : [lar] "=&s"(a_lar), [aim] "=&s"(a_aim), [cre] "=&s"(a_cre), [cim] "=&s"(a_cim), [ldt] "=&s"(a_ldt), [dsk] "=&s"(a_dsk), [ws] "=&s"(a_ws),
        [oth] "=&s"(a_oth), [wv] "=&s"(wave), [tg] "=&s"(ctag), [tl] "=&s"(t_ldt), [td] "=&s"(t_dsk),
        [r0] "=&v"(r_lar), [r1] "=&v"(r_aim), [r2] "=&v"(r_cre), [r3] "=&v"(r_cim)
      : [ka] "s"(__builtin_amdgcn_kernarg_segment_ptr()), [tid] "v"(threadIdx.x), [b] "s"(blockIdx.x), [pp] "i"(PPAIR) : "scc");
  SB();
  const u32 w = wave & 3;
  const u32 b = blockIdx.x, xcd = b & 7, bi = b >> 3;
  const u32 hf = XMAP ? (xcd ^ (xcd >> 2)) & 1 : xcd & 1, p = 4 * (XMAP ? xcd & 3 : xcd >> 1) + (bi >> 3), jj = bi & 7;
  const bool spare = jj == 7;
  const u32 cb1 = 7 * hf + jj, rb = 2 * p + hf, pair = b;
  const u32 wm = w >> 1, wn = w & 1;
#define LANES() const u32 tid = opq(threadIdx.x) & 255, lane = tid & 63, hh = lane >> 5, blk = (lane >> 4) & 1, q4 = (lane & 15) >> 2, p4 = lane & 3, g4 = lane >> 4, l16 = opq(lane * 16)
  const u32 rot = 2 * (bi >> 3) + jj;

  if (wave >= 4) {
    u32x16 k0;
    u32x4 k1;
    asm volatile("s_load_dwordx16 %0, %2, 0\n\ts_load_dwordx4 %1, %2, 120" : "=&s"(k0), "=&s"(k1) : "s"(__builtin_amdgcn_kernarg_segment_ptr()));
    SB();
    LANES();
    const u32 ta = opq(sb + TAB + tid * 224);
    const u32 t0 = opq(sb + TAB + (wm * 128 + 4 * g4 + q4) * 224 + 8 * p4), t1 = opq(t0 + 32 * wn);
    const u32 ko = opq(sb + KD + wm * KD_CH + (4 * g4 * 36 + 16 * wn + (lane & 15)) * 2);
    char* const ws = (char*)a_ws;
    const __amdgpu_buffer_rsrc_t sWS = __builtin_amdgcn_make_buffer_rsrc((void*)ws, 0, WS_END, 0x00020000);
    u32 ga[9];
    {
      auto cell = [&](u32 pos) {
        const u32 hi = pos >> 8, ex = min(3u, 32u - (u32)__clz(hi));
        return sb + KD + (4 * ex + ((8 * hi) >> ex)) * 72 + ((pos & 255) >> 2);
      };
      const u32 P = 2040 - 8 * tid, a0 = cell(P), a8 = cell(P + 8);
      const u32 c0 = w == 3 ? 0x83222100u : w == 2 ? 0x88111000u : 0x88880000u, c1 = w == 3 ? 0x76543210u : w == 2 ? 0x88111000u : 0x88800000u;
      const u32 code = (w == 3 ? hh : (lane & 1)) ? c1 : c0;
#pragma unroll
      for (int k = 0; k < 8; k++) {
        const u32 n = (code >> (4 * k)) & 15u;
        ga[k + 1] = opq(n == 8 ? a8 : a0 + n * 72);
      }
      const u32 lowa = opq(P == 0 ? sb + KD + KD_ZERO : sb + KD + 7 * 72 + ((P - 8) >> 2));
      ga[0] = opq(P > 256 ? a0 : lowa);
    }
    *(LAS u32*)(hh ? sb + CNT + (lane & 15) * 4 : sb + KD + (lane & 1) * KD_CH + KD_ZERO) = 0;
    const u32 to = opq(sb + TC + tid * 16);
    const u32 ur = opq(sb + hf * 8192 + (lane & 7) * 1024 + (((lane >> 3) ^ (4 * (lane & 1))) << 4) + 4 * w);
    const u32 uw = opq(WS_UT + ((rot & 7) * 64 + (lane >> 3) * 8 + 2 * w) * UTS + rb * 128 + (lane & 7) * 16);

    STN(1);
    asm volatile("s_waitcnt lgkmcnt(0)" : "+s"(ctag), "+s"(t_ldt), "+s"(t_dsk), "+s"(k0), "+s"(k1)::"memory");
    asm volatile("s_waitcnt vmcnt(0)" : "+v"(r_lar), "+v"(r_aim), "+v"(r_cre), "+v"(r_cim)::"memory");
    const u32 tag = ctag + 1, cb2 = colblock(jj, k0[8]);
    ST(2);
    {
      const float dt = rbf(__expf(bf2f(wm ? t_ldt >> 16 : t_ldt & 0xffffu)));
      const float er = -(rbf(__expf(bf2f(r_lar))) * dt) * 1.44269504088896f;
      const float wi = (bf2f(r_aim) * dt) * 0.159154943091895f;
      auto seed = [&](float t) {
        const float m = __builtin_amdgcn_exp2f(er * t), a = __builtin_amdgcn_fractf(wi * t);
        return f32x2{m * __builtin_amdgcn_cosf(a), m * __builtin_amdgcn_sinf(a)};
      };
      auto put8 = [&](const f32x2* v, u32 off) {
        u32x4 re = {pk2(v[0].x, v[1].x), pk2(v[2].x, v[3].x), pk2(v[4].x, v[5].x), pk2(v[6].x, v[7].x)};
        u32x4 im = {pk2(v[0].y, v[1].y), pk2(v[2].y, v[3].y), pk2(v[4].y, v[5].y), pk2(v[6].y, v[7].y)};
        *(LAS u32x4*)((LAS char*)ta + off) = re;
        *(LAS u32x4*)((LAS char*)ta + 112 + off) = im;
      };
      f32x2 v[8];
      const Mul w8 = mk(seed(8.f)), w64 = mk(seed(64.f));
      v[0] = f32x2{1.f, 0.f}; v[1] = w8.b; v[2] = cmul(v[1], w8);
      const Mul w16 = mk(v[2]);
#pragma unroll
      for (int k = 3; k < 8; k++) v[k] = cmul(v[k - 2], w16);
      put8(v, 0); SB();
      v[0] = w64.b;
#pragma unroll
      for (int r = 1; r < 4; r++) {
#pragma unroll
        for (int k = (r == 1); k < 8; k++) v[k] = cmul(v[k], w64);
        put8(v, 16 * r); SB();
      }
      f32x2 s1 = seed(1.f), s256 = seed(256.f);
      s1.y = -s1.y; s256.y = -s256.y;
      const Mul v1 = mk(s1), W = mk(s256), v2c = mk(cmul(s1, v1));
      f32x2 u[8];
      u[0] = f32x2{2.f * (bf2f(r_cre) * dt), -2.f * (bf2f(r_cim) * dt)};
      u[1] = cmul(u[0], v1);
#pragma unroll
      for (int k = 2; k < 8; k++) u[k] = cmul(u[k - 2], v2c);
      put8(u, 64); SB();
      v[0] = cmul(u[0], W); v[1] = cmul(u[2], W); v[2] = cmul(u[4], W); v[3] = cmul(u[6], W);
      v[4] = cmul(v[0], W); v[5] = cmul(v[2], W); v[6] = cmul(v[4], W); v[7] = cmul(v[5], W);
      u[0] = cmul(v[6], W); u[1] = cmul(u[0], W); u[2] = cmul(u[1], W); u[3] = cmul(u[2], W); u[4] = cmul(u[3], W);
      put8(v, 80);
      u[5] = u[6] = u[7] = f32x2{0.f, 0.f};
      put8(u, 96);
      ST(3);
      BAR();
      ST(14);
      f32x4 ka[2] = {{(lane == 0 && wn == 0) ? bf2f(wm ? t_dsk >> 16 : t_dsk & 0xffffu) : 0.f, 0.f, 0.f, 0.f}, {0.f, 0.f, 0.f, 0.f}};
      u32x4 fv[8], f0[8], f1[8];
#pragma unroll
      for (int ks = 0; ks < 10; ks++) {
        const u32 o = ks * 16 * 224;
        if (ks < 8) { fv[ks] = ldstr(t1, o, o + 112); f0[ks] = ldstr(t0, o + 64, o + 64 + 112); f1[ks] = ldstr(t0, o + 96, o + 96 + 112); }
        if (ks >= 2) { ka[0] = MF16(f0[ks - 2], fv[ks - 2], ka[0]); ka[1] = MF16(f1[ks - 2], fv[ks - 2], ka[1]); }
        SB();
      }
#pragma unroll
      for (int rt = 0; rt < 2; rt++)
#pragma unroll
        for (int r = 0; r < 4; r += 2) {
          const u32 pr = pk2(ka[rt][r], ka[rt][r + 1]);
          *(LAS u16*)((LAS char*)ko + (16 * rt + r) * 72) = (u16)pr;
          *(LAS u16*)((LAS char*)ko + (16 * rt + r + 1) * 72) = (u16)(pr >> 16);
        }
    }
    ST(9);
    BAR();
    ST(4);
    {
      u32 xr[8];
#pragma unroll
      for (int j = 0; j < 8; j++) xr[j] = *(const LAS u32*)((LAS char*)(ur ^ ((j >> 1) << 4)) + j * 128);
#pragma unroll
      for (int c = 0; c < 2; c++) {
        u32x4 ou;
#pragma unroll
        for (int e = 0; e < 4; e++) ou[e] = __builtin_amdgcn_perm(xr[2 * e + 1], xr[2 * e], c ? 0x07060302 : 0x05040100);
        __builtin_amdgcn_raw_buffer_store_b128(ou, sWS, uw + c * UTS, 0, 16);
      }
    }
    ST(10);
    if (!spare) BAR();
    const u32 ufo = opq(WS_UF + (pair >> 5) * 128 + (lane & 31) * 4);
    const u32 ua = opq(sb + US + wm * US_STR + (256 - 16 - 128 * wn - 96 + 16 * (lane & 15) + 8 * g4) * 2);
    u32 tb[4];
#pragma unroll
    for (int j = 0; j < 4; j++) tb[j] = opq(sb + TC + (2 * wm + (lane & 1)) * TC_STR + (2046 - 16 - 128 * wn - 96 - 1792 + 8 * g4 - (lane & 14)) * 2 + 1024 * j);
    const u32 cw = opq(sb + CNT + 4 * w), cr = opq(sb + CNT + 4 * (lane & 3));
    auto arrive = [&](u32 n) { *(volatile LAS u32*)cw = n; };
    auto meet = [&](u32 n) { LGKM0(); arrive(n); lds_wait(cr, n); };
    if (spare) arrive(1);
    VMCNT(0);
    if (!spare) BAR();
    else {
      meet(2);
      if (tid == 0) asm volatile("buffer_store_dword %0, %1, %2, 0 offen sc1" ::"v"(tag), "v"(WS_UF + (rot & 7) * 128 + rb * 4), "s"(sWS) : "memory");
    }
    ST(5);
    const u32 row = tid >> 3, ck = lane & 7;
    const u32 ro = opq(sb + RED + w * 6144 + l16), r1 = opq(sb + RED + (w ^ 1) * 6144 + l16), r2 = opq(sb + RED + (w ^ 2) * 6144 + l16), r3 = opq(sb + RED + (w ^ 3) * 6144 + l16);
    const u32 ao = opq(WS_AT + pair * ATS + (8 * w + g4) * 512 + (lane & 15) * 16);
    const u32 sa = opq(sb + 2 * SLOT + ck * 1024 + ((row ^ (2 * ck)) << 4));
    if (!spare) BAR();
    const u32 ato = opq(WS_AT + (32 * cb2 + 8 * w + hh) * ATS + rb * 512 + (lane & 31) * 16);
    const u32 go = opq(WS_G + rb * 512 + row * 16);
    const u32 oo = opq((64 * rb + row) * 1024 + cb2 * 128 + ck * 16);
    const u32 aw = opq(sb + TC + (lane & 31) * 144 + (8 * w + hh) * 4), ar = opq(sb + TC + row * 144 + ck * 16);
    const u32 zo = opq(wn ? sb + US + hh * US_STR + (lane & 31) * 16 : sb + TC + ((lane >> 1) & 3) * TC_STR + 4096 + (lane & 1) * 16);
    auto fetch_u = [&]() {
#pragma unroll
      for (int q = 0; q < 8; q++) {
        if (!(q & 3)) SETM0(sb, US + 512 + (q >> 2) * US_STR);
        DMAP(" sc1", l16, sWS, WS_UT + (2 * pair + (q >> 2)) * UTS, q & 3);
      }
    };
    bool got = w != 0;
    if (!spare) {
      BAR();
#pragma unroll
      for (int i = 0; i < 2; i++) {
        if (got || i < UTRY) {
          BAR(); BAR();
        } else {
          u32 uf;
          asm volatile("buffer_load_dword %0, %1, %2, 0 offen sc1\n\ts_barrier\n\ts_barrier\n\ts_waitcnt vmcnt(0)" : "=&v"(uf) : "v"(ufo), "s"(sWS) : "memory");
          got = __all(uf == tag);
          if (got) fetch_u();
        }
      }
    }
    __builtin_amdgcn_s_setprio(CVPRIO);
    ST(21);
    {
      u32 ek[2][9];
#pragma unroll
      for (int i = 0; i < 9; i++) { ek[0][i] = *(const LAS u16*)ga[i]; ek[1][i] = *(const LAS u16*)((LAS char*)ga[i] + KD_CH); }
      *(LAS u32x4*)zo = u32x4{0, 0, 0, 0};
#pragma unroll
      for (int i = 0; i < 4; i++) {
        const u32* e = ek[i >> 1] + (i & 1);
        *(LAS u32x4*)((LAS char*)to + i * TC_STR) = u32x4{e[7] | (e[6] << 16), e[5] | (e[4] << 16), e[3] | (e[2] << 16), e[1] | (e[0] << 16)};
      }
    }
    if (!got) {
      __builtin_amdgcn_s_setprio(3);
      if (spare) __builtin_amdgcn_s_sleep(SPWAIT);
      _Pragma("unroll 1") for (u32 spin = 0; spin < SPIN_CAP; spin++) {
        u32 uf;
        asm volatile("buffer_load_dword %0, %1, %2, 0 offen sc1\n\ts_waitcnt vmcnt(0)" : "=&v"(uf) : "v"(ufo), "s"(sWS) : "memory");
        if (__all(uf == tag)) break;
        __builtin_amdgcn_s_sleep(USLEEP);
      }
      fetch_u();
      __builtin_amdgcn_s_setprio(CVPRIO);
    }
    VMCNT(0);
    ST(6);
    meet(4);
    ST(7);

    f32x4 ca[8];
#pragma unroll
    for (int c = 0; c < 8; c++) ca[c] = f32x4{0.f, 0.f, 0.f, 0.f};
    u32x4 fu[2][8];
    u32x2 ft[2][16];
    auto cread = [&](int f, int r) {
      const int k = r < 2 ? 8 + r : r < 10 ? r - 2 : r;
      if (k < 8) fu[f & 1][k] = lds128(ua, (96 - 32 * f + 256 * k) * 2);
      else {
        const int i = (k - 8) >> 1, hl = k & 1, e = 7 - i;
        const LAS u32* t = (const LAS u32*)((LAS char*)tb[e >> 1] + (96 - 32 * f + 256 * (e & 1)) * 2 + 8 * hl);
        ft[f & 1][2 * i + hl] = u32x2{t[0], t[1]};
      }
    };
#pragma unroll
    for (int r = 0; r < 24; r++) { cread(0, r); SB(); }
#pragma unroll
    for (int f = 0; f < 4; f++) {
      int m = 0;
#pragma unroll
      for (int i = 0; i < 8; i++)
#pragma unroll
        for (int e = 0; e + i < 8; e++, m++) {
          const u32x4 tf = {ft[f & 1][2 * i][0], ft[f & 1][2 * i][1], ft[f & 1][2 * i + 1][0], ft[f & 1][2 * i + 1][1]};
          ca[i + e] = MF16(fu[f & 1][e], tf, ca[i + e]);
          if (f < 3 && m >= 4 && m < 28) cread(f + 1, m - 4);
          if (f == 3 && m == 0) arrive(5);
          SB();
        }
    }
    ST(8);
    f32x4 keep[2];
    __builtin_amdgcn_s_setprio(3);
    lds_wait(cr, 5);
#define SEND(W) { _Pragma("unroll") for (int j = 1; j < 4; j++) { *(LAS f32x4*)((LAS char*)ro + (j - 1) * 2048) = ca[2 * (W ^ j)]; *(LAS f32x4*)((LAS char*)ro + (j - 1) * 2048 + 1024) = ca[2 * (W ^ j) + 1]; } \
                  keep[0] = ca[2 * W]; keep[1] = ca[2 * W + 1]; asm volatile("; send " #W ::: "memory"); }
    if (w == 0) SEND(0) else if (w == 1) SEND(1) else if (w == 2) SEND(2) else SEND(3)
    asm volatile("" ::: "memory");
    arrive(6);
    lds_wait(cr, 6);
    const u32 sel = wm ? 0x01000302 : 0x03020100;
    f32x4 pp[2][3];
#pragma unroll
    for (int c = 0; c < 2; c++) {
      pp[c][0] = *(const LAS f32x4*)((LAS char*)r1 + c * 1024);
      pp[c][1] = *(const LAS f32x4*)((LAS char*)r2 + 2048 + c * 1024);
      pp[c][2] = *(const LAS f32x4*)((LAS char*)r3 + 4096 + c * 1024);
    }
#pragma unroll
    for (int c = 0; c < 2; c++) {
      u32 o[4];
#pragma unroll
      for (int r = 0; r < 4; r++) {
        const u32 v = pk2(add1(keep[c][r], pp[c][0][r]), add1(pp[c][1][r], pp[c][2][r]));
        o[r] = __builtin_amdgcn_perm(v, v, sel);
      }
#pragma unroll
      for (int r = 0; r < 2; r++) __builtin_amdgcn_raw_buffer_store_b128(u32x4{o[r], tag, o[r + 2], tag}, sWS, ao + c * 2048 + r * 256, 0, 16);
    }
    arrive(7);
    ST(11);
    const __amdgpu_buffer_rsrc_t sB2 = __builtin_amdgcn_make_buffer_rsrc((void*)mk64(k1[0], k1[1]), 0, D * 2, 0x00020000);
    const __amdgpu_buffer_rsrc_t sOut = __builtin_amdgcn_make_buffer_rsrc((void*)mk64(k0[14], k0[15]), 0, L * D * 2, 0x00020000);
    const u32x4 b2v = __builtin_amdgcn_raw_buffer_load_b128(sB2, ck * 16, cb2 * 128, 0);
    lds_wait<LSL>(cr + RDY - CNT, 1);
    ST(12);
    u32x4 gg, at[4];
    _Pragma("unroll 1") for (u32 spin = 0; spin < SPIN_CAP; spin++) {
      asm volatile("" ::: "memory");
#pragma unroll
      for (int j = 0; j < 4; j++) at[j] = __builtin_amdgcn_raw_buffer_load_b128(sWS, ato, 2 * j * ATS, 16);
      gg = __builtin_amdgcn_raw_buffer_load_b128(sWS, go, 0, 16);
      bool ok = gg[1] == tag && gg[3] == tag;
#pragma unroll
      for (int j = 0; j < 4; j++) ok &= at[j][1] == tag && at[j][3] == tag;
      if (__all(ok)) break;
      __builtin_amdgcn_s_sleep(RSLEEP);
    }
    __builtin_amdgcn_s_setprio(TPRIO);
    ST(13);
    lds_wait(cr, 7);
#pragma unroll
    for (int j = 0; j < 4; j++) {
      *(LAS u32*)((LAS char*)aw + j * 8) = at[j][0];
      *(LAS u32*)((LAS char*)aw + j * 8 + 32 * 144) = at[j][2];
    }
    asm volatile("" ::: "memory");
    arrive(8);
    lds_wait(cr, 8);
    ST(15);
#pragma unroll
    for (int t = 0; t < 2; t++) {
      const float g = bf2f(gg[2 * t] & 0xffffu), gm = hi2f(gg[2 * t]);
      const u32x4 av = lds128(ar, t * 32 * 144);
      float base[8];
#pragma unroll
      for (int i = 0; i < 4; i++) {
        base[2 * i] = fopq(fma1(gm, bf2f(b2v[i] & 0xffffu), mul1(g, bf2f(av[i] & 0xffffu))));
        base[2 * i + 1] = fopq(fma1(gm, hi2f(b2v[i]), mul1(g, hi2f(av[i]))));
      }
      if (t == 0) { ST(16); lds_wait<LSL>(cr + RDY - CNT, 2); } else { ST(18); asm volatile("s_barrier" ::: "memory"); ST(19); }
      f32x4 pt[4][2];
#pragma unroll
      for (int k = 0; k < 4; k++)
#pragma unroll
        for (int q = 0; q < 2; q++) pt[k][q] = *(const LAS f32x4*)((LAS char*)sa + (1 - t) * SLOT + k * 8192 + q * 512);
      u32x4 ow;
#pragma unroll
      for (int i = 0; i < 4; i++) {
        float o[2];
#pragma unroll
        for (int e = 0; e < 2; e++) {
          const int q = i >> 1, r = 2 * (i & 1) + e;
          o[e] = t ? __builtin_fmaf(gm, (pt[0][q][r] + pt[1][q][r]) + (pt[2][q][r] + pt[3][q][r]), base[2 * i + e])
                   : fma1(gm, add1(add1(pt[0][q][r], pt[1][q][r]), add1(pt[2][q][r], pt[3][q][r])), base[2 * i + e]);
        }
        ow[i] = pk2(o[0], o[1]);
      }
      __builtin_amdgcn_raw_buffer_store_b128(ow, sOut, oo + t * 32768, 0, OUTPOL);
      SB();
      if (t == 0) ST(17);
    }
    asm volatile(".Lcode_end:");
    STAMP_OUT();
    return;
  }

  u32x16 k0;
  u32x4 k1;
  asm volatile("s_load_dwordx16 %0, %2, 0\n\ts_load_dwordx4 %1, %2, 112"
               : "=&s"(k0), "=&s"(k1) : "s"(__builtin_amdgcn_kernarg_segment_ptr()));
  SB();
  LANES();
  const u32 o1 = opq(((spare ? 0 : cb1) * 128 + wn * 64 + (lane & 31)) * 2);
  u32 vA[4], vB[4], v2[4];
#pragma unroll
  for (int q = 0; q < 4; q++) {
    vA[q] = opq((lane >> 3) * 1024 + (((lane & 7) ^ g4 ^ (4 * (q & 1))) << 4) + q * (8192 - 1024));
    vB[q] = opq(g4 * (F * 2) + (((lane & 15) ^ (g4 << 2) ^ q) << 4) + q * (4 * F * 2 - 1024));
    v2[q] = opq((lane >> 3) * 1024 + (((lane & 7) ^ (blk << 2)) << 4) + q * (8192 - 1024));
  }
  const u32 xbase = sopq((128 * p + 32 * w) * 1024), wbase = sopq(16 * w * (F * 2) + cb1 * 256);
  const u32 m4 = sopq(sb + w * 4096), m8 = sopq(sb + w * 8192);
  asm volatile("s_waitcnt lgkmcnt(0)" : "+s"(k0), "+s"(k1) : "s"(a_lar), "s"(a_aim), "s"(a_cre), "s"(a_cim), "s"(a_ldt), "s"(a_dsk), "s"(a_ws));
  ST(14);
  const u32x4 sX = k0.s0123, sW1 = k0.s4567;
  const u32x4 sW2 = k0.s89ab;
  char* const ws = (char*)mk64(k0[12], k0[13]);
  const u32 w2base = 28 * w * 16384 + colblock(jj, sW2[0]) * 128;
  u32 tag;
  const __amdgpu_buffer_rsrc_t sWS = __builtin_amdgcn_make_buffer_rsrc((void*)ws, 0, WS_END, 0x00020000);
  auto w2piece = [&](int c, int q) {
    if ((q & 3) == 0) SETM0(m8, (c & 3) * SLOT + (q >> 2) * 4096);
    DMAQ(v2[q & 3], sW2, w2base + c * 65536 + (q >> 2) * 32768, q & 3);
  };

  if (!spare) {
    auto g1piece = [&](int g, int q) {
      if (q == 0) SETM0(m4, (g & 3) * SLOT);
      if (q == 4) SETM0(m4, (g & 3) * SLOT + 16384);
      if (q < 4) DMAP(XPOL, vA[q], sX, xbase + ((g + rot) & 7) * 128, q);
      else DMAQ(vB[q - 4], sW1, wbase + ((g + rot) & 7) * 64 * (F * 2), q - 4);
    };
    u32 r_b1a, r_b1b;
    asm volatile("s_load_dword %0, %1, 0" : "=s"(tag) : "s"(ws));
    asm volatile("global_load_ushort %0, %1, %2" : "=v"(r_b1a) : "v"(o1), "s"(mk64(k1[0], k1[1])));
    asm volatile("global_load_ushort %0, %1, %2 offset:64" : "=v"(r_b1b) : "v"(o1), "s"(mk64(k1[0], k1[1])));
#pragma unroll
    for (int i = 0; i < 12; i++) g1piece(i >> 3, i < 8 ? i : i - 4);
    ST(1);
    u32 ax[2][4], aw[2][4];
    {
      const u32 xa = sb + (64 * wm + (lane & 31)) * 128 + ((hh ^ ((lane >> 1) & 7)) << 4);
      const u32 wa = sb + 16384 + 256 * (8 * hh + q4) + 8 * (p4 & 1) + (((8 * wn + 2 * blk + (p4 >> 1)) ^ ((q4 << 2) | (2 * hh))) << 4);
#pragma unroll
      for (int k = 0; k < 4; k++) {
        ax[0][k] = opq(xa ^ (k << 5)); aw[0][k] = opq(wa ^ ((k & 1) << 4) ^ ((k >> 1) << 6));
        ax[1][k] = opq(ax[0][k] + 2 * SLOT); aw[1][k] = opq(aw[0][k] + 2 * SLOT);
      }
    }
    const u32 ho = opq(l16 + WS_H + ((4 * p + 2 * wm) * 112 + cb1 * 8 + wn * 4) * 1024), ho1 = opq(ho + 112 * 1024);
    asm volatile("s_waitcnt lgkmcnt(0)" : "+s"(tag));
    const u32 vtag = opq(tag + 1), vuf = opq(WS_UF + (rot & 7) * 128 + rb * 4);
#pragma unroll
    for (int q = 0; q < 8 * LA - 12; q++) {
      if (q == PRET) { asm volatile("s_barrier" ::: "memory"); ST(3); }
      g1piece(q < 4 ? 1 : 2, q < 4 ? q : q - 4);
    }
    if (PRET >= 8 * LA - 12) { asm volatile("s_barrier" ::: "memory"); ST(3); }
    asm volatile("s_waitcnt vmcnt(%2)" : "+v"(r_b1a), "+v"(r_b1b) : "i"(8 * LA));
    f32x16 acc[2][2];
    {
      const u32x4 one = {hh ? 0u : 0x3f80u, 0u, 0u, 0u};
      const u32x4 ba = {r_b1a, 0u, 0u, 0u}, bb = {r_b1b, 0u, 0u, 0u};
      asm volatile("s_nop 1\n\tv_mfma_f32_32x32x16_bf16 %0, %4, %6, 0\n\tv_mfma_f32_32x32x16_bf16 %1, %4, %6, 0\n\t"
                   "v_mfma_f32_32x32x16_bf16 %2, %5, %6, 0\n\tv_mfma_f32_32x32x16_bf16 %3, %5, %6, 0"
                   : "=&v"(acc[0][0]), "=&v"(acc[0][1]), "=&v"(acc[1][0]), "=&v"(acc[1][1]) : "v"(ba), "v"(bb), "v"(one));
    }
    u32x4 fx[2][2];
    s16x4 fw[2][4];
    VMCNT(8 * LA - 8);
    ST(2);
    BAR();
    ST(4);
    fx[0][0] = lds128(ax[0][0]); fw[0][0] = tr64(aw[0][0], 0); fw[0][1] = tr64(aw[0][1], 1024);
    fx[0][1] = lds128(ax[0][0], 4096); fw[0][2] = tr64(aw[0][2], 0); fw[0][3] = tr64(aw[0][3], 1024);
#pragma unroll
    for (int s = 0; s < 8; s++) {
      auto piece = [&](int q) { if (s + LA < 8) g1piece(s + LA, q); else if (s + LA - 8 < WPRE) w2piece(s + LA - 8, q); };
#pragma unroll
      for (int k = 0; k < 4; k++) {
        const int c = k & 1, n = c ^ 1, kn = (k + 1) & 3, sl = (k == 3 ? s + 1 : s) & 3, hi = sl >> 1;
        const u32 o = (sl & 1) * SLOT, ow = o + kn * 4096;
        const bool more = s < 7 || k < 3;
        MMV(acc[0][0], cat(fw[c][0], fw[c][1]), fx[c][0]);
        if (k == 3) {
          if (s < 7) VMCNT((LA == 3 && s + 2 < 8 + WPRE ? 8 : 0) + (s + LA < 8 + WPRE ? 6 : 0));
          BAR();
        }
        if (more) { fx[n][0] = lds128(ax[hi][kn], o); fw[n][0] = tr64(aw[hi][0], ow); fw[n][1] = tr64(aw[hi][1], ow + 1024); }
        MMV(acc[0][1], cat(fw[c][0], fw[c][1]), fx[c][1]);
        if (more) { fx[n][1] = lds128(ax[hi][kn], o + 4096); fw[n][2] = tr64(aw[hi][2], ow); }
        piece(2 * k);
        MMV(acc[1][0], cat(fw[c][2], fw[c][3]), fx[c][0]);
        if (more) fw[n][3] = tr64(aw[hi][3], ow + 1024);
        MMV(acc[1][1], cat(fw[c][2], fw[c][3]), fx[c][1]);
        piece(2 * k + 1);
      }
      if (s == 1)
        asm volatile("s_cmp_lg_u32 %3, 0\n\ts_cbranch_scc1 1f\n\ts_mov_b64 exec, 1\n\tbuffer_store_dword %0, %1, %2, 0 offen sc1\n\ts_mov_b64 exec, -1\n1:"
                     ::"v"(vtag), "v"(vuf), "s"(sWS), "s"(w) : "memory", "scc");
      if (s == 0) ST(8);
      if (s == 1) ST(15);
      if (s == 2) ST(16);
      if (s == 3) ST(17);
      if (s == 4) ST(19);
      if (s == 5) ST(20);
      if (s == 6) ST(21);
    }
    asm volatile("s_nop 7\n\ts_nop 7" : "+v"(acc[0][0]), "+v"(acc[0][1]), "+v"(acc[1][0]), "+v"(acc[1][1]));
    __builtin_amdgcn_s_setprio(2);
    ST(5);
#pragma unroll
    for (int u = 0; u < 2; u++)
#pragma unroll
      for (int t = 0; t < 2; t++)
#pragma unroll
        for (int s = 0; s < 2; s++) {
          u32x4 o;
#pragma unroll
          for (int e = 0; e < 4; e++) {
            const float a0 = relu(acc[u][t][8 * s + 2 * e]), a1 = relu(acc[u][t][8 * s + 2 * e + 1]);
            o[e] = EPK ? pk2(a0 * a0, a1 * a1) : pk2(mul1(a0, a0), mul1(a1, a1));
          }
          __builtin_amdgcn_raw_buffer_store_b128(o, sWS, (t ? ho1 : ho) + (2 * u + s) * 1024, 0, 16);
          SB();
        }
    tag += 1;
  } else {
    asm volatile("s_load_dword %0, %1, 0" : "=s"(tag) : "s"(ws));
    SETM0(m4, 0);
#pragma unroll
    for (int q = 0; q < 4; q++) DMAP(XPOL, vA[q], sX, xbase + (rot & 7) * 128, q);
    u32x4 gwv, xr[16];
    asm volatile("global_load_dwordx4 %0, %1, %2" : "=v"(gwv) : "v"(l16), "s"(((const CAS Args*)__builtin_amdgcn_kernarg_segment_ptr())->gw));
#pragma unroll
    for (int j = 0; j < 16; j++) {
      if (j == 7) asm volatile("s_barrier" ::: "memory");
      asm volatile("buffer_load_dwordx4 %0, %1, %2, %3 offen offset:%4" : "=v"(xr[j]) : "v"(l16), "s"(sX), "s"((64 * rb + 16 * w + (j & ~3)) * 1024), "i"((j & 3) * 1024));
    }
    asm volatile("s_waitcnt vmcnt(17) lgkmcnt(0)\n\ts_barrier" : "+s"(tag)::"memory");
    tag += 1;
    ST(2);
    asm volatile("s_waitcnt vmcnt(0)" : "+v"(gwv), "+v"(xr[0]), "+v"(xr[1]), "+v"(xr[2]), "+v"(xr[3]), "+v"(xr[4]), "+v"(xr[5]), "+v"(xr[6]), "+v"(xr[7]),
                 "+v"(xr[8]), "+v"(xr[9]), "+v"(xr[10]), "+v"(xr[11]), "+v"(xr[12]), "+v"(xr[13]), "+v"(xr[14]), "+v"(xr[15])::"memory");
    SETM0(m8, SLOT);
    auto pull = [&](u32 n) {
#pragma unroll
      for (int q = 0; q < 4; q++) DMAQ(l16, sW2, min(14 * xcd + 4 * n + (bi >> 3), 111u) * 16384 + w * 4096, q);
    };
    pull(0); pull(1);
    ST(8);
    const u32 zs = m8 + 2 * SLOT;
#pragma unroll
    for (int j = 0; j < 16; j++) {
      float z = 0.f;
#pragma unroll
      for (int e = 0; e < 4; e++) z = dot2(xr[j][e], gwv[e], z);
      *(LAS float*)((LAS char*)(zs + lane * 4) + j * 272) = z;
    }
    VMCNT(4); pull(2);
    LGKM0();
    f32x4 z4 = {};
#pragma unroll
    for (int k = 0; k < 4; k++) z4 += *(const LAS f32x4*)((LAS char*)(zs + (lane & 15) * 272 + g4 * 64) + k * 16);
    float zz = (z4[0] + z4[1]) + (z4[2] + z4[3]);
    zz += __shfl_xor(zz, 16); zz += __shfl_xor(zz, 32);
    const float g = rbf(1.f / (1.f + __expf(-zz)));
    VMCNT(4); pull(3);
    if (lane < 16) __builtin_amdgcn_raw_buffer_store_b64(u32x2{pk2(g, 1.f - g), tag}, sWS, WS_G + rb * 512 + (wn * 16 + lane) * 16 + wm * 8, 0, 16);
    lds_wait(sb + CNT + 4 * (lane & 3), 1);
    ST(4);
#pragma unroll
    for (int i = 0; i < 8 * WPRE; i++) { if (!(i & 3)) VMCNT(THR); w2piece(i >> 3, i & 7); }
  }
  SB();
  ST(6);

  u32 w2a[2][2];
  {
    const u32 a = m8 + 128 * (4 * hh + q4) + 8 * (p4 & 1) + (((2 * blk + (p4 >> 1)) ^ ((q4 >> 1) << 2)) << 4);
    w2a[0][0] = opq(a); w2a[1][0] = opq(a ^ 64); w2a[0][1] = opq(a + 2 * SLOT); w2a[1][1] = opq((a ^ 64) + 2 * SLOT);
  }
  auto w2frag = [&](int st, int v) {
    const int sl = (st >> 2) & 3;
    const u32 o = (sl & 1) * SLOT + (st & 3) * 2048;
    return ldstr(w2a[v][sl >> 1], o, o + 1024);
  };
  u32x4 w2r[12][2];
  u32 fl = 0;
  const u32 fo = opq(WS_HF + p * 128 + (((lane & 7) == 7) ? 0 : (lane & 15) * 4));
#pragma unroll
  for (int c = WPRE; c < 8; c++) {
    if (c == WPRE + PUBAT) {
      VMCNT(8 * PUBAT);
      *(volatile LAS u32*)(sb + FD + 4 * w) = 1;
      if (w == 0 && !spare) {
        lds_wait(sb + FD + 4 * (lane & 3), 1);
        if (lane == 0) asm volatile("buffer_store_dword %0, %1, %2, 0 offen sc1" ::"v"(tag), "v"(WS_HF + p * 128 + (8 * hf + jj) * 4), "s"(sWS) : "memory");
      }
      ST(7);
      __builtin_amdgcn_s_sleep(WDELAY);
    }
    if (c >= 4 && c < 7) {
      VMCNT(24);
#pragma unroll
      for (int j = 0; j < 8; j++) w2r[4 * (c - 4) + (j >> 1)][j & 1] = w2frag(4 * (c - 4) + (j >> 1), j & 1);
      LGKM0();
    }
    if (c == POLLAT) asm volatile("s_cmp_lg_u32 %3, 3\n\ts_cbranch_scc1 1f\n\tbuffer_load_dword %0, %1, %2, 0 offen sc1\n1:" : "+v"(fl) : "v"(fo), "s"(sWS), "s"(w) : "memory", "scc");
    if (c < 7) {
#pragma unroll
      for (int q = 0; q < 8; q++) {
        if (!(q & 3)) asm volatile("s_cmp_lg_u32 %0, 7\n\ts_cbranch_scc1 1f\n\ts_waitcnt vmcnt(%1)\n1:" ::"s"(jj), "i"(THR) : "memory", "scc");
        w2piece(c, q);
      }
    }
  }
  const u32 hb = sopq(WS_H + (2 * rb * 112 + 28 * w) * 1024);
  const u32 xw = opq(m8 + 2 * SLOT + l16), rdw = opq(sb + RDY + 4 * w);
  asm volatile("s_waitcnt vmcnt(%1)" : "+v"(fl) : "i"(8 * (7 - POLLAT)) : "memory");
  ST(11);
  if (w == 3) {
    _Pragma("unroll 1") for (u32 spin = 0; spin < SPIN_CAP; spin++) {
      if (__all(fl == tag)) break;
      __builtin_amdgcn_s_sleep(HSLEEP);
      asm volatile("buffer_load_dword %0, %1, %2, 0 offen sc1\n\ts_waitcnt vmcnt(0)" : "=&v"(fl) : "v"(fo), "s"(sWS) : "memory");
    }
    *(volatile LAS u32*)(sb + GO) = 1;
  } else {
    lds_wait(sb + GO, 1);
  }
  __builtin_amdgcn_s_setprio(G2PRIO);
  ST(10);
  SB();

  u32x4 hfr[56];
  auto load_h = [&](int i) {
    const int st = step(i);
    hfr[i] = __builtin_amdgcn_raw_buffer_load_b128(sWS, l16 + (st & 3) * 1024, hb + ((i / 28) * 112 + (st & ~3)) * 1024, 16);
  };
#pragma unroll
  for (int i = 0; i < HEAD; i++) load_h(i);
  SB();
  f32x16 oc[2][2] = {};
  auto send = [&](int t, int j) {
    const f32x16& s = oc[t][j >> 2];
    const int r = 4 * (j & 3);
    *(LAS f32x4*)((LAS char*)(xw ^ (j << 5)) + (1 - t) * SLOT + j * 1024) = f32x4{s[r], s[r + 1], s[r + 2], s[r + 3]};
  };
  auto signal = [&](u32 n) { asm volatile("" ::: "memory"); *(volatile LAS u32*)rdw = n; };
  u32x4 wl[56][2];
  auto fetch_w = [&](int i) { if (i < 56 && step(i) >= 12) { wl[i][0] = w2frag(step(i), 0); wl[i][1] = w2frag(step(i), 1); } };
  sfor<0, 56>([&](auto ic) {
    constexpr int i = decltype(ic)::v, t = i / 28, st = step(i);
    constexpr int D2 = 53 - TAILN;
    if (i < D2 || !TAILN) fetch_w(i + 1); else { fetch_w(2 * i - D2 + 1); fetch_w(2 * i - D2 + 2); }
    SB();
    u32x4 a0, a1;
    if (st < 12) { a0 = w2r[st < 12 ? st : 0][0]; a1 = w2r[st < 12 ? st : 0][1]; } else { a0 = wl[i][0]; a1 = wl[i][1]; }
    oc[t][0] = MF32(a0, hfr[i], oc[t][0]);
    if (i + HEAD < 56) load_h(i + HEAD);
    SB();
    if (i < 56 - TAILN) oc[t][1] = MF32(a1, hfr[i], oc[t][1]);
    if (i == ATAT) signal(1);
    if (i >= 31 && i < 39) send(0, i - 31);
    if (i == 38) signal(2);
    SB();
    if (i == 27) ST(18);
  });
#pragma unroll
  for (int i = 0; i < TAILN; i++) {
    oc[1][1] = MF32(wl[56 - TAILN + i][1], hfr[56 - TAILN + i], oc[1][1]);
    SB();
    if (i >= 1 && i < 3) { send(1, 2 * i - 2); send(1, 2 * i - 1); }
    SB();
  }
  ST(12);
#pragma unroll
  for (int j = TAILN >= 3 ? 4 : 0; j < 8; j++) send(1, j);
  BAR();
  if (b == 0 && w == 3) {
    _Pragma("unroll 1") for (u32 spin = 0; spin < SPIN_CAP; spin++) {
      asm volatile("" ::: "memory");
      const u32x4 f0 = __builtin_amdgcn_raw_buffer_load_b128(sWS, WS_UF + l16, 0, 16);
      if (__all(f0[0] == tag && f0[1] == tag && f0[2] == tag && f0[3] == tag)) break;
    }
    if (lane == 0) __builtin_amdgcn_raw_buffer_store_b32(tag, sWS, 0, 0, 16);
  }
  STAMP_OUT();
}

extern "C" void kernel_launch(void* const* d_in, const int* in_sizes, int n_in, void* d_out, int out_size,
                              void* d_ws, size_t ws_size, hipStream_t stream) {
  Args a;
  a.x = Srd{d_in[0], L * D * 2, 0x00020000}; a.w1 = Srd{d_in[8], D * F * 2, 0x00020000}; a.w2 = Srd{d_in[10], D * F * 2, 0x00020000};
  a.ws = (char*)d_ws + WSO; a.out = (u16*)d_out;
  a.lar = (const u16*)d_in[1]; a.aim = (const u16*)d_in[2]; a.cre = (const u16*)d_in[3]; a.cim = (const u16*)d_in[4];
  a.ldt = (const u16*)d_in[5]; a.dsk = (const u16*)d_in[6]; a.b1 = (const u16*)d_in[9]; a.b2 = (const u16*)d_in[11];
  a.gw = (const u16*)d_in[12];
  fused_mix<<<256, 512, 0, stream>>>(a);
}
```

```cpp
#include <hip/hip_runtime.h>
#pragma clang diagnostic ignored "-Wint-to-pointer-cast"

typedef unsigned short u16;
typedef unsigned u32;
typedef unsigned long long u64;
typedef __bf16 h16x8 __attribute__((ext_vector_type(8)));
typedef __bf16 h16x2 __attribute__((ext_vector_type(2)));
typedef short s16x4 __attribute__((ext_vector_type(4)));
typedef short s16x8 __attribute__((ext_vector_type(8)));
typedef short s16x2 __attribute__((ext_vector_type(2)));
typedef float f32x2 __attribute__((ext_vector_type(2)));
typedef float f32x4 __attribute__((ext_vector_type(4)));
typedef float f32x16 __attribute__((ext_vector_type(16)));
typedef u32 u32x2 __attribute__((ext_vector_type(2)));
typedef u32 u32x4 __attribute__((ext_vector_type(4)));
typedef u32 u32x16 __attribute__((ext_vector_type(16)));
#define LAS __attribute__((address_space(3)))
#define CAS __attribute__((address_space(4)))
#define GAS __attribute__((address_space(1)))
#define DI __device__ __forceinline__

constexpr int L = 2048, D = 512, F = 1792;
constexpr u32 WS_UF = 8192;
constexpr u32 WS_HF = 4096;
constexpr u32 WS_G = 1u << 19;
#define UTS 4096
#define ATS 16384
#define WSO 0
constexpr u32 WS_UT = 1u << 20;
constexpr u32 WS_AT = 4u << 20;
constexpr u32 WS_H = 9u << 20;
constexpr u32 WS_END = 16u << 20;
constexpr u32 WS_STAMP = 32u << 20;
constexpr u32 SLOT = 32768;
constexpr u32 TAB = 3 * SLOT;
constexpr u32 TC = 4 * SLOT;
constexpr u32 TC_STR = 4160;
constexpr u32 US = TC + 4 * TC_STR;
constexpr u32 US_STR = (256 + 2048) * 2;
constexpr u32 KD_CH = 32 * 36 * 2;
constexpr u32 LDS_BYTES = 163840;
constexpr u32 KD = LDS_BYTES - 2 * KD_CH;
constexpr u32 KD_ZERO = 34 * 2;
constexpr u32 CNT = KD - 64;
constexpr u32 FD = CNT + 16, RDY = CNT + 32, GO = CNT + 48;
constexpr u32 RED = TC;
static_assert(US + 2 * US_STR <= CNT && TAB + 256 * 224 + 16 <= CNT, "lds map");
constexpr u32 SPIN_CAP = 1u << 16;
#define EPK 0
#define WDELAY 0
#define CBO 4
#define TAILN 4
#define EXPE 0
#define LSL 4
#define TPRIO 3
#define UTRY 1
#define XMAP 1
#define PRET 0
#define THR 4
#define SPWAIT 32
#define XEARLY 0
#define ADELAY 0
#define TBAR 17
#define PW 1
#define GSL 0
#define HEAD 12
#define WPRE 2
#define PUBAT 0
#define PPOL ""
#define PPOL2 ""
#define XPOL ""
#define NPF 4
#define PFDELAY 24
#define POLLAT 6
#define PFC 0
#define LA 3
#define OUTPOL 16
#define UPUB 2
#define RSLEEP 8
#define USLEEP 8
#define HSLEEP 2
#define PPAIR 4
#define ATAT 16
#define G2PRIO 0
#define CVPRIO 1

DI float bf2f(u32 v) { return __uint_as_float(v << 16); }
DI u32 pk2(float lo, float hi) { f32x2 a = {lo, hi}; return __builtin_bit_cast(u32, __builtin_convertvector(a, h16x2)); }
DI float rbf(float x) { return bf2f(pk2(x, x) & 0xffffu); }
DI float dot2(u32 a, u32 b, float c) { return __builtin_amdgcn_fdot2_f32_bf16(__builtin_bit_cast(h16x2, a), __builtin_bit_cast(h16x2, b), c, false); }
DI u32 rfl(u32 x) { return __builtin_amdgcn_readfirstlane(x); }
struct Mul { f32x2 b, n; };
DI Mul mk(f32x2 b) { return Mul{b, f32x2{-b.y, b.y}}; }
DI f32x2 cmul(f32x2 a, Mul m) { return __builtin_elementwise_fma(a.yx, m.n, a * m.b.xx); }
DI float mul1(float a, float b) { float r; asm("v_mul_f32 %0, %1, %2" : "=v"(r) : "v"(a), "v"(b)); return r; }
DI float add1(float a, float b) { float r; asm("v_add_f32 %0, %1, %2" : "=v"(r) : "v"(a), "v"(b)); return r; }
DI float fma1(float a, float b, float c) { float r; asm("v_fma_f32 %0, %1, %2, %3" : "=v"(r) : "v"(a), "v"(b), "v"(c)); return r; }
DI float hi2f(u32 v) { return __uint_as_float(v & 0xffff0000u); }
DI u32 relu2(float a, float b) {
  u32 r, t;
  asm("v_max_i32 %0, 0, %2\n\tv_max_i32 %1, 0, %3\n\tv_mul_f32 %0, %0, %0\n\tv_mul_f32 %1, %1, %1\n\tv_cvt_pk_bf16_f32 %0, %0, %1" : "=&v"(r), "=&v"(t) : "v"(a), "v"(b));
  return r;
}
DI float fopq(float x) { asm volatile("" : "+v"(x)); return x; }
DI float relu(float x) { return __int_as_float(max(__float_as_int(x), 0)); }
DI u32 sopq(u32 x) { asm volatile("" : "+s"(x)); return x; }
DI u32 opq(u32 x) { asm volatile("" : "+v"(x)); return x; }
#define MF32(a, b, c) __builtin_amdgcn_mfma_f32_32x32x16_bf16(__builtin_bit_cast(h16x8, a), __builtin_bit_cast(h16x8, b), c, 0, 0, 0)
#define MF16(a, b, c) __builtin_amdgcn_mfma_f32_16x16x32_bf16(__builtin_bit_cast(h16x8, a), __builtin_bit_cast(h16x8, b), c, 0, 0, 0)
#define DMA(voff, srd, soff, dst) \
  asm volatile("s_mov_b32 m0, %0\n\ts_nop 0\n\tbuffer_load_dwordx4 %1, %2, %3 offen lds" ::"s"(dst), "v"(voff), "s"(srd), "s"(soff) : "memory")
#define SETM0(base, off) asm volatile("s_add_u32 m0, %0, %1\n\ts_nop 0" ::"s"(base), "i"(off) : "memory", "scc")
#define DMAP(pol, voff, srd, soff, q) asm volatile("buffer_load_dwordx4 %0, %1, %2 offen offset:%3" pol " lds" ::"v"(voff), "s"(srd), "s"(soff), "i"((q) * 1024) : "memory")
#define DMAQ(voff, srd, soff, q) DMAP("", voff, srd, soff, q)
#define MMV(acc, a, b) asm volatile("v_mfma_f32_32x32x16_bf16 %0, %1, %2, %0" : "+v"(acc) : "v"(a), "v"(b) : "memory")
#define VMCNT(n) asm volatile("s_waitcnt vmcnt(%0)" ::"i"(n) : "memory")
#define LGKM0() asm volatile("s_waitcnt lgkmcnt(0)" ::: "memory")
#define BAR() asm volatile("s_waitcnt lgkmcnt(0)\n\ts_barrier" ::: "memory")
#define SB() __builtin_amdgcn_sched_barrier(0)
#define ST(i)
#define STN(i)

template <int SL = 1> DI void lds_wait(u32 a, u32 n) {
  _Pragma("unroll 1") for (u32 spin = 0; spin < (1u << 22); spin++) {
    asm volatile("" ::: "memory");
    if (__all(*(volatile LAS u32*)a >= n)) break;
    __builtin_amdgcn_s_sleep(SL);
  }
  asm volatile("" ::: "memory");
}
template <int V> struct IC { static constexpr int v = V; };
template <int I, int N, class Fn> DI void sfor(Fn&& f) { if constexpr (I < N) { f(IC<I>{}); sfor<I + 1, N>(f); } }
constexpr int step(int i) { return i < 28 ? i : i < 32 ? i - 16 : i < 44 ? i - 32 : i - 28; }
DI u32x4 lds128(u32 a, u32 o = 0) { return *(const LAS u32x4*)((LAS char*)a + o); }
DI s16x4 tr64(u32 a, u32 o) { return __builtin_amdgcn_ds_read_tr16_b64_v4i16((LAS s16x4*)((LAS char*)a + o)); }
DI u32x4 cat(s16x4 lo, s16x4 hi) { return __builtin_bit_cast(u32x4, __builtin_shufflevector(lo, hi, 0, 1, 2, 3, 4, 5, 6, 7)); }
DI u32x4 ldstr(u32 a, u32 o0, u32 o1) { return cat(tr64(a, o0), tr64(a, o1)); }

struct Srd { const void* p; u32 bytes, flags; };
struct Args {
  Srd x, w1, w2;
  char* ws;
  u16* out;
  const u16 *lar, *aim, *cre, *cim, *ldt, *dsk, *b1, *b2, *gw;
};
DI u32 colblock(u32 jj, u32 w2lo) { return (jj + CBO - (w2lo >> 7)) & 7; }
DI u64 mk64(u32 lo, u32 hi) { return (u64)lo | ((u64)hi << 32); }
#define STAMP_OUT()

__global__ void __launch_bounds__(512, 2) fused_mix(Args) {
  __shared__ __attribute__((aligned(16))) char smem[LDS_BYTES];
  const u32 sb = (u32)(size_t)(LAS char*)smem;
  ST(0);
  u64 a_lar, a_aim, a_cre, a_cim, a_ldt, a_dsk, a_ws, a_oth;
  u32 wave, r_lar, r_aim, r_cre, r_cim, ctag, t_ldt, t_dsk;
  asm volatile(
      "s_load_dwordx2 %[lar], %[ka], 64\n\ts_load_dwordx2 %[ws], %[ka], 48\n\ts_load_dwordx2 %[aim], %[ka], 72\n\ts_load_dwordx2 %[cre], %[ka], 80\n\t"
      "s_load_dwordx2 %[cim], %[ka], 88\n\ts_load_dwordx2 %[ldt], %[ka], 96\n\ts_load_dwordx2 %[dsk], %[ka], 104\n\t"
      "v_readfirstlane_b32 %[wv], %[tid]\n\ts_getpc_b64 %[oth]\n.Lcode_start:\n\t"
      "s_lshr_b32 %[wv], %[wv], 6\n\ts_cmp_lt_u32 %[wv], 4\n\ts_cbranch_scc1 1f\n\t"
      "s_setprio 3\n\tv_and_b32 %[r3], 0xff, %[tid]\n\ts_lshr_b32 %[tg], %[b], 3\n\tv_lshlrev_b32 %[r0], 2, %[r3]\n\t"
      "v_lshl_or_b32 %[r0], %[tg], 10, %[r0]\n\tv_min_u32 %[r0], .Lcode_end - .Lcode_start - 4, %[r0]\n\tglobal_load_dword %[r0], %[r0], %[oth]\n\t"
      "s_lshl_b32 %[tg], %[wv], 3\n\ts_add_u32 %[tg], %[tg], 32\n\ts_load_dwordx2 %[oth], %[ka], %[tg]\n\t"
      "v_lshlrev_b32 %[r3], 1, %[r3]\n\tv_lshl_or_b32 %[r3], %[b], 9, %[r3]\n\ts_xor_b32 %[tl], %[b], %[pp]\n\t"
      "v_and_b32 %[r2], 63, %[tid]\n\tv_lshlrev_b32 %[r2], 3, %[r2]\n\tv_lshl_or_b32 %[r2], %[tl], 9, %[r2]\n\ts_lshl_b32 %[td], %[b], 2\n\t"
      "s_waitcnt lgkmcnt(0)\n\t"
      "global_load_ushort %[r0], %[r3], %[lar]" PPOL "\n\tglobal_load_ushort %[r1], %[r3], %[aim]" PPOL "\n\t"
      "global_load_dword %[r2], %[r2], %[oth]\n\t"
      "global_load_ushort %[r2], %[r3], %[cre]" PPOL "\n\tglobal_load_ushort %[r3], %[r3], %[cim]" PPOL "\n\t"
      "s_load_dword %[tg], %[ws], 0\n\ts_load_dword %[tl], %[ldt], %[td]\n\ts_load_dword %[td], %[dsk], %[td]\n1:"
      : [lar] "=&s"(a_lar), [aim] "=&s"(a_aim), [cre] "=&s"(a_cre), [cim] "=&s"(a_cim), [ldt] "=&s"(a_ldt), [dsk] "=&s"(a_dsk), [ws] "=&s"(a_ws),
        [oth] "=&s"(a_oth), [wv] "=&s"(wave), [tg] "=&s"(ctag), [tl] "=&s"(t_ldt), [td] "=&s"(t_dsk),
        [r0] "=&v"(r_lar), [r1] "=&v"(r_aim), [r2] "=&v"(r_cre), [r3] "=&v"(r_cim)
      : [ka] "s"(__builtin_amdgcn_kernarg_segment_ptr()), [tid] "v"(threadIdx.x), [b] "s"(blockIdx.x), [pp] "i"(PPAIR) : "scc");
  SB();
  const u32 w = wave & 3;
  const u32 b = blockIdx.x, xcd = b & 7, bi = b >> 3;
  const u32 hf = XMAP ? (xcd ^ (xcd >> 2)) & 1 : xcd & 1, p = 4 * (XMAP ? xcd & 3 : xcd >> 1) + (bi >> 3), jj = bi & 7;
  const bool spare = jj == 7;
  const u32 cb1 = 7 * hf + jj, rb = 2 * p + hf, pair = b;
  const u32 wm = w >> 1, wn = w & 1;
#define LANES() const u32 tid = opq(threadIdx.x) & 255, lane = tid & 63, hh = lane >> 5, blk = (lane >> 4) & 1, q4 = (lane & 15) >> 2, p4 = lane & 3, g4 = lane >> 4, l16 = opq(lane * 16)
  const u32 rot = 2 * (bi >> 3) + jj;

  if (wave >= 4) {
    u32x16 k0;
    u32x4 k1;
    asm volatile("s_load_dwordx16 %0, %2, 0\n\ts_load_dwordx4 %1, %2, 120" : "=&s"(k0), "=&s"(k1) : "s"(__builtin_amdgcn_kernarg_segment_ptr()));
    SB();
    LANES();
    const u32 ta = opq(sb + TAB + tid * 224);
    const u32 t0 = opq(sb + TAB + (wm * 128 + 4 * g4 + q4) * 224 + 8 * p4), t1 = opq(t0 + 32 * wn);
    const u32 ko = opq(sb + KD + wm * KD_CH + (4 * g4 * 36 + 16 * wn + (lane & 15)) * 2);
    char* const ws = (char*)a_ws;
    const __amdgpu_buffer_rsrc_t sWS = __builtin_amdgcn_make_buffer_rsrc((void*)ws, 0, WS_END, 0x00020000);
    u32 ga[9];
    {
      auto cell = [&](u32 pos) {
        const u32 hi = pos >> 8, ex = min(3u, 32u - (u32)__clz(hi));
        return sb + KD + (4 * ex + ((8 * hi) >> ex)) * 72 + ((pos & 255) >> 2);
      };
      const u32 P = 2040 - 8 * tid, a0 = cell(P), a8 = cell(P + 8);
      const u32 c0 = w == 3 ? 0x83222100u : w == 2 ? 0x88111000u : 0x88880000u, c1 = w == 3 ? 0x76543210u : w == 2 ? 0x88111000u : 0x88800000u;
      const u32 code = (w == 3 ? hh : (lane & 1)) ? c1 : c0;
#pragma unroll
      for (int k = 0; k < 8; k++) {
        const u32 n = (code >> (4 * k)) & 15u;
        ga[k + 1] = opq(n == 8 ? a8 : a0 + n * 72);
      }
      const u32 lowa = opq(P == 0 ? sb + KD + KD_ZERO : sb + KD + 7 * 72 + ((P - 8) >> 2));
      ga[0] = opq(P > 256 ? a0 : lowa);
    }
    *(LAS u32*)(hh ? sb + CNT + (lane & 15) * 4 : sb + KD + (lane & 1) * KD_CH + KD_ZERO) = 0;
    const u32 to = opq(sb + TC + tid * 16);
    const u32 ur = opq(sb + hf * 8192 + (lane & 7) * 1024 + (((lane >> 3) ^ (4 * (lane & 1))) << 4) + 4 * w);
    const u32 uw = opq(WS_UT + ((rot & 7) * 64 + (lane >> 3) * 8 + 2 * w) * UTS + rb * 128 + (lane & 7) * 16);

    STN(1);
    asm volatile("s_waitcnt lgkmcnt(0)" : "+s"(ctag), "+s"(t_ldt), "+s"(t_dsk), "+s"(k0), "+s"(k1)::"memory");
    asm volatile("s_waitcnt vmcnt(0)" : "+v"(r_lar), "+v"(r_aim), "+v"(r_cre), "+v"(r_cim)::"memory");
    const u32 tag = ctag + 1, cb2 = colblock(jj, k0[8]);
    ST(2);
    {
      const float dt = rbf(__expf(bf2f(wm ? t_ldt >> 16 : t_ldt & 0xffffu)));
      const float er = -(rbf(__expf(bf2f(r_lar))) * dt) * 1.44269504088896f;
      const float wi = (bf2f(r_aim) * dt) * 0.159154943091895f;
      auto seed = [&](float t) {
        const float m = __builtin_amdgcn_exp2f(er * t), a = __builtin_amdgcn_fractf(wi * t);
        return f32x2{m * __builtin_amdgcn_cosf(a), m * __builtin_amdgcn_sinf(a)};
      };
      auto put8 = [&](const f32x2* v, u32 off) {
        u32x4 re = {pk2(v[0].x, v[1].x), pk2(v[2].x, v[3].x), pk2(v[4].x, v[5].x), pk2(v[6].x, v[7].x)};
        u32x4 im = {pk2(v[0].y, v[1].y), pk2(v[2].y, v[3].y), pk2(v[4].y, v[5].y), pk2(v[6].y, v[7].y)};
        *(LAS u32x4*)((LAS char*)ta + off) = re;
        *(LAS u32x4*)((LAS char*)ta + 112 + off) = im;
      };
      f32x2 v[8];
      const Mul w8 = mk(seed(8.f)), w64 = mk(seed(64.f));
      v[0] = f32x2{1.f, 0.f}; v[1] = w8.b; v[2] = cmul(v[1], w8);
      const Mul w16 = mk(v[2]);
#pragma unroll
      for (int k = 3; k < 8; k++) v[k] = cmul(v[k - 2], w16);
      put8(v, 0); SB();
      v[0] = w64.b;
#pragma unroll
      for (int r = 1; r < 4; r++) {
#pragma unroll
        for (int k = (r == 1); k < 8; k++) v[k] = cmul(v[k], w64);
        put8(v, 16 * r); SB();
      }
      f32x2 s1 = seed(1.f), s256 = seed(256.f);
      s1.y = -s1.y; s256.y = -s256.y;
      const Mul v1 = mk(s1), W = mk(s256), v2c = mk(cmul(s1, v1));
      f32x2 u[8];
      u[0] = f32x2{2.f * (bf2f(r_cre) * dt), -2.f * (bf2f(r_cim) * dt)};
      u[1] = cmul(u[0], v1);
#pragma unroll
      for (int k = 2; k < 8; k++) u[k] = cmul(u[k - 2], v2c);
      put8(u, 64); SB();
      v[0] = cmul(u[0], W); v[1] = cmul(u[2], W); v[2] = cmul(u[4], W); v[3] = cmul(u[6], W);
      v[4] = cmul(v[0], W); v[5] = cmul(v[2], W); v[6] = cmul(v[4], W); v[7] = cmul(v[5], W);
      u[0] = cmul(v[6], W); u[1] = cmul(u[0], W); u[2] = cmul(u[1], W); u[3] = cmul(u[2], W); u[4] = cmul(u[3], W);
      put8(v, 80);
      u[5] = u[6] = u[7] = f32x2{0.f, 0.f};
      put8(u, 96);
      ST(3);
      BAR();
      ST(14);
      f32x4 ka[2] = {{(lane == 0 && wn == 0) ? bf2f(wm ? t_dsk >> 16 : t_dsk & 0xffffu) : 0.f, 0.f, 0.f, 0.f}, {0.f, 0.f, 0.f, 0.f}};
      u32x4 fv[8], f0[8], f1[8];
#pragma unroll
      for (int ks = 0; ks < 10; ks++) {
        const u32 o = ks * 16 * 224;
        if (ks < 8) { fv[ks] = ldstr(t1, o, o + 112); f0[ks] = ldstr(t0, o + 64, o + 64 + 112); f1[ks] = ldstr(t0, o + 96, o + 96 + 112); }
        if (ks >= 2) { ka[0] = MF16(f0[ks - 2], fv[ks - 2], ka[0]); ka[1] = MF16(f1[ks - 2], fv[ks - 2], ka[1]); }
        SB();
      }
#pragma unroll
      for (int rt = 0; rt < 2; rt++)
#pragma unroll
        for (int r = 0; r < 4; r += 2) {
          const u32 pr = pk2(ka[rt][r], ka[rt][r + 1]);
          *(LAS u16*)((LAS char*)ko + (16 * rt + r) * 72) = (u16)pr;
          *(LAS u16*)((LAS char*)ko + (16 * rt + r + 1) * 72) = (u16)(pr >> 16);
        }
    }
    ST(9);
    BAR();
    ST(4);
    {
      u32 xr[8];
#pragma unroll
      for (int j = 0; j < 8; j++) xr[j] = *(const LAS u32*)((LAS char*)(ur ^ ((j >> 1) << 4)) + j * 128);
#pragma unroll
      for (int c = 0; c < 2; c++) {
        u32x4 ou;
#pragma unroll
        for (int e = 0; e < 4; e++) ou[e] = __builtin_amdgcn_perm(xr[2 * e + 1], xr[2 * e], c ? 0x07060302 : 0x05040100);
        __builtin_amdgcn_raw_buffer_store_b128(ou, sWS, uw + c * UTS, 0, 16);
      }
    }
    ST(10);
    if (!spare) BAR();
    const u32 ufo = opq(WS_UF + (pair >> 5) * 128 + (lane & 31) * 4);
    const u32 ua = opq(sb + US + wm * US_STR + (256 - 16 - 128 * wn - 96 + 16 * (lane & 15) + 8 * g4) * 2);
    u32 tb[4];
#pragma unroll
    for (int j = 0; j < 4; j++) tb[j] = opq(sb + TC + (2 * wm + (lane & 1)) * TC_STR + (2046 - 16 - 128 * wn - 96 - 1792 + 8 * g4 - (lane & 14)) * 2 + 1024 * j);
    const u32 cw = opq(sb + CNT + 4 * w), cr = opq(sb + CNT + 4 * (lane & 3));
    auto arrive = [&](u32 n) { *(volatile LAS u32*)cw = n; };
    auto meet = [&](u32 n) { LGKM0(); arrive(n); lds_wait(cr, n); };
    if (spare) arrive(1);
    VMCNT(0);
    if (!spare) BAR();
    else {
      meet(2);
      if (tid == 0) asm volatile("buffer_store_dword %0, %1, %2, 0 offen sc1" ::"v"(tag), "v"(WS_UF + (rot & 7) * 128 + rb * 4), "s"(sWS) : "memory");
    }
    ST(5);
    const u32 row = tid >> 3, ck = lane & 7;
    const u32 ro = opq(sb + RED + w * 6144 + l16), r1 = opq(sb + RED + (w ^ 1) * 6144 + l16), r2 = opq(sb + RED + (w ^ 2) * 6144 + l16), r3 = opq(sb + RED + (w ^ 3) * 6144 + l16);
    const u32 ao = opq(WS_AT + pair * ATS + (8 * w + g4) * 512 + (lane & 15) * 16);
    const u32 sa = opq(sb + 2 * SLOT + ck * 1024 + ((row ^ (2 * ck)) << 4));
    if (!spare) BAR();
    const u32 ato = opq(WS_AT + (32 * cb2 + 8 * w + hh) * ATS + rb * 512 + (lane & 31) * 16);
    const u32 go = opq(WS_G + rb * 512 + row * 16);
    const u32 oo = opq((64 * rb + row) * 1024 + cb2 * 128 + ck * 16);
    const u32 aw = opq(sb + TC + (lane & 31) * 144 + (8 * w + hh) * 4), ar = opq(sb + TC + row * 144 + ck * 16);
    const u32 zo = opq(wn ? sb + US + hh * US_STR + (lane & 31) * 16 : sb + TC + ((lane >> 1) & 3) * TC_STR + 4096 + (lane & 1) * 16);
    auto fetch_u = [&]() {
#pragma unroll
      for (int q = 0; q < 8; q++) {
        if (!(q & 3)) SETM0(sb, US + 512 + (q >> 2) * US_STR);
        DMAP(" sc1", l16, sWS, WS_UT + (2 * pair + (q >> 2)) * UTS, q & 3);
      }
    };
    bool got = w != 0;
    if (!spare) {
      BAR(); BAR(); BAR();
      if (got) BAR();
      else {
        u32 uf;
        asm volatile("buffer_load_dword %0, %1, %2, 0 offen sc1\n\ts_barrier\n\ts_waitcnt vmcnt(0)" : "=&v"(uf) : "v"(ufo), "s"(sWS) : "memory");
        got = __all(uf == tag);
        if (got) fetch_u();
      }
    }
    __builtin_amdgcn_s_setprio(CVPRIO);
    ST(21);
    {
      u32 ek[2][9];
#pragma unroll
      for (int i = 0; i < 9; i++) { ek[0][i] = *(const LAS u16*)ga[i]; ek[1][i] = *(const LAS u16*)((LAS char*)ga[i] + KD_CH); }
      *(LAS u32x4*)zo = u32x4{0, 0, 0, 0};
#pragma unroll
      for (int i = 0; i < 4; i++) {
        const u32* e = ek[i >> 1] + (i & 1);
        *(LAS u32x4*)((LAS char*)to + i * TC_STR) = u32x4{e[7] | (e[6] << 16), e[5] | (e[4] << 16), e[3] | (e[2] << 16), e[1] | (e[0] << 16)};
      }
    }
    if (!got) {
      __builtin_amdgcn_s_setprio(3);
      if (spare) __builtin_amdgcn_s_sleep(SPWAIT);
      _Pragma("unroll 1") for (u32 spin = 0; spin < SPIN_CAP; spin++) {
        u32 uf;
        asm volatile("buffer_load_dword %0, %1, %2, 0 offen sc1\n\ts_waitcnt vmcnt(0)" : "=&v"(uf) : "v"(ufo), "s"(sWS) : "memory");
        if (__all(uf == tag)) break;
        __builtin_amdgcn_s_sleep(USLEEP);
      }
      fetch_u();
      __builtin_amdgcn_s_setprio(CVPRIO);
    }
    VMCNT(0);
    ST(6);
    meet(4);
    ST(7);

    f32x4 ca[8];
#pragma unroll
    for (int c = 0; c < 8; c++) ca[c] = f32x4{0.f, 0.f, 0.f, 0.f};
    u32x4 fu[2][8];
    u32x2 ft[2][16];
    auto cread = [&](int f, int r) {
      const int k = r < 2 ? 8 + r : r < 10 ? r - 2 : r;
      if (k < 8) fu[f & 1][k] = lds128(ua, (96 - 32 * f + 256 * k) * 2);
      else {
        const int i = (k - 8) >> 1, hl = k & 1, e = 7 - i;
        const LAS u32* t = (const LAS u32*)((LAS char*)tb[e >> 1] + (96 - 32 * f + 256 * (e & 1)) * 2 + 8 * hl);
        ft[f & 1][2 * i + hl] = u32x2{t[0], t[1]};
      }
    };
#pragma unroll
    for (int r = 0; r < 24; r++) { cread(0, r); SB(); }
#pragma unroll
    for (int f = 0; f < 4; f++) {
      int m = 0;
#pragma unroll
      for (int i = 0; i < 8; i++)
#pragma unroll
        for (int e = 0; e + i < 8; e++, m++) {
          const u32x4 tf = {ft[f & 1][2 * i][0], ft[f & 1][2 * i][1], ft[f & 1][2 * i + 1][0], ft[f & 1][2 * i + 1][1]};
          ca[i + e] = MF16(fu[f & 1][e], tf, ca[i + e]);
          if (f < 3 && m >= 4 && m < 28) cread(f + 1, m - 4);
          if (f == 3 && m == 0) arrive(5);
          SB();
        }
    }
    ST(8);
    f32x4 keep[2];
    __builtin_amdgcn_s_setprio(3);
    lds_wait(cr, 5);
#define SEND(W) { _Pragma("unroll") for (int j = 1; j < 4; j++) { *(LAS f32x4*)((LAS char*)ro + (j - 1) * 2048) = ca[2 * (W ^ j)]; *(LAS f32x4*)((LAS char*)ro + (j - 1) * 2048 + 1024) = ca[2 * (W ^ j) + 1]; } \
                  keep[0] = ca[2 * W]; keep[1] = ca[2 * W + 1]; asm volatile("; send " #W ::: "memory"); }
    if (w == 0) SEND(0) else if (w == 1) SEND(1) else if (w == 2) SEND(2) else SEND(3)
    asm volatile("" ::: "memory");
    arrive(6);
    lds_wait(cr, 6);
    const u32 sel = wm ? 0x01000302 : 0x03020100;
    if (ADELAY) lds_wait<2>(sb + GO, 1);
    f32x4 pp[2][3];
#pragma unroll
    for (int c = 0; c < 2; c++) {
      pp[c][0] = *(const LAS f32x4*)((LAS char*)r1 + c * 1024);
      pp[c][1] = *(const LAS f32x4*)((LAS char*)r2 + 2048 + c * 1024);
      pp[c][2] = *(const LAS f32x4*)((LAS char*)r3 + 4096 + c * 1024);
    }
#pragma unroll
    for (int c = 0; c < 2; c++) {
      u32 o[4];
#pragma unroll
      for (int r = 0; r < 4; r++) {
        const u32 v = pk2(add1(keep[c][r], pp[c][0][r]), add1(pp[c][1][r], pp[c][2][r]));
        o[r] = __builtin_amdgcn_perm(v, v, sel);
      }
#pragma unroll
      for (int r = 0; r < 2; r++) __builtin_amdgcn_raw_buffer_store_b128(u32x4{o[r], tag, o[r + 2], tag}, sWS, ao + c * 2048 + r * 256, 0, 16);
    }
    arrive(7);
    ST(11);
    const __amdgpu_buffer_rsrc_t sB2 = __builtin_amdgcn_make_buffer_rsrc((void*)mk64(k1[0], k1[1]), 0, D * 2, 0x00020000);
    const __amdgpu_buffer_rsrc_t sOut = __builtin_amdgcn_make_buffer_rsrc((void*)mk64(k0[14], k0[15]), 0, L * D * 2, 0x00020000);
    const u32x4 b2v = __builtin_amdgcn_raw_buffer_load_b128(sB2, ck * 16, cb2 * 128, 0);
    lds_wait<LSL>(cr + RDY - CNT, 1);
    ST(12);
    u32x4 gg, at[4];
    _Pragma("unroll 1") for (u32 spin = 0; spin < SPIN_CAP; spin++) {
      asm volatile("" ::: "memory");
#pragma unroll
      for (int j = 0; j < 4; j++) at[j] = __builtin_amdgcn_raw_buffer_load_b128(sWS, ato, 2 * j * ATS, 16);
      gg = __builtin_amdgcn_raw_buffer_load_b128(sWS, go, 0, 16);
      bool ok = gg[1] == tag && gg[3] == tag;
#pragma unroll
      for (int j = 0; j < 4; j++) ok &= at[j][1] == tag && at[j][3] == tag;
      if (__all(ok)) break;
      __builtin_amdgcn_s_sleep(RSLEEP);
    }
    __builtin_amdgcn_s_setprio(TPRIO);
    ST(13);
    lds_wait(cr, 7);
#pragma unroll
    for (int j = 0; j < 4; j++) {
      *(LAS u32*)((LAS char*)aw + j * 8) = at[j][0];
      *(LAS u32*)((LAS char*)aw + j * 8 + 32 * 144) = at[j][2];
    }
    asm volatile("" ::: "memory");
    arrive(8);
    lds_wait(cr, 8);
    ST(15);
#pragma unroll
    for (int t = 0; t < 2; t++) {
      const float g = bf2f(gg[2 * t] & 0xffffu), gm = hi2f(gg[2 * t]);
      const u32x4 av = lds128(ar, t * 32 * 144);
      float base[8];
#pragma unroll
      for (int i = 0; i < 4; i++) {
        base[2 * i] = fopq(fma1(gm, bf2f(b2v[i] & 0xffffu), mul1(g, bf2f(av[i] & 0xffffu))));
        base[2 * i + 1] = fopq(fma1(gm, hi2f(b2v[i]), mul1(g, hi2f(av[i]))));
      }
      if (t == 0) { ST(16); lds_wait<LSL>(cr + RDY - CNT, 2); } else { ST(18); asm volatile("s_barrier" ::: "memory"); ST(19); }
      f32x4 pt[4][2];
#pragma unroll
      for (int k = 0; k < 4; k++)
#pragma unroll
        for (int q = 0; q < 2; q++) pt[k][q] = *(const LAS f32x4*)((LAS char*)sa + (1 - t) * SLOT + k * 8192 + q * 512);
      u32x4 ow;
#pragma unroll
      for (int i = 0; i < 4; i++) {
        float o[2];
#pragma unroll
        for (int e = 0; e < 2; e++) {
          const int q = i >> 1, r = 2 * (i & 1) + e;
          o[e] = t ? __builtin_fmaf(gm, (pt[0][q][r] + pt[1][q][r]) + (pt[2][q][r] + pt[3][q][r]), base[2 * i + e])
                   : fma1(gm, add1(add1(pt[0][q][r], pt[1][q][r]), add1(pt[2][q][r], pt[3][q][r])), base[2 * i + e]);
        }
        ow[i] = pk2(o[0], o[1]);
      }
      __builtin_amdgcn_raw_buffer_store_b128(ow, sOut, oo + t * 32768, 0, OUTPOL);
      SB();
      if (t == 0) ST(17);
    }
    asm volatile(".Lcode_end:");
    STAMP_OUT();
    return;
  }

  u32x16 k0;
  u32x4 k1;
  asm volatile("s_load_dwordx16 %0, %2, 0\n\ts_load_dwordx4 %1, %2, 112"
               : "=&s"(k0), "=&s"(k1) : "s"(__builtin_amdgcn_kernarg_segment_ptr()));
  SB();
  LANES();
  const u32 o1 = opq(((spare ? 0 : cb1) * 128 + wn * 64 + (lane & 31)) * 2);
  u32 vA[4], vB[4], v2[4];
#pragma unroll
  for (int q = 0; q < 4; q++) {
    vA[q] = opq((lane >> 3) * 1024 + (((lane & 7) ^ g4 ^ (4 * (q & 1))) << 4) + q * (8192 - 1024));
    vB[q] = opq(g4 * (F * 2) + (((lane & 15) ^ (g4 << 2) ^ q) << 4) + q * (4 * F * 2 - 1024));
    v2[q] = opq((lane >> 3) * 1024 + (((lane & 7) ^ (blk << 2)) << 4) + q * (8192 - 1024));
  }
  const u32 xbase = sopq((128 * p + 32 * w) * 1024), wbase = sopq(16 * w * (F * 2) + cb1 * 256);
  const u32 m4 = sopq(sb + w * 4096), m8 = sopq(sb + w * 8192);
  asm volatile("s_waitcnt lgkmcnt(0)" : "+s"(k0), "+s"(k1) : "s"(a_lar), "s"(a_aim), "s"(a_cre), "s"(a_cim), "s"(a_ldt), "s"(a_dsk), "s"(a_ws));
  ST(14);
  const u32x4 sX = k0.s0123, sW1 = k0.s4567;
  const u32x4 sW2 = k0.s89ab;
  char* const ws = (char*)mk64(k0[12], k0[13]);
  const u32 w2base = 28 * w * 16384 + colblock(jj, sW2[0]) * 128;
  u32 tag;
  const __amdgpu_buffer_rsrc_t sWS = __builtin_amdgcn_make_buffer_rsrc((void*)ws, 0, WS_END, 0x00020000);
  auto w2piece = [&](int c, int q) {
    if ((q & 3) == 0) SETM0(m8, (c & 3) * SLOT + (q >> 2) * 4096);
    DMAQ(v2[q & 3], sW2, w2base + c * 65536 + (q >> 2) * 32768, q & 3);
  };

  if (!spare) {
    auto g1piece = [&](int g, int q) {
      if (q == 0) SETM0(m4, (g & 3) * SLOT);
      if (q == 4) SETM0(m4, (g & 3) * SLOT + 16384);
      if (q < 4) DMAP(XPOL, vA[q], sX, xbase + ((g + rot) & 7) * 128, q);
      else DMAQ(vB[q - 4], sW1, wbase + ((g + rot) & 7) * 64 * (F * 2), q - 4);
    };
    u32 r_b1a, r_b1b;
    asm volatile("s_load_dword %0, %1, 0" : "=s"(tag) : "s"(ws));
    asm volatile("global_load_ushort %0, %1, %2" : "=v"(r_b1a) : "v"(o1), "s"(mk64(k1[0], k1[1])));
    asm volatile("global_load_ushort %0, %1, %2 offset:64" : "=v"(r_b1b) : "v"(o1), "s"(mk64(k1[0], k1[1])));
#pragma unroll
    for (int i = 0; i < 12; i++) g1piece(i >> 3, i < 8 ? i : i - 4);
    ST(1);
    u32 ax[2][4], aw[2][4];
    {
      const u32 xa = sb + (64 * wm + (lane & 31)) * 128 + ((hh ^ ((lane >> 1) & 7)) << 4);
      const u32 wa = sb + 16384 + 256 * (8 * hh + q4) + 8 * (p4 & 1) + (((8 * wn + 2 * blk + (p4 >> 1)) ^ ((q4 << 2) | (2 * hh))) << 4);
#pragma unroll
      for (int k = 0; k < 4; k++) {
        ax[0][k] = opq(xa ^ (k << 5)); aw[0][k] = opq(wa ^ ((k & 1) << 4) ^ ((k >> 1) << 6));
        ax[1][k] = opq(ax[0][k] + 2 * SLOT); aw[1][k] = opq(aw[0][k] + 2 * SLOT);
      }
    }
    const u32 ho = opq(l16 + WS_H + ((4 * p + 2 * wm) * 112 + cb1 * 8 + wn * 4) * 1024), ho1 = opq(ho + 112 * 1024);
    asm volatile("s_waitcnt lgkmcnt(0)" : "+s"(tag));
    const u32 vtag = opq(tag + 1), vuf = opq(WS_UF + (rot & 7) * 128 + rb * 4);
#pragma unroll
    for (int q = 0; q < 8 * LA - 12; q++) {
      if (q == PRET) { asm volatile("s_barrier" ::: "memory"); ST(3); }
      g1piece(q < 4 ? 1 : 2, q < 4 ? q : q - 4);
    }
    if (PRET >= 8 * LA - 12) { asm volatile("s_barrier" ::: "memory"); ST(3); }
    asm volatile("s_waitcnt vmcnt(%2)" : "+v"(r_b1a), "+v"(r_b1b) : "i"(8 * LA));
    f32x16 acc[2][2];
    {
      const u32x4 one = {hh ? 0u : 0x3f80u, 0u, 0u, 0u};
      const u32x4 ba = {r_b1a, 0u, 0u, 0u}, bb = {r_b1b, 0u, 0u, 0u};
      asm volatile("s_nop 1\n\tv_mfma_f32_32x32x16_bf16 %0, %4, %6, 0\n\tv_mfma_f32_32x32x16_bf16 %1, %4, %6, 0\n\t"
                   "v_mfma_f32_32x32x16_bf16 %2, %5, %6, 0\n\tv_mfma_f32_32x32x16_bf16 %3, %5, %6, 0"
                   : "=&v"(acc[0][0]), "=&v"(acc[0][1]), "=&v"(acc[1][0]), "=&v"(acc[1][1]) : "v"(ba), "v"(bb), "v"(one));
    }
    u32x4 fx[2][2];
    s16x4 fw[2][4];
    VMCNT(8 * LA - 8);
    ST(2);
    BAR();
    ST(4);
    fx[0][0] = lds128(ax[0][0]); fw[0][0] = tr64(aw[0][0], 0); fw[0][1] = tr64(aw[0][1], 1024);
    fx[0][1] = lds128(ax[0][0], 4096); fw[0][2] = tr64(aw[0][2], 0); fw[0][3] = tr64(aw[0][3], 1024);
    u32x4 tx[2][8];
    s16x4 tw[4][8];
    auto tread = [&](int r) {
      if (r < 24) {
        const int ks = r / 3, j = r % 3;
        if (j == 2) tx[0][ks] = lds128(ax[1][ks & 3], (ks >> 2) * SLOT);
        else tw[j][ks] = tr64(aw[1][j], (ks >> 2) * SLOT + (ks & 3) * 4096 + j * 1024);
      } else if (r < 32) tx[1][r - 24] = lds128(ax[1][r & 3], ((r - 24) >> 2) * SLOT + 4096);
      else if (r < 48) { const int ks = (r - 32) >> 1, j = r & 1; tw[2 + j][ks] = tr64(aw[1][2 + j], (ks >> 2) * SLOT + (ks & 3) * 4096 + j * 1024); }
    };
#pragma unroll
    for (int s = 0; s < 6; s++) {
      auto piece = [&](int q) { if (s < 5) g1piece(s + 3, q); else if (WPRE > 0) w2piece(0, q); };
#pragma unroll
      for (int k = 0; k < 4; k++) {
        const int c = k & 1, n = c ^ 1, kn = (k + 1) & 3, sl = (k == 3 ? s + 1 : s) & 3, hi = sl >> 1;
        const u32 o = (sl & 1) * SLOT, ow = o + kn * 4096;
        const bool more = s < 5 || k < 3;
        MMV(acc[0][0], cat(fw[c][0], fw[c][1]), fx[c][0]);
        if (k == 3) { VMCNT(s < 5 ? 14 : WPRE > 0 ? 6 : 0); BAR(); }
        if (more) { fx[n][0] = lds128(ax[hi][kn], o); fw[n][0] = tr64(aw[hi][0], ow); fw[n][1] = tr64(aw[hi][1], ow + 1024); }
        else { tread(0); tread(1); tread(2); }
        MMV(acc[0][1], cat(fw[c][0], fw[c][1]), fx[c][1]);
        if (more) { fx[n][1] = lds128(ax[hi][kn], o + 4096); fw[n][2] = tr64(aw[hi][2], ow); }
        else { tread(3); tread(4); tread(5); }
        piece(2 * k);
        MMV(acc[1][0], cat(fw[c][2], fw[c][3]), fx[c][0]);
        if (more) fw[n][3] = tr64(aw[hi][3], ow + 1024); else { tread(6); tread(7); tread(8); }
        MMV(acc[1][1], cat(fw[c][2], fw[c][3]), fx[c][1]);
        if (!more) { tread(9); tread(10); tread(11); }
        piece(2 * k + 1);
      }
      if (s == 1)
        asm volatile("s_cmp_lg_u32 %3, 0\n\ts_cbranch_scc1 1f\n\ts_mov_b64 exec, 1\n\tbuffer_store_dword %0, %1, %2, 0 offen sc1\n\ts_mov_b64 exec, -1\n1:"
                     ::"v"(vtag), "v"(vuf), "s"(sWS), "s"(w) : "memory", "scc");
      if (s == 0) ST(8);
      if (s == 1) ST(15);
      if (s == 4) ST(19);
      if (s == 5) ST(20);
    }
    __builtin_amdgcn_s_setprio(2);
    u32x4 ho4;
    auto square = [&](int u, int t, int i) {
      ho4[i & 3] = relu2(acc[u][t][2 * i], acc[u][t][2 * i + 1]);
      if ((i & 3) == 3) __builtin_amdgcn_raw_buffer_store_b128(ho4, sWS, (t ? ho1 : ho) + (2 * u + (i >> 2)) * 1024, 0, 16);
    };
    sfor<0, 32>([&](auto ic) {
      constexpr int m = decltype(ic)::v, ph = m >> 3, ks = m & 7, u = ph >> 1, t = (ph ^ u) & 1;
      MMV(acc[u][t], cat(tw[2 * u][ks], tw[2 * u + 1][ks]), tx[t][ks]);
      if (m < 8) { tread(12 + 3 * m); tread(13 + 3 * m); tread(14 + 3 * m); }
      else if (m < 14) { tread(20 + 2 * m); tread(21 + 2 * m); }
      if (m < 8 && WPRE > 1) w2piece(1, m);
      if (m == TBAR) BAR();
      if (m >= 10) {
        constexpr int e = m - 10, pp = e >> 3, pu = pp >> 1, pt = (pp ^ pu) & 1;
        if ((e & 7) == 0) asm volatile("" : "+v"(acc[pu][pt]));
        square(pu, pt, e & 7);
      }
      SB();
    });
    ST(5);
    square(1, 1, 6); square(1, 1, 7);
    asm volatile("s_nop 7" : "+v"(acc[1][0]));
#pragma unroll
    for (int i = 0; i < 8; i++) square(1, 0, i);
    tag += 1;
  } else {
    asm volatile("s_load_dword %0, %1, 0" : "=s"(tag) : "s"(ws));
    SETM0(m4, 0);
#pragma unroll
    for (int q = 0; q < 4; q++) DMAP(XPOL, vA[q], sX, xbase + (rot & 7) * 128, q);
    u32x4 gwv, xr[16];
    asm volatile("global_load_dwordx4 %0, %1, %2" : "=v"(gwv) : "v"(l16), "s"(((const CAS Args*)__builtin_amdgcn_kernarg_segment_ptr())->gw));
#pragma unroll
    for (int j = 0; j < 16; j++) {
      if (j == 7) asm volatile("s_barrier" ::: "memory");
      asm volatile("buffer_load_dwordx4 %0, %1, %2, %3 offen offset:%4" : "=v"(xr[j]) : "v"(l16), "s"(sX), "s"((64 * rb + 16 * w + (j & ~3)) * 1024), "i"((j & 3) * 1024));
    }
    asm volatile("s_waitcnt vmcnt(17) lgkmcnt(0)\n\ts_barrier" : "+s"(tag)::"memory");
    tag += 1;
    ST(2);
    asm volatile("s_waitcnt vmcnt(0)" : "+v"(gwv), "+v"(xr[0]), "+v"(xr[1]), "+v"(xr[2]), "+v"(xr[3]), "+v"(xr[4]), "+v"(xr[5]), "+v"(xr[6]), "+v"(xr[7]),
                 "+v"(xr[8]), "+v"(xr[9]), "+v"(xr[10]), "+v"(xr[11]), "+v"(xr[12]), "+v"(xr[13]), "+v"(xr[14]), "+v"(xr[15])::"memory");
    SETM0(m8, SLOT);
    auto pull = [&](u32 n) {
#pragma unroll
      for (int q = 0; q < 4; q++) DMAQ(l16, sW2, min(14 * xcd + 4 * n + (bi >> 3), 111u) * 16384 + w * 4096, q);
    };
    pull(0); pull(1);
    ST(8);
    const u32 zs = m8 + 2 * SLOT;
#pragma unroll
    for (int j = 0; j < 16; j++) {
      float z = 0.f;
#pragma unroll
      for (int e = 0; e < 4; e++) z = dot2(xr[j][e], gwv[e], z);
      *(LAS float*)((LAS char*)(zs + lane * 4) + j * 272) = z;
    }
    VMCNT(4); pull(2);
    LGKM0();
    f32x4 z4 = {};
#pragma unroll
    for (int k = 0; k < 4; k++) z4 += *(const LAS f32x4*)((LAS char*)(zs + (lane & 15) * 272 + g4 * 64) + k * 16);
    float zz = (z4[0] + z4[1]) + (z4[2] + z4[3]);
    zz += __shfl_xor(zz, 16); zz += __shfl_xor(zz, 32);
    const float g = rbf(1.f / (1.f + __expf(-zz)));
    VMCNT(4); pull(3);
    if (lane < 16) __builtin_amdgcn_raw_buffer_store_b64(u32x2{pk2(g, 1.f - g), tag}, sWS, WS_G + rb * 512 + (wn * 16 + lane) * 16 + wm * 8, 0, 16);
    lds_wait(sb + CNT + 4 * (lane & 3), 1);
    ST(4);
#pragma unroll
    for (int i = 0; i < 8 * WPRE; i++) { if (!(i & 3)) VMCNT(THR); w2piece(i >> 3, i & 7); }
  }
  SB();
  ST(6);

  u32 w2a[2][2];
  {
    const u32 a = m8 + 128 * (4 * hh + q4) + 8 * (p4 & 1) + (((2 * blk + (p4 >> 1)) ^ ((q4 >> 1) << 2)) << 4);
    w2a[0][0] = opq(a); w2a[1][0] = opq(a ^ 64); w2a[0][1] = opq(a + 2 * SLOT); w2a[1][1] = opq((a ^ 64) + 2 * SLOT);
  }
  auto w2frag = [&](int st, int v) {
    const int sl = (st >> 2) & 3;
    const u32 o = (sl & 1) * SLOT + (st & 3) * 2048;
    return ldstr(w2a[v][sl >> 1], o, o + 1024);
  };
  u32x4 w2r[12][2];
  u32 fl = 0;
  const u32 fo = opq(WS_HF + p * 128 + (((lane & 7) == 7) ? 0 : (lane & 15) * 4));
  if (XEARLY) {
    asm volatile("s_cmp_lg_u32 %0, 7\n\ts_cbranch_scc1 1f\n\ts_waitcnt vmcnt(0)\n1: s_waitcnt vmcnt(8)" ::"s"(jj) : "memory", "scc");
#pragma unroll
    for (int j = 0; j < 24; j++) w2r[j >> 1][j & 1] = w2frag(j >> 1, j & 1);
    LGKM0();
  }
#pragma unroll
  for (int c = WPRE; c < 8; c++) {
    if (c == WPRE + PUBAT) {
      VMCNT(8 * PUBAT);
      *(volatile LAS u32*)(sb + FD + 4 * w) = 1;
      asm volatile("" ::: "memory");
      if (__all(*(volatile LAS u32*)(sb + FD + 4 * (lane & 3)) != 0) && !spare && lane == 0)
        asm volatile("buffer_store_dword %0, %1, %2, 0 offen sc1" ::"v"(tag), "v"(WS_HF + p * 128 + (8 * hf + jj) * 4), "s"(sWS) : "memory");
      ST(7);
      __builtin_amdgcn_s_sleep(WDELAY);
    }
    if (!XEARLY && c >= 4 && c < 7) {
      VMCNT(24);
#pragma unroll
      for (int j = 0; j < 8; j++) w2r[4 * (c - 4) + (j >> 1)][j & 1] = w2frag(4 * (c - 4) + (j >> 1), j & 1);
      LGKM0();
    }
    if (c == POLLAT + 1) {
      asm volatile("s_cmp_lg_u32 %1, %2\n\ts_cbranch_scc1 1f\n\ts_waitcnt vmcnt(8)\n1:" : "+v"(fl) : "s"(w), "i"(PW) : "memory", "scc");
      if (w == PW && __all(fl == tag)) *(volatile LAS u32*)(sb + GO) = 1;
    }
    if (c == POLLAT) asm volatile("s_cmp_lg_u32 %3, %4\n\ts_cbranch_scc1 1f\n\tbuffer_load_dword %0, %1, %2, 0 offen sc1\n1:" : "+v"(fl) : "v"(fo), "s"(sWS), "s"(w), "i"(PW) : "memory", "scc");
    if (c < 7) {
#pragma unroll
      for (int q = 0; q < 8; q++) {
        if (!(q & 3)) asm volatile("s_cmp_lg_u32 %0, 7\n\ts_cbranch_scc1 1f\n\ts_waitcnt vmcnt(%1)\n1:" ::"s"(jj), "i"(THR) : "memory", "scc");
        w2piece(c, q);
      }
    }
  }
  const u32 hb = sopq(WS_H + (2 * rb * 112 + 28 * w) * 1024);
  const u32 xw = opq(m8 + 2 * SLOT + l16), rdw = opq(sb + RDY + 4 * w);
  ST(11);
  if (w == PW) {
    _Pragma("unroll 1") for (u32 spin = 0; spin < SPIN_CAP; spin++) {
      if (__all(fl == tag)) break;
      if (spin) __builtin_amdgcn_s_sleep(HSLEEP);
      asm volatile("buffer_load_dword %0, %1, %2, 0 offen sc1\n\ts_waitcnt vmcnt(0)" : "=&v"(fl) : "v"(fo), "s"(sWS) : "memory");
    }
    *(volatile LAS u32*)(sb + GO) = 1;
  } else {
    lds_wait<GSL>(sb + GO, 1);
  }
  __builtin_amdgcn_s_setprio(G2PRIO);
  ST(10);
  SB();

  u32x4 hfr[56];
  auto load_h = [&](int i) {
    const int st = step(i);
    hfr[i] = __builtin_amdgcn_raw_buffer_load_b128(sWS, l16 + (st & 3) * 1024, hb + ((i / 28) * 112 + (st & ~3)) * 1024, 16);
  };
#pragma unroll
  for (int i = 0; i < HEAD; i++) load_h(i);
  SB();
  f32x16 oc[2][2] = {};
  auto send = [&](int t, int j) {
    const f32x16& s = oc[t][j >> 2];
    const int r = 4 * (j & 3);
    *(LAS f32x4*)((LAS char*)(xw ^ (j << 5)) + (1 - t) * SLOT + j * 1024) = f32x4{s[r], s[r + 1], s[r + 2], s[r + 3]};
  };
  auto signal = [&](u32 n) { asm volatile("" ::: "memory"); *(volatile LAS u32*)rdw = n; };
  u32x4 wl[56][2];
  auto fetch_w = [&](int i) { if (i < 56 && step(i) >= 12) { wl[i][0] = w2frag(step(i), 0); wl[i][1] = w2frag(step(i), 1); } };
  sfor<0, 56>([&](auto ic) {
    constexpr int i = decltype(ic)::v, t = i / 28, st = step(i);
    constexpr int D2 = 53 - TAILN;
    if (i < D2 || !TAILN) fetch_w(i + 1); else { fetch_w(2 * i - D2 + 1); fetch_w(2 * i - D2 + 2); }
    SB();
    u32x4 a0, a1;
    if (st < 12) { a0 = w2r[st < 12 ? st : 0][0]; a1 = w2r[st < 12 ? st : 0][1]; } else { a0 = wl[i][0]; a1 = wl[i][1]; }
    oc[t][0] = MF32(a0, hfr[i], oc[t][0]);
    if (i + HEAD < 56) load_h(i + HEAD);
    SB();
    if (i < 56 - TAILN) oc[t][1] = MF32(a1, hfr[i], oc[t][1]);
    if (i == ATAT) signal(1);
    if (i >= 31 && i < 39) send(0, i - 31);
    if (i == 38) signal(2);
    SB();
    if (i == 27) ST(18);
  });
#pragma unroll
  for (int i = 0; i < TAILN; i++) {
    oc[1][1] = MF32(wl[56 - TAILN + i][1], hfr[56 - TAILN + i], oc[1][1]);
    SB();
    if (i >= 1 && i < 3) { send(1, 2 * i - 2); send(1, 2 * i - 1); }
    SB();
  }
  ST(12);
#pragma unroll
  for (int j = TAILN >= 3 ? 4 : 0; j < 8; j++) send(1, j);
  BAR();
  if (b == 0 && w == 3) {
    _Pragma("unroll 1") for (u32 spin = 0; spin < SPIN_CAP; spin++) {
      asm volatile("" ::: "memory");
      const u32x4 f0 = __builtin_amdgcn_raw_buffer_load_b128(sWS, WS_UF + l16, 0, 16);
      if (__all(f0[0] == tag && f0[1] == tag && f0[2] == tag && f0[3] == tag)) break;
    }
    if (lane == 0) __builtin_amdgcn_raw_buffer_store_b32(tag, sWS, 0, 0, 16);
  }
  STAMP_OUT();
}

extern "C" void kernel_launch(void* const* d_in, const int* in_sizes, int n_in, void* d_out, int out_size,
                              void* d_ws, size_t ws_size, hipStream_t stream) {
  Args a;
  a.x = Srd{d_in[0], L * D * 2, 0x00020000}; a.w1 = Srd{d_in[8], D * F * 2, 0x00020000}; a.w2 = Srd{d_in[10], D * F * 2, 0x00020000};
  a.ws = (char*)d_ws + WSO; a.out = (u16*)d_out;
  a.lar = (const u16*)d_in[1]; a.aim = (const u16*)d_in[2]; a.cre = (const u16*)d_in[3]; a.cim = (const u16*)d_in[4];
  a.ldt = (const u16*)d_in[5]; a.dsk = (const u16*)d_in[6]; a.b1 = (const u16*)d_in[9]; a.b2 = (const u16*)d_in[11];
  a.gw = (const u16*)d_in[12];
  fused_mix<<<256, 512, 0, stream>>>(a);
}
```

```cpp
#include <hip/hip_runtime.h>
#pragma clang diagnostic ignored "-Wint-to-pointer-cast"

typedef unsigned short u16;
typedef unsigned u32;
typedef unsigned long long u64;
typedef __bf16 h16x8 __attribute__((ext_vector_type(8)));
typedef __bf16 h16x2 __attribute__((ext_vector_type(2)));
typedef short s16x4 __attribute__((ext_vector_type(4)));
typedef short s16x8 __attribute__((ext_vector_type(8)));
typedef short s16x2 __attribute__((ext_vector_type(2)));
typedef float f32x2 __attribute__((ext_vector_type(2)));
typedef float f32x4 __attribute__((ext_vector_type(4)));
typedef float f32x16 __attribute__((ext_vector_type(16)));
typedef u32 u32x2 __attribute__((ext_vector_type(2)));
typedef u32 u32x4 __attribute__((ext_vector_type(4)));
typedef u32 u32x16 __attribute__((ext_vector_type(16)));
#define LAS __attribute__((address_space(3)))
#define CAS __attribute__((address_space(4)))
#define GAS __attribute__((address_space(1)))
#define DI __device__ __forceinline__

constexpr int L = 2048, D = 512, F = 1792;
constexpr u32 WS_UF = 8192;
constexpr u32 WS_HF = 65536;
constexpr u32 WS_AF = 12288;
constexpr u32 WS_G = 1u << 19;
#define UTS 4096
#define ATS 8192
#define WSO 0
constexpr u32 WS_UT = 1u << 20;
constexpr u32 WS_AT = 4u << 20;
constexpr u32 WS_H = 9u << 20;
constexpr u32 WS_END = 200u << 20;
constexpr u32 WS_STAMP = 220u << 20;
constexpr u32 SLOT = 32768;
constexpr u32 TAB = 3 * SLOT;
constexpr u32 TC = 4 * SLOT;
constexpr u32 TC_STR = 4160;
constexpr u32 US = TC + 4 * TC_STR;
constexpr u32 US_STR = (256 + 2048) * 2;
constexpr u32 KD_CH = 32 * 36 * 2;
constexpr u32 LDS_BYTES = 163840;
constexpr u32 KD = LDS_BYTES - 2 * KD_CH;
constexpr u32 KD_ZERO = 34 * 2;
constexpr u32 CNT = KD - 64;
constexpr u32 FD = CNT + 16, RDY = CNT + 32, GO = CNT + 48;
constexpr u32 RED = TC;
static_assert(US + 2 * US_STR <= CNT && TAB + 256 * 224 + 16 <= CNT, "lds map");
constexpr u32 SPIN_CAP = 1u << 16;
#define EPK 0
#define WDELAY 0
#define CBO 4
#define TAILN 4
#define EXPE 0
#define LSL 4
#define TPRIO 3
#define UTRY 1
#define XMAP 1
#define PRET 0
#define THR 4
#define SPWAIT 32
#define XEARLY 1
#define ADELAY 0
#define WBAL 0
#define BASE1 1
#define GSL 1
#define POLLP 42
#define HROW 112
#define ROT2 (xcd % 7)
#define FTHR 0
#define PUBM 2
#define ALLW 1
#define HFL 0
#define SPSL 12
#define TBAR 24
#define PW 0
#define UEARLY 1
#define UPB 0
#define CHK 9
#define NC 3
#define TORD 1
#define LSL2 1
#define ROT1 0
#define NOSH 0
#define PFD2 0
#define SH2 0
#define PFIRST 0
#define AWF 0
#define SPRIO 2
#define AORD 0
#define HEAD 13
#define WPRE 2
#define PUBAT 0
#define PPOL ""
#define PPOL2 ""
#define XPOL ""
#define NPF 4
#define PFDELAY 24
#define POLLAT 6
#define PFC 0
#define LA 3
#define OUTPOL 16
#define UPUB 2
#define RSLEEP 4
#define USLEEP 16
#define HSLEEP 2
#define PPAIR 4
#define ATAT 20
#define G2PRIO 0
#define CVPRIO 0

DI float bf2f(u32 v) { return __uint_as_float(v << 16); }
DI u32 pk2(float lo, float hi) { f32x2 a = {lo, hi}; return __builtin_bit_cast(u32, __builtin_convertvector(a, h16x2)); }
DI float rbf(float x) { return bf2f(pk2(x, x) & 0xffffu); }
DI float dot2(u32 a, u32 b, float c) { return __builtin_amdgcn_fdot2_f32_bf16(__builtin_bit_cast(h16x2, a), __builtin_bit_cast(h16x2, b), c, false); }
DI u32 rfl(u32 x) { return __builtin_amdgcn_readfirstlane(x); }
struct Mul { f32x2 b, n; };
DI Mul mk(f32x2 b) { return Mul{b, f32x2{-b.y, b.y}}; }
DI f32x2 cmul(f32x2 a, Mul m) { return __builtin_elementwise_fma(a.yx, m.n, a * m.b.xx); }
DI float mul1(float a, float b) { float r; asm("v_mul_f32 %0, %1, %2" : "=v"(r) : "v"(a), "v"(b)); return r; }
DI float add1(float a, float b) { float r; asm("v_add_f32 %0, %1, %2" : "=v"(r) : "v"(a), "v"(b)); return r; }
DI float fma1(float a, float b, float c) { float r; asm("v_fma_f32 %0, %1, %2, %3" : "=v"(r) : "v"(a), "v"(b), "v"(c)); return r; }
DI float hi2f(u32 v) { return __uint_as_float(v & 0xffff0000u); }
DI u32 relu2(float a, float b) {
  u32 r, t;
  asm("v_max_i32 %0, 0, %2\n\tv_max_i32 %1, 0, %3\n\tv_mul_f32 %0, %0, %0\n\tv_mul_f32 %1, %1, %1\n\tv_cvt_pk_bf16_f32 %0, %0, %1" : "=&v"(r), "=&v"(t) : "v"(a), "v"(b));
  return r;
}
DI float add4(float a0, float a1, float a2, float a3) {
  float r, t;
  asm("v_add_f32 %0, %2, %3\n\tv_add_f32 %1, %4, %5\n\tv_add_f32 %0, %0, %1" : "=&v"(r), "=&v"(t) : "v"(a0), "v"(a1), "v"(a2), "v"(a3));
  return r;
}
DI float sum4(float g, float a0, float a1, float a2, float a3, float c) {
  float r, t;
  asm("v_add_f32 %0, %3, %4\n\tv_add_f32 %1, %5, %6\n\tv_add_f32 %0, %0, %1\n\tv_fma_f32 %0, %2, %0, %7" : "=&v"(r), "=&v"(t) : "v"(g), "v"(a0), "v"(a1), "v"(a2), "v"(a3), "v"(c));
  return r;
}
DI float fopq(float x) { asm volatile("" : "+v"(x)); return x; }
DI float relu(float x) { return __int_as_float(max(__float_as_int(x), 0)); }
DI u32 sopq(u32 x) { asm volatile("" : "+s"(x)); return x; }
DI u32 opq(u32 x) { asm volatile("" : "+v"(x)); return x; }
#define MF32(a, b, c) __builtin_amdgcn_mfma_f32_32x32x16_bf16(__builtin_bit_cast(h16x8, a), __builtin_bit_cast(h16x8, b), c, 0, 0, 0)
#define MF16(a, b, c) __builtin_amdgcn_mfma_f32_16x16x32_bf16(__builtin_bit_cast(h16x8, a), __builtin_bit_cast(h16x8, b), c, 0, 0, 0)
#define DMA(voff, srd, soff, dst) \
  asm volatile("s_mov_b32 m0, %0\n\ts_nop 0\n\tbuffer_load_dwordx4 %1, %2, %3 offen lds" ::"s"(dst), "v"(voff), "s"(srd), "s"(soff) : "memory")
#define SETM0(base, off) asm volatile("s_add_u32 m0, %0, %1\n\ts_nop 0" ::"s"(base), "i"(off) : "memory", "scc")
#define DMAP(pol, voff, srd, soff, q) asm volatile("buffer_load_dwordx4 %0, %1, %2 offen offset:%3" pol " lds" ::"v"(voff), "s"(srd), "s"(soff), "i"((q) * 1024) : "memory")
#define DMAQ(voff, srd, soff, q) DMAP("", voff, srd, soff, q)
#define MMV(acc, a, b) asm volatile("v_mfma_f32_32x32x16_bf16 %0, %1, %2, %0" : "+v"(acc) : "v"(a), "v"(b) : "memory")
#define VMCNT(n) asm volatile("s_waitcnt vmcnt(%0)" ::"i"(n) : "memory")
#define LGKM0() asm volatile("s_waitcnt lgkmcnt(0)" ::: "memory")
#define BAR() asm volatile("s_waitcnt lgkmcnt(0)\n\ts_barrier" ::: "memory")
#define SB() __builtin_amdgcn_sched_barrier(0)
#define ST(i)
#define STN(i)

template <int SL = 1> DI void lds_wait(u32 a, u32 n) {
  _Pragma("unroll 1") for (u32 spin = 0; spin < (1u << 22); spin++) {
    asm volatile("" ::: "memory");
    if (__all(*(volatile LAS u32*)a >= n)) break;
    __builtin_amdgcn_s_sleep(SL);
  }
  asm volatile("" ::: "memory");
}
template <int V> struct IC { static constexpr int v = V; };
template <int I, int N, class Fn> DI void sfor(Fn&& f) { if constexpr (I < N) { f(IC<I>{}); sfor<I + 1, N>(f); } }
constexpr int step(int i) { return i < 28 ? i : i < 32 ? i - 16 : i < 44 ? i - 32 : i - 28; }
DI u32x4 lds128(u32 a, u32 o = 0) { return *(const LAS u32x4*)((LAS char*)a + o); }
DI s16x4 tr64(u32 a, u32 o) { return __builtin_amdgcn_ds_read_tr16_b64_v4i16((LAS s16x4*)((LAS char*)a + o)); }
DI u32x4 cat(s16x4 lo, s16x4 hi) { return __builtin_bit_cast(u32x4, __builtin_shufflevector(lo, hi, 0, 1, 2, 3, 4, 5, 6, 7)); }
DI u32x4 ldstr(u32 a, u32 o0, u32 o1) { return cat(tr64(a, o0), tr64(a, o1)); }

struct Srd { const void* p; u32 bytes, flags; };
struct Args {
  Srd x, w1, w2;
  char* ws;
  u16* out;
  const u16 *lar, *aim, *cre, *cim, *ldt, *dsk, *b1, *b2, *gw;
};
DI u32 colblock(u32 jj, u32 w2lo) { return (jj + CBO - (w2lo >> 7)) & 7; }
DI u32 hfo(u32 p, u32 i) { return HFL == 0 ? p * 128 + i * 4 : HFL == 1 ? p * 256 + (i >> 3) * 128 + (i & 7) * 4 : HFL == 2 ? (p * 16 + i) * 128 : (p * 16 + i) * 1152; }
DI u64 mk64(u32 lo, u32 hi) { return (u64)lo | ((u64)hi << 32); }
#define STAMP_OUT()

__global__ void __launch_bounds__(512, 2) fused_mix(Args) {
  __shared__ __attribute__((aligned(16))) char smem[LDS_BYTES];
  const u32 sb = (u32)(size_t)(LAS char*)smem;
  ST(0);
  u64 a_lar, a_aim, a_cre, a_cim, a_ldt, a_dsk, a_ws, a_oth;
  u32 wave, r_lar, r_aim, r_cre, r_cim, ctag, t_ldt, t_dsk;
  asm volatile(
      "s_load_dwordx2 %[lar], %[ka], 64\n\ts_load_dwordx2 %[ws], %[ka], 48\n\ts_load_dwordx2 %[aim], %[ka], 72\n\ts_load_dwordx2 %[cre], %[ka], 80\n\t"
      "s_load_dwordx2 %[cim], %[ka], 88\n\ts_load_dwordx2 %[ldt], %[ka], 96\n\ts_load_dwordx2 %[dsk], %[ka], 104\n\t"
      "v_readfirstlane_b32 %[wv], %[tid]\n\ts_getpc_b64 %[oth]\n.Lcode_start:\n\t"
      "s_lshr_b32 %[wv], %[wv], 6\n\ts_cmp_lt_u32 %[wv], 4\n\ts_cbranch_scc1 1f\n\t"
      "s_setprio 3\n\tv_and_b32 %[r3], 0xff, %[tid]\n\ts_lshr_b32 %[tg], %[b], 3\n\tv_lshlrev_b32 %[r0], 2, %[r3]\n\t"
      "v_lshl_or_b32 %[r0], %[tg], 10, %[r0]\n\tv_min_u32 %[r0], .Lcode_end - .Lcode_start - 4, %[r0]\n\tglobal_load_dword %[r0], %[r0], %[oth]\n\t"
      "s_lshl_b32 %[tg], %[wv], 3\n\ts_add_u32 %[tg], %[tg], 32\n\ts_load_dwordx2 %[oth], %[ka], %[tg]\n\t"
      "v_lshlrev_b32 %[r3], 1, %[r3]\n\tv_lshl_or_b32 %[r3], %[b], 9, %[r3]\n\ts_xor_b32 %[tl], %[b], %[pp]\n\t"
      "v_and_b32 %[r2], 63, %[tid]\n\tv_lshlrev_b32 %[r2], 3, %[r2]\n\tv_lshl_or_b32 %[r2], %[tl], 9, %[r2]\n\ts_lshl_b32 %[td], %[b], 2\n\t"
      "s_waitcnt lgkmcnt(0)\n\t"
      "global_load_ushort %[r0], %[r3], %[lar]" PPOL "\n\tglobal_load_ushort %[r1], %[r3], %[aim]" PPOL "\n\t"
      "global_load_dword %[r2], %[r2], %[oth]\n\t"
      "global_load_ushort %[r2], %[r3], %[cre]" PPOL "\n\tglobal_load_ushort %[r3], %[r3], %[cim]" PPOL "\n\t"
      "s_load_dword %[tg], %[ws], 0\n\ts_load_dword %[tl], %[ldt], %[td]\n\ts_load_dword %[td], %[dsk], %[td]\n1:"
      : [lar] "=&s"(a_lar), [aim] "=&s"(a_aim), [cre] "=&s"(a_cre), [cim] "=&s"(a_cim), [ldt] "=&s"(a_ldt), [dsk] "=&s"(a_dsk), [ws] "=&s"(a_ws),
        [oth] "=&s"(a_oth), [wv] "=&s"(wave), [tg] "=&s"(ctag), [tl] "=&s"(t_ldt), [td] "=&s"(t_dsk),
        [r0] "=&v"(r_lar), [r1] "=&v"(r_aim), [r2] "=&v"(r_cre), [r3] "=&v"(r_cim)
      : [ka] "s"(__builtin_amdgcn_kernarg_segment_ptr()), [tid] "v"(threadIdx.x), [b] "s"(blockIdx.x), [pp] "i"(PPAIR) : "scc");
  SB();
  const u32 w = wave & 3;
  const u32 b = blockIdx.x, xcd = b & 7, bi = b >> 3;
  const u32 hf = XMAP ? (xcd ^ (xcd >> 2)) & 1 : xcd & 1, p = 4 * (XMAP ? xcd & 3 : xcd >> 1) + (bi >> 3), jj = bi & 7;
  const bool spare = jj == 7;
  const u32 cb1 = 7 * hf + jj, rb = 2 * p + hf, pair = b;
  const u32 wm = w >> 1, wn = w & 1;
#define LANES() const u32 tid = opq(threadIdx.x) & 255, lane = tid & 63, hh = lane >> 5, blk = (lane >> 4) & 1, q4 = (lane & 15) >> 2, p4 = lane & 3, g4 = lane >> 4, l16 = opq(lane * 16)

  if (wave >= 4) {
    u32x16 k0;
    u32x4 k1;
    asm volatile("s_load_dwordx16 %0, %2, 0\n\ts_load_dwordx4 %1, %2, 120" : "=&s"(k0), "=&s"(k1) : "s"(__builtin_amdgcn_kernarg_segment_ptr()));
    SB();
    LANES();
    const u32 ta = opq(sb + TAB + tid * 224);
    const u32 t0 = opq(sb + TAB + (wm * 128 + 4 * g4 + q4) * 224 + 8 * p4), t1 = opq(t0 + 32 * wn);
    const u32 ko = opq(sb + KD + wm * KD_CH + (4 * g4 * 36 + 16 * wn + (lane & 15)) * 2);
    char* const ws = (char*)a_ws;
    const __amdgpu_buffer_rsrc_t sWS = __builtin_amdgcn_make_buffer_rsrc((void*)ws, 0, WS_END, 0x00020000);
    u32 ga[9];
    {
      auto cell = [&](u32 pos) {
        const u32 hi = pos >> 8, ex = min(3u, 32u - (u32)__clz(hi));
        return sb + KD + (4 * ex + ((8 * hi) >> ex)) * 72 + ((pos & 255) >> 2);
      };
      const u32 P = 2040 - 8 * tid, a0 = cell(P), a8 = cell(P + 8);
      const u32 c0 = w == 3 ? 0x83222100u : w == 2 ? 0x88111000u : 0x88880000u, c1 = w == 3 ? 0x76543210u : w == 2 ? 0x88111000u : 0x88800000u;
      const u32 code = (w == 3 ? hh : (lane & 1)) ? c1 : c0;
#pragma unroll
      for (int k = 0; k < 8; k++) {
        const u32 n = (code >> (4 * k)) & 15u;
        ga[k + 1] = opq(n == 8 ? a8 : a0 + n * 72);
      }
      const u32 lowa = opq(P == 0 ? sb + KD + KD_ZERO : sb + KD + 7 * 72 + ((P - 8) >> 2));
      ga[0] = opq(P > 256 ? a0 : lowa);
    }
    *(LAS u32*)(hh ? sb + CNT + (lane & 15) * 4 : sb + KD + (lane & 1) * KD_CH + KD_ZERO) = 0;
    const u32 to = opq(sb + TC + tid * 16);

    STN(1);
    asm volatile("s_waitcnt lgkmcnt(0)" : "+s"(ctag), "+s"(t_ldt), "+s"(t_dsk), "+s"(k0), "+s"(k1)::"memory");
    asm volatile("s_waitcnt vmcnt(0)" : "+v"(r_lar), "+v"(r_aim), "+v"(r_cre), "+v"(r_cim)::"memory");
    const u32 tag = ctag + 1, cb2 = colblock(jj, k0[8]);
    ST(2);
    {
      const float dt = rbf(__expf(bf2f(wm ? t_ldt >> 16 : t_ldt & 0xffffu)));
      const float er = -(rbf(__expf(bf2f(r_lar))) * dt) * 1.44269504088896f;
      const float wi = (bf2f(r_aim) * dt) * 0.159154943091895f;
      auto seed = [&](float t) {
        const float m = __builtin_amdgcn_exp2f(er * t), a = __builtin_amdgcn_fractf(wi * t);
        return f32x2{m * __builtin_amdgcn_cosf(a), m * __builtin_amdgcn_sinf(a)};
      };
      auto put8 = [&](const f32x2* v, u32 off) {
        u32x4 re = {pk2(v[0].x, v[1].x), pk2(v[2].x, v[3].x), pk2(v[4].x, v[5].x), pk2(v[6].x, v[7].x)};
        u32x4 im = {pk2(v[0].y, v[1].y), pk2(v[2].y, v[3].y), pk2(v[4].y, v[5].y), pk2(v[6].y, v[7].y)};
        *(LAS u32x4*)((LAS char*)ta + off) = re;
        *(LAS u32x4*)((LAS char*)ta + 112 + off) = im;
      };
      f32x2 v[8];
      const Mul w8 = mk(seed(8.f)), w64 = mk(seed(64.f));
      v[0] = f32x2{1.f, 0.f}; v[1] = w8.b; v[2] = cmul(v[1], w8);
      const Mul w16 = mk(v[2]);
#pragma unroll
      for (int k = 3; k < 8; k++) v[k] = cmul(v[k - 2], w16);
      put8(v, 0); SB();
      v[0] = w64.b;
#pragma unroll
      for (int r = 1; r < 4; r++) {
#pragma unroll
        for (int k = (r == 1); k < 8; k++) v[k] = cmul(v[k], w64);
        put8(v, 16 * r); SB();
      }
      f32x2 s1 = seed(1.f), s256 = seed(256.f);
      s1.y = -s1.y; s256.y = -s256.y;
      const Mul v1 = mk(s1), W = mk(s256), v2c = mk(cmul(s1, v1));
      f32x2 u[8];
      u[0] = f32x2{2.f * (bf2f(r_cre) * dt), -2.f * (bf2f(r_cim) * dt)};
      u[1] = cmul(u[0], v1);
#pragma unroll
      for (int k = 2; k < 8; k++) u[k] = cmul(u[k - 2], v2c);
      put8(u, 64); SB();
      v[0] = cmul(u[0], W); v[1] = cmul(u[2], W); v[2] = cmul(u[4], W); v[3] = cmul(u[6], W);
      v[4] = cmul(v[0], W); v[5] = cmul(v[2], W); v[6] = cmul(v[4], W); v[7] = cmul(v[5], W);
      u[0] = cmul(v[6], W); u[1] = cmul(u[0], W); u[2] = cmul(u[1], W); u[3] = cmul(u[2], W); u[4] = cmul(u[3], W);
      put8(v, 80);
      u[5] = u[6] = u[7] = f32x2{0.f, 0.f};
      put8(u, 96);
      ST(3);
      BAR();
      ST(14);
      f32x4 ka[2] = {{(lane == 0 && wn == 0) ? bf2f(wm ? t_dsk >> 16 : t_dsk & 0xffffu) : 0.f, 0.f, 0.f, 0.f}, {0.f, 0.f, 0.f, 0.f}};
      u32x4 fv[8], f0[8], f1[8];
#pragma unroll
      for (int ks = 0; ks < 10; ks++) {
        const u32 o = ks * 16 * 224;
        if (ks < 8) { fv[ks] = ldstr(t1, o, o + 112); f0[ks] = ldstr(t0, o + 64, o + 64 + 112); f1[ks] = ldstr(t0, o + 96, o + 96 + 112); }
        if (ks >= 2) { ka[0] = MF16(f0[ks - 2], fv[ks - 2], ka[0]); ka[1] = MF16(f1[ks - 2], fv[ks - 2], ka[1]); }
        SB();
      }
#pragma unroll
      for (int rt = 0; rt < 2; rt++)
#pragma unroll
        for (int r = 0; r < 4; r += 2) {
          const u32 pr = pk2(ka[rt][r], ka[rt][r + 1]);
          *(LAS u16*)((LAS char*)ko + (16 * rt + r) * 72) = (u16)pr;
          *(LAS u16*)((LAS char*)ko + (16 * rt + r + 1) * 72) = (u16)(pr >> 16);
        }
    }
    ST(9);
    BAR();
    ST(4);
    const u32 ufo = opq(WS_UF + (pair >> 5) * 128 + (lane & 31) * 4);
    const u32 ua = opq(sb + US + wm * US_STR + (256 - 16 - 224 + 16 * (lane & 15) + 8 * g4) * 2);
    u32 tb[4];
#pragma unroll
    for (int j = 0; j < 4; j++) tb[j] = opq(sb + TC + (2 * wm + (lane & 1)) * TC_STR + (2046 - 16 - 224 - 1792 + 8 * g4 - (lane & 14)) * 2 + 1024 * j);
    const u32 cw = opq(sb + CNT + 4 * w), cr = opq(sb + CNT + 4 * (lane & 3));
    auto arrive = [&](u32 n) { *(volatile LAS u32*)cw = n; };
    auto meet = [&](u32 n) { LGKM0(); arrive(n); lds_wait(cr, n); };
    if (!spare) BAR();
    const u32 row = tid >> 3, ck = lane & 7;
    const u32 ao = opq(WS_AT + (2 * pair + wm) * 4096 + (g4 + ((lane & 8) ? 0 : 4)) * 128 + (lane & 7) * 16);
    const u32 sa = opq(sb + 2 * SLOT + ck * 1024 + ((row ^ (2 * ck)) << 4));
    if (!spare) BAR();
    const u32 ato = opq(WS_AT + (64 * cb2 + (lane >> 3)) * 4096 + rb * 128 + (lane & 7) * 16);
    const u32 afo = opq(AWF ? WS_AF + (((rb >> 3) ^ (rb >> 4)) & 1) * 2048 + cb2 * 256 + lane * 4 : WS_AF + cb2 * 128 + (lane & 31) * 4);
    const u32 go = opq(WS_G + rb * 512 + row * 16);
    const u32 oo = opq((64 * rb + row) * 1024 + cb2 * 128 + ck * 16);
    const u32 ar = opq(sb + TC + ck * 1040 + (row & 7) * 16 + (row & 8) + (row >> 4) * 4);
    const u32 zo = opq(wn ? sb + US + hh * US_STR + (lane & 31) * 16 : sb + TC + ((lane >> 1) & 3) * TC_STR + 4096 + (lane & 1) * 16);
    auto fetch_u = [&]() {
#pragma unroll
      for (int q = 0; q < 8; q++) {
        if (!(q & 3)) SETM0(sb, US + 512 + (q >> 2) * US_STR);
        DMAP(" sc1", l16, sWS, WS_UT + (2 * pair + (q >> 2)) * UTS, q & 3);
      }
    };
    bool got = w != 0;
    if (!spare) {
      BAR(); BAR(); BAR();
      if (!UEARLY) BAR();
      if (got) { BAR(); if (TORD && UEARLY) BAR(); }
      else {
        u32 uf;
        if (TORD && UEARLY) asm volatile("buffer_load_dword %0, %1, %2, 0 offen sc1\n\ts_barrier\n\ts_barrier\n\ts_waitcnt vmcnt(0)" : "=&v"(uf) : "v"(ufo), "s"(sWS) : "memory");
        else asm volatile("buffer_load_dword %0, %1, %2, 0 offen sc1\n\ts_barrier\n\ts_waitcnt vmcnt(0)" : "=&v"(uf) : "v"(ufo), "s"(sWS) : "memory");
        got = __all(uf == tag);
        if (got) fetch_u();
      }
      if (UEARLY) BAR();
      if (TORD && !UEARLY) BAR();
    }
    __builtin_amdgcn_s_setprio(CVPRIO);
    ST(21);
    {
      u32 ek[2][9];
#pragma unroll
      for (int i = 0; i < 9; i++) { ek[0][i] = *(const LAS u16*)ga[i]; ek[1][i] = *(const LAS u16*)((LAS char*)ga[i] + KD_CH); }
      *(LAS u32x4*)zo = u32x4{0, 0, 0, 0};
#pragma unroll
      for (int i = 0; i < 4; i++) {
        const u32* e = ek[i >> 1] + (i & 1);
        *(LAS u32x4*)((LAS char*)to + i * TC_STR) = u32x4{e[7] | (e[6] << 16), e[5] | (e[4] << 16), e[3] | (e[2] << 16), e[1] | (e[0] << 16)};
      }
    }
    if (!got) {
      __builtin_amdgcn_s_setprio(3);
      if (spare) __builtin_amdgcn_s_sleep(SPWAIT);
      _Pragma("unroll 1") for (u32 spin = 0; spin < SPIN_CAP; spin++) {
        u32 uf;
        asm volatile("buffer_load_dword %0, %1, %2, 0 offen sc1\n\ts_waitcnt vmcnt(0)" : "=&v"(uf) : "v"(ufo), "s"(sWS) : "memory");
        if (__all(uf == tag)) break;
        __builtin_amdgcn_s_sleep(USLEEP);
      }
      fetch_u();
      __builtin_amdgcn_s_setprio(CVPRIO);
    }
    VMCNT(0);
    ST(6);
    meet(4);
    ST(7);

    auto conv = [&](auto hi_c) {
      constexpr int H = decltype(hi_c)::v, LO = 8 - H, NR = 3 * (H + 1);
      f32x4 ca[4] = {};
      u32x4 fu[2][8];
      u32x2 ft[2][16];
      auto cread = [&](int f, int r) {
        if (r >= 2 && r < H + 3) fu[f & 1][H + 2 - r] = lds128(ua, (224 - 32 * f + 256 * (H + 2 - r)) * 2);
        else if (r < NR) {
          const int k = r < 2 ? r : r - H - 1, i = k >> 1, hl = k & 1, e = 7 - i;
          const LAS u32* t = (const LAS u32*)((LAS char*)tb[e >> 1] + (224 - 32 * f + 256 * (e & 1)) * 2 + 8 * hl);
          ft[f & 1][k] = u32x2{t[0], t[1]};
        }
      };
#pragma unroll
      for (int r = 0; r < NR; r++) { cread(0, r); SB(); }
      sfor<0, 8>([&](auto fc) {
        constexpr int f = decltype(fc)::v;
        int m = 0;
#pragma unroll
        for (int i = 0; i <= H; i++)
#pragma unroll
          for (int j = 0; j < 4; j++) {
            const int c = j < 2 ? H - j : LO + 2 - j;
            if (c < i) continue;
            const u32x4 tf = {ft[f & 1][2 * i][0], ft[f & 1][2 * i][1], ft[f & 1][2 * i + 1][0], ft[f & 1][2 * i + 1][1]};
            ca[j] = MF16(fu[f & 1][c - i], tf, ca[j]);
            if (f < 7) {
              if (H == 7) { cread(f + 1, m + (m + 1) / 2); if (!(m & 1)) cread(f + 1, m + m / 2 + 1); }
              else { cread(f + 1, m + (m + 3) / 4); if (!(m & 3)) cread(f + 1, m + m / 4 + 1); }
            }
            m++;
            SB();
          }
      });
      __builtin_amdgcn_s_setprio(3);
      ST(8);
#pragma unroll
      for (int j = 0; j < 4; j += 2) {
        u32x4 o;
#pragma unroll
        for (int r = 0; r < 2; r++) {
          const u32 up = pk2(ca[j][r], ca[j][r + 2]), lo = pk2(ca[j + 1][r], ca[j + 1][r + 2]);
          o[r] = __builtin_amdgcn_update_dpp(up, lo, 0x128, 0xf, 0xc, false);
          o[2 + r] = __builtin_amdgcn_update_dpp(lo, up, 0x128, 0xf, 0x3, false);
        }
        __builtin_amdgcn_raw_buffer_store_b128(o, sWS, ao + ((j ? LO : H) - 1) * 512, 0, 16);
      }
    };
    if (wn) conv(IC<5>{}); else conv(IC<7>{});
    ST(11);
    VMCNT(0);
    arrive(7);
    if (AWF) {
      if (lane == 0) __builtin_amdgcn_raw_buffer_store_b32(tag, sWS, WS_AF + wn * 2048 + (2 * pair + wm) * 4, 0, 16);
      if (w == 0) lds_wait(cr, 7);
    } else if (w == 0) {
      lds_wait(cr, 7);
      if (lane == 0) __builtin_amdgcn_raw_buffer_store_b32(tag, sWS, WS_AF + pair * 4, 0, 16);
    }
    ST(20);
    const __amdgpu_buffer_rsrc_t sB2 = __builtin_amdgcn_make_buffer_rsrc((void*)mk64(k1[0], k1[1]), 0, D * 2, 0x00020000);
    const __amdgpu_buffer_rsrc_t sOut = __builtin_amdgcn_make_buffer_rsrc((void*)mk64(k0[14], k0[15]), 0, L * D * 2, 0x00020000);
    const u32x4 b2v = __builtin_amdgcn_raw_buffer_load_b128(sB2, ck * 16, cb2 * 128, 0);
    if (b == 0 && w == 3) {
      _Pragma("unroll 1") for (u32 spin = 0; spin < SPIN_CAP; spin++) {
        asm volatile("" ::: "memory");
        const u32x4 f0 = __builtin_amdgcn_raw_buffer_load_b128(sWS, WS_UF + l16, 0, 16);
        if (__all(f0[0] == tag && f0[1] == tag && f0[2] == tag && f0[3] == tag)) break;
      }
      if (lane == 0) __builtin_amdgcn_raw_buffer_store_b32(tag, sWS, 0, 0, 16);
    }
    u32x4 gg;
    _Pragma("unroll 1") for (u32 spin = 0; spin < SPIN_CAP; spin++) {
      asm volatile("" ::: "memory");
      gg = __builtin_amdgcn_raw_buffer_load_b128(sWS, go, 0, 16);
      if (__all(gg[1] == tag && gg[3] == tag)) break;
      __builtin_amdgcn_s_sleep(RSLEEP);
    }
    if (w == 0) {
      lds_wait<LSL>(cr + RDY - CNT, 1);
      ST(12);
      _Pragma("unroll 1") for (u32 spin = 0; spin < SPIN_CAP; spin++) {
        u32 af;
        asm volatile("buffer_load_dword %0, %1, %2, 0 offen sc1\n\ts_waitcnt vmcnt(0)" : "=&v"(af) : "v"(afo), "s"(sWS) : "memory");
        if (__all(af == tag)) break;
        __builtin_amdgcn_s_sleep(RSLEEP);
      }
      ST(13);
#pragma unroll
      for (int j = 0; j < 8; j++) { SETM0(sb, TC + j * 1040); DMAP(" sc1", ato, sWS, j * 32768, 0); }
    }
    float ffn[8];
    f32x4 pt[4][2];
    auto gather = [&](int t) {
#pragma unroll
      for (int k = 0; k < 4; k++)
#pragma unroll
        for (int q = 0; q < 2; q++) pt[k][q] = *(const LAS f32x4*)((LAS char*)sa + (1 - t) * SLOT + k * 8192 + q * 512);
    };
    const float g0 = bf2f(gg[0] & 0xffffu), gm0 = hi2f(gg[0]), g1 = bf2f(gg[2] & 0xffffu), gm1 = hi2f(gg[2]);
    auto half = [](u32 v, int e) { return e ? hi2f(v) : bf2f(v & 0xffffu); };
    lds_wait<LSL>(cr + RDY - CNT, 2);
    gather(0);
#pragma unroll
    for (int i = 0; i < 8; i++) ffn[i] = fopq(sum4(gm0, pt[0][i >> 2][i & 3], pt[1][i >> 2][i & 3], pt[2][i >> 2][i & 3], pt[3][i >> 2][i & 3], mul1(gm0, half(b2v[i >> 1], i & 1))));
    ST(16);
    if (w == 0) {
      VMCNT(0);
      *(volatile LAS u32*)(sb + GO + 4) = 1;
    } else lds_wait<LSL2>(sb + GO + 4, 1);
    ST(15);
    u32 a2[8];
#pragma unroll
    for (int i = 0; i < 8; i++) a2[i] = *(const LAS u32*)((LAS char*)ar + i * 128);
    u32x4 ow;
#pragma unroll
    for (int i = 0; i < 4; i++) ow[i] = pk2(fma1(g0, half(a2[2 * i], 0), ffn[2 * i]), fma1(g0, half(a2[2 * i + 1], 0), ffn[2 * i + 1]));
    __builtin_amdgcn_raw_buffer_store_b128(ow, sOut, oo, 0, OUTPOL);
#pragma unroll
    for (int i = 0; i < 8; i++) ffn[i] = fopq(fma1(gm1, half(b2v[i >> 1], i & 1), mul1(g1, half(a2[i], 1))));
    ST(18);
    asm volatile("s_barrier" ::: "memory");
    ST(19);
    gather(1);
#pragma unroll
    for (int i = 0; i < 8; i++) ffn[i] = __builtin_fmaf(gm1, (pt[0][i >> 2][i & 3] + pt[1][i >> 2][i & 3]) + (pt[2][i >> 2][i & 3] + pt[3][i >> 2][i & 3]), ffn[i]);
#pragma unroll
    for (int i = 0; i < 4; i++) ow[i] = pk2(ffn[2 * i], ffn[2 * i + 1]);
    __builtin_amdgcn_raw_buffer_store_b128(ow, sOut, oo + 32768, 0, OUTPOL);
    asm volatile(".Lcode_end:");
    STAMP_OUT();
    return;
  }

  u32x16 k0;
  u32x4 k1;
  asm volatile("s_load_dwordx16 %0, %2, 0\n\ts_load_dwordx4 %1, %2, 112"
               : "=&s"(k0), "=&s"(k1) : "s"(__builtin_amdgcn_kernarg_segment_ptr()));
  SB();
  LANES();
  const u32 o1 = opq(((spare ? 0 : cb1) * 128 + wn * 64 + (lane & 31)) * 2);
  u32 vA[4], vB[4], v2[4];
#pragma unroll
  for (int q = 0; q < 4; q++) {
    vA[q] = opq((lane >> 3) * 1024 + (((lane & 7) ^ g4 ^ (4 * (q & 1))) << 4) + q * (8192 - 1024));
    vB[q] = opq(g4 * (F * 2) + (((lane & 15) ^ (g4 << 2) ^ q) << 4) + q * (4 * F * 2 - 1024));
    v2[q] = opq((lane >> 3) * 1024 + (((lane & 7) ^ (blk << 2)) << 4) + q * (8192 - 1024));
  }
  const u32 xbase = sopq((128 * p + 32 * w) * 1024), wbase = sopq(16 * w * (F * 2) + cb1 * 256);
  const u32 m4 = sopq(sb + w * 4096), m8 = sopq(sb + w * 8192), m2 = sopq(sb + w * 2048);
  const u32 vs = opq((lane >> 3) * 1024 + (((lane & 7) ^ (2 * w)) << 4)), vs1 = opq((vs ^ 16) + 8192 - 1024);
  const u32 w2s = sopq((7 * b + min(2 * w, 5u)) * 1024);
  const u32 uxo = sopq((64 * rb + 16 * w) * 1024 + jj * 128), w1s = sopq(((bi * 4 + w) * 4 + (p >> 2)) * (F * 2) + hf * F + 768);
  asm volatile("s_waitcnt lgkmcnt(0)" : "+s"(k0), "+s"(k1) : "s"(a_lar), "s"(a_aim), "s"(a_cre), "s"(a_cim), "s"(a_ldt), "s"(a_dsk), "s"(a_ws));
  ST(14);
  const u32x4 sX = k0.s0123, sW1 = k0.s4567;
  const u32x4 sW2 = k0.s89ab;
  char* const ws = (char*)mk64(k0[12], k0[13]);
  const u32 w2base = 28 * w * 16384 + colblock(jj, sW2[0]) * 128;
  u32 tag;
  const __amdgpu_buffer_rsrc_t sWS = __builtin_amdgcn_make_buffer_rsrc((void*)ws, 0, WS_END, 0x00020000);
  auto chunk = [&](int c) { const u32 r = c + ROT2; return r >= 7 ? r - 7 : r; };
  auto w2piece = [&](int c, int q) {
    if ((q & 3) == 0) SETM0(m8, (c & 3) * SLOT + (q >> 2) * 4096);
    DMAQ(v2[q & 3], sW2, w2base + chunk(c) * 65536 + (q >> 2) * 32768, q & 3);
  };
  constexpr u32 UST = 2 * SLOT + 16384;
  SETM0(m2, UST);
  DMAQ(vs, sX, uxo, 0); DMAQ(vs1, sX, uxo, 1);
  SETM0(m2, UST + 8192);
  if (SH2 == 1) { DMAQ(l16, sW2, w2s, 0); DMAQ(l16, sW2, w2s, 1); }
  else { DMAQ(l16, sW1, NOSH ? 1u << 30 : w1s - 768, 0); DMAQ(l16, sW1, NOSH ? 1u << 30 : w1s, 0); }
  auto send_u = [&]() {
    const u32 ur = sb + UST + (lane & 7) * 1024 + (((lane >> 3) ^ (lane & 7)) << 4) + 4 * w;
    const u32 uw = WS_UT + (jj * 64 + (lane >> 3) * 8 + 2 * w) * UTS + rb * 128 + (lane & 7) * 16;
    u32 xr[8];
#pragma unroll
    for (int j = 0; j < 8; j++) xr[j] = *(const LAS u32*)((LAS char*)ur + j * 128);
#pragma unroll
    for (int c = 0; c < 2; c++) {
      u32x4 ou;
#pragma unroll
      for (int e = 0; e < 4; e++) ou[e] = __builtin_amdgcn_perm(xr[2 * e + 1], xr[2 * e], c ? 0x07060302 : 0x05040100);
      __builtin_amdgcn_raw_buffer_store_b128(ou, sWS, uw + c * UTS, 0, 16);
    }
    SB();
  };
  auto pub_u = [&](u32 vtag, u32 vuf) {
    asm volatile("s_cmp_lg_u32 %3, 0\n\ts_cbranch_scc1 1f\n\ts_mov_b64 exec, 1\n\tbuffer_store_dword %0, %1, %2, 0 offen sc1\n\ts_mov_b64 exec, -1\n1:"
                 ::"v"(vtag), "v"(vuf), "s"(sWS), "s"(w) : "memory", "scc");
  };

  if (!spare) {
    auto g1piece = [&](int g, int q) {
      if (q == 0) SETM0(m4, (g & 3) * SLOT);
      if (q == 4) SETM0(m4, (g & 3) * SLOT + 16384);
      if (q < 4) DMAP(XPOL, vA[q], sX, xbase + ((g + ROT1) & 7) * 128, q);
      else DMAQ(vB[q - 4], sW1, wbase + ((g + ROT1) & 7) * 64 * (F * 2), q - 4);
    };
    u32 r_b1a, r_b1b;
    asm volatile("s_load_dword %0, %1, 0" : "=s"(tag) : "s"(ws));
    asm volatile("global_load_ushort %0, %1, %2" : "=v"(r_b1a) : "v"(o1), "s"(mk64(k1[0], k1[1])));
    asm volatile("global_load_ushort %0, %1, %2 offset:64" : "=v"(r_b1b) : "v"(o1), "s"(mk64(k1[0], k1[1])));
#pragma unroll
    for (int i = 0; i < 8; i++) g1piece(0, i);
    ST(1);
    u32 ax[2][4], aw[2][4];
    {
      const u32 xa = sb + (64 * wm + (lane & 31)) * 128 + ((hh ^ ((lane >> 1) & 7)) << 4);
      const u32 wa = sb + 16384 + 256 * (8 * hh + q4) + 8 * (p4 & 1) + (((8 * wn + 2 * blk + (p4 >> 1)) ^ ((q4 << 2) | (2 * hh))) << 4);
#pragma unroll
      for (int k = 0; k < 4; k++) {
        ax[0][k] = opq(xa ^ (k << 5)); aw[0][k] = opq(wa ^ ((k & 1) << 4) ^ ((k >> 1) << 6));
        ax[1][k] = opq(ax[0][k] + 2 * SLOT); aw[1][k] = opq(aw[0][k] + 2 * SLOT);
      }
    }
    const u32 ho = opq(l16 + WS_H + ((4 * p + 2 * wm) * HROW + cb1 * 8 + wn * 4) * 1024), ho1 = opq(ho + HROW * 1024);
    asm volatile("s_waitcnt lgkmcnt(0)" : "+s"(tag));
    const u32 vtag = opq(tag + 1), vuf = opq(WS_UF + jj * 128 + rb * 4);
#pragma unroll
    for (int q = 0; q < PRET; q++) g1piece(1, q);
    asm volatile("s_waitcnt vmcnt(%2)\n\ts_barrier" : "+v"(r_b1a), "+v"(r_b1b) : "i"(8 + PRET) : "memory");
    ST(3);
#pragma unroll
    for (int q = PRET; q < 8; q++) g1piece(1, q);
    send_u();
#pragma unroll
    for (int q = 0; q < 4; q++) g1piece(2, q);
    f32x16 acc[2][2];
    {
      const u32x4 one = {hh ? 0u : 0x3f80u, 0u, 0u, 0u};
      const u32x4 ba = {r_b1a, 0u, 0u, 0u}, bb = {r_b1b, 0u, 0u, 0u};
      asm volatile("s_nop 1\n\tv_mfma_f32_32x32x16_bf16 %0, %4, %6, 0\n\tv_mfma_f32_32x32x16_bf16 %1, %4, %6, 0\n\t"
                   "v_mfma_f32_32x32x16_bf16 %2, %5, %6, 0\n\tv_mfma_f32_32x32x16_bf16 %3, %5, %6, 0"
                   : "=&v"(acc[0][0]), "=&v"(acc[0][1]), "=&v"(acc[1][0]), "=&v"(acc[1][1]) : "v"(ba), "v"(bb), "v"(one));
    }
    u32x4 fx[2][2];
    s16x4 fw[2][4];
    VMCNT(14);
    ST(2);
    BAR();
    ST(4);
    fx[0][0] = lds128(ax[0][0]); fw[0][0] = tr64(aw[0][0], 0); fw[0][1] = tr64(aw[0][1], 1024);
    fx[0][1] = lds128(ax[0][0], 4096); fw[0][2] = tr64(aw[0][2], 0); fw[0][3] = tr64(aw[0][3], 1024);
    u32x4 tx[2][8];
    s16x4 tw[4][8];
    auto tread = [&](int r0) {
      const int r = !TORD ? r0 : r0 < 12 ? r0 : r0 < 16 ? r0 + 12 : r0 < 24 ? r0 + 16 : r0 < 36 ? r0 - 12 : r0 < 40 ? r0 - 8 : r0;
      if (r < 24) {
        const int ks = r / 3, j = r % 3;
        if (j == 2) tx[0][ks] = lds128(ax[1][ks & 3], (ks >> 2) * SLOT);
        else tw[j][ks] = tr64(aw[1][j], (ks >> 2) * SLOT + (ks & 3) * 4096 + j * 1024);
      } else if (r < 32) tx[1][r - 24] = lds128(ax[1][r & 3], ((r - 24) >> 2) * SLOT + 4096);
      else if (r < 48) { const int ks = (r - 32) >> 1, j = r & 1; tw[2 + j][ks] = tr64(aw[1][2 + j], (ks >> 2) * SLOT + (ks & 3) * 4096 + j * 1024); }
    };
#pragma unroll
    for (int s = 0; s < 6; s++) {
      auto one = [&](int i) { if (i < 64) g1piece(i >> 3, i & 7); else w2piece((i - 64) >> 3, i & 7); };
      auto piece = [&](int q) {
        if (s >= NC) one(20 + 2 * NC + 8 * s + q);
        else { const int i = 20 + 10 * s + q + (q > 1) + (q > 4); one(i); if (q == 1 || q == 4) one(i + 1); }
      };
#pragma unroll
      for (int k = 0; k < 4; k++) {
        const int c = k & 1, n = c ^ 1, kn = (k + 1) & 3, sl = (k == 3 ? s + 1 : s) & 3, hi = sl >> 1;
        const u32 o = (sl & 1) * SLOT, ow = o + kn * 4096;
        const bool more = s < 5 || k < 3;
        MMV(acc[0][0], cat(fw[c][0], fw[c][1]), fx[c][0]);
        if (k == 3) { VMCNT(s == 0 ? 12 + (UPB ? 2 : 0) : (s < NC ? 12 + 2 * s : 10 + 2 * NC) - (s == 5 && !TORD ? 8 : 0)); BAR(); }
        if (more) { fx[n][0] = lds128(ax[hi][kn], o); fw[n][0] = tr64(aw[hi][0], ow); fw[n][1] = tr64(aw[hi][1], ow + 1024); }
        else { tread(0); tread(1); tread(2); }
        MMV(acc[0][1], cat(fw[c][0], fw[c][1]), fx[c][1]);
        if (more) { fx[n][1] = lds128(ax[hi][kn], o + 4096); fw[n][2] = tr64(aw[hi][2], ow); }
        else { tread(3); tread(4); tread(5); }
        piece(2 * k);
        MMV(acc[1][0], cat(fw[c][2], fw[c][3]), fx[c][0]);
        if (more) fw[n][3] = tr64(aw[hi][3], ow + 1024); else { tread(6); tread(7); tread(8); }
        MMV(acc[1][1], cat(fw[c][2], fw[c][3]), fx[c][1]);
        if (!more) { tread(9); tread(10); tread(11); }
        piece(2 * k + 1);
      }
      if (s == UPB) pub_u(vtag, vuf);
      if (s == 0) ST(8);
      if (s == 1) ST(15);
      if (s == 2) ST(16);
      if (s == 3) ST(17);
      if (s == 4) ST(19);
      if (s == 5) ST(20);
    }
    __builtin_amdgcn_s_setprio(2);
    u32x4 ho4;
    auto square = [&](int u, int t, int i) {
      ho4[i & 3] = relu2(acc[u][t][2 * i], acc[u][t][2 * i + 1]);
      if ((i & 3) == 3) __builtin_amdgcn_raw_buffer_store_b128(ho4, sWS, (t ? ho1 : ho) + (2 * u + (i >> 2)) * 1024, 0, 16);
    };
    constexpr int E0 = TORD ? 14 : 10;
    auto unit = [&](auto ic) {
      constexpr int e = decltype(ic)::v, pp = e >> 3, pu = pp >> 1, pt = (pp ^ pu) & 1;
      if ((e & 7) == 0) asm volatile("" : "+v"(acc[pu][pt]));
      square(pu, pt, e & 7);
    };
    sfor<0, 32>([&](auto ic) {
      constexpr int m = decltype(ic)::v, ph = TORD && m < 16 ? (m >> 2) & 1 : m >> 3, ks = TORD && m < 16 ? (m & 3) + 4 * (m >> 3) : m & 7;
      constexpr int u = ph >> 1, t = (ph ^ u) & 1;
      MMV(acc[u][t], cat(tw[2 * u][ks], tw[2 * u + 1][ks]), tx[t][ks]);
      if (TORD && m == 4) { VMCNT(4 + 2 * NC + 4); BAR(); }
      if (m < 8) { tread(12 + 3 * m); tread(13 + 3 * m); tread(14 + 3 * m); }
      else if (m < 14) { tread(20 + 2 * m); tread(21 + 2 * m); }
      if (m + 2 * NC - 4 < 8) w2piece(1, m + 2 * NC - 4);
      if (m == TBAR) BAR();
      if constexpr (m >= E0) unit(IC<m - E0>{});
      SB();
    });
    ST(5);
    sfor<32 - E0, 24>(unit);
    asm volatile("s_nop 7" : "+v"(acc[1][0]));
    sfor<24, 32>(unit);
    tag += 1;
  } else {
    asm volatile("s_load_dword %0, %1, 0" : "=s"(tag) : "s"(ws));
    if (PFIRST) { SETM0(m8, SLOT); for (int n = 0; n < PFIRST; n++) for (int q = 0; q < 4; q++) DMAQ(l16, sW2, min(14 * xcd + 4 * (3 - n) + (bi >> 3), 111u) * 16384 + w * 4096 + q * 1024, 0); }
    u32x4 gwv, xr[16];
    asm volatile("global_load_dwordx4 %0, %1, %2" : "=v"(gwv) : "v"(l16), "s"(((const CAS Args*)__builtin_amdgcn_kernarg_segment_ptr())->gw));
#pragma unroll
    for (int j = 0; j < 16; j++) {
      if (j == 7 - 4 * PFIRST) { asm volatile("s_waitcnt vmcnt(8)\n\ts_barrier" ::: "memory"); send_u(); }
      asm volatile("buffer_load_dwordx4 %0, %1, %2, %3 offen offset:%4" : "=v"(xr[j]) : "v"(l16), "s"(sX), "s"((64 * rb + 16 * w + (j & ~3)) * 1024), "i"((j & 3) * 1024));
    }
    asm volatile("s_waitcnt vmcnt(%1) lgkmcnt(0)\n\ts_barrier" : "+s"(tag) : "i"(9 + 4 * PFIRST) : "memory");
    tag += 1;
    pub_u(tag, WS_UF + jj * 128 + rb * 4);
    ST(2);
    asm volatile("s_waitcnt vmcnt(0)" : "+v"(gwv), "+v"(xr[0]), "+v"(xr[1]), "+v"(xr[2]), "+v"(xr[3]), "+v"(xr[4]), "+v"(xr[5]), "+v"(xr[6]), "+v"(xr[7]),
                 "+v"(xr[8]), "+v"(xr[9]), "+v"(xr[10]), "+v"(xr[11]), "+v"(xr[12]), "+v"(xr[13]), "+v"(xr[14]), "+v"(xr[15])::"memory");
    SETM0(m8, SLOT);
    auto pull = [&](u32 n) {
#pragma unroll
      for (int q = 0; q < 4; q++) DMAQ(l16, sW2, min(14 * xcd + 4 * n + (bi >> 3), 111u) * 16384 + w * 4096, q);
    };
    __builtin_amdgcn_s_sleep(PFD2);
    if (!SH2) { pull(0); pull(1); }
    ST(8);
    const u32 zs = m8 + 3 * SLOT;
#pragma unroll
    for (int j = 0; j < 16; j++) {
      float z = 0.f;
#pragma unroll
      for (int e = 0; e < 4; e++) z = dot2(xr[j][e], gwv[e], z);
      *(LAS float*)((LAS char*)(zs + lane * 4) + j * 272) = z;
    }
    if (!SH2) { VMCNT(4); pull(2); }
    LGKM0();
    f32x4 z4 = {};
#pragma unroll
    for (int k = 0; k < 4; k++) z4 += *(const LAS f32x4*)((LAS char*)(zs + (lane & 15) * 272 + g4 * 64) + k * 16);
    float zz = (z4[0] + z4[1]) + (z4[2] + z4[3]);
    zz += __shfl_xor(zz, 16); zz += __shfl_xor(zz, 32);
    const float g = rbf(1.f / (1.f + __expf(-zz)));
    if (!SH2 && !PFIRST) { VMCNT(4); pull(3); }
    if (lane < 16) __builtin_amdgcn_raw_buffer_store_b64(u32x2{pk2(g, 1.f - g), tag}, sWS, WS_G + rb * 512 + (wn * 16 + lane) * 16 + wm * 8, 0, 16);
    __builtin_amdgcn_s_setprio(SPRIO);
    ST(4);
#pragma unroll
    for (int i = 0; i < 8 * WPRE; i++) { if (!(i & 3)) VMCNT(THR); w2piece(i >> 3, i & 7); }
  }
  SB();
  ST(6);

  u32 w2a[2][2];
  {
    const u32 a = m8 + 128 * (4 * hh + q4) + 8 * (p4 & 1) + (((2 * blk + (p4 >> 1)) ^ ((q4 >> 1) << 2)) << 4);
    w2a[0][0] = opq(a); w2a[1][0] = opq(a ^ 64); w2a[0][1] = opq(a + 2 * SLOT); w2a[1][1] = opq((a ^ 64) + 2 * SLOT);
  }
  auto w2frag = [&](int st, int v) {
    const int sl = (st >> 2) & 3;
    const u32 o = (sl & 1) * SLOT + (st & 3) * 2048;
    return ldstr(w2a[v][sl >> 1], o, o + 1024);
  };
  u32x4 w2r[12][2];
  u32 fl = 0;
  const u32 fo = opq(WS_HF + hfo(p, ((lane & 7) == 7) ? 0 : (lane & 15)));
  if (XEARLY) {
    asm volatile("s_cmp_lg_u32 %0, 7\n\ts_cbranch_scc1 1f\n\ts_waitcnt vmcnt(0)\n1: s_waitcnt vmcnt(8)" ::"s"(jj) : "memory", "scc");
#pragma unroll
    for (int j = 0; j < 8 * WPRE; j++) w2r[j >> 1][j & 1] = w2frag(j >> 1, j & 1);
    LGKM0();
  }
#pragma unroll
  for (int c = WPRE; c < 8; c++) {
    if (c == WPRE + PUBAT) {
      VMCNT(8 * PUBAT);
      *(volatile LAS u32*)(sb + FD + 4 * w) = 1;
      asm volatile("" ::: "memory");
      if (PUBM == 0) {
        if (__all(*(volatile LAS u32*)(sb + FD + 4 * (lane & 3)) != 0) && !spare && lane == 0)
          asm volatile("buffer_store_dword %0, %1, %2, 0 offen sc1" ::"v"(tag), "v"(WS_HF + hfo(p, 8 * hf + jj)), "s"(sWS) : "memory");
      } else if ((w == 0 || PUBM == 2) && !spare) {
        lds_wait(sb + FD + 4 * (lane & 3), 1);
        if (tid == 0) asm volatile("buffer_store_dword %0, %1, %2, 0 offen sc1" ::"v"(tag), "v"(WS_HF + hfo(p, 8 * hf + jj)), "s"(sWS) : "memory");
      }
      ST(7);
      __builtin_amdgcn_s_sleep(WDELAY);
    }
    if (c >= 4 + (XEARLY ? WPRE : 0) && c < 7) {
      VMCNT(24);
#pragma unroll
      for (int j = 0; j < 8; j++) w2r[4 * (c - 4) + (j >> 1)][j & 1] = w2frag(4 * (c - 4) + (j >> 1), j & 1);
      LGKM0();
    }
    if (c < 7) {
#pragma unroll
      for (int q = 0; q < 8; q++) {
        if (!(q & 3)) asm volatile("s_cmp_lg_u32 %0, 7\n\ts_cbranch_scc1 1f\n\ts_waitcnt vmcnt(%1)\n1:" ::"s"(jj), "i"(THR) : "memory", "scc");
        if (FTHR && !(q & 1)) VMCNT(FTHR);
        if (8 * c + q == POLLP)
          asm volatile("s_cmp_lg_u32 %3, %4\n\ts_cbranch_scc1 1f\n\tbuffer_load_dword %0, %1, %2, 0 offen sc1\n1:" : "+v"(fl) : "v"(fo), "s"(sWS), "s"(w), "i"(PW) : "memory", "scc");
        w2piece(c, q);
        if (8 * c + q == (POLLP + CHK < 55 ? POLLP + CHK : 55)) {
          asm volatile("s_cmp_lg_u32 %1, %2\n\ts_cbranch_scc1 1f\n\ts_waitcnt vmcnt(%3)\n1:" : "+v"(fl) : "s"(w), "i"(PW), "i"(8 * c + q + 1 - POLLP) : "memory", "scc");
          if (w == PW && __all(fl == tag)) *(volatile LAS u32*)(sb + GO) = 1;
        }
      }
    }
  }
  u32 hb[7];
#pragma unroll
  for (int c = 0; c < 7; c++) hb[c] = sopq(WS_H + (2 * rb * HROW + 28 * w + 4 * chunk(c)) * 1024);
  const u32 l16b = opq(l16 + HROW * 1024);
  const u32 xw = opq(m8 + 2 * SLOT + l16), rdw = opq(sb + RDY + 4 * w);
  ST(11);
  if (w == PW) {
    _Pragma("unroll 1") for (u32 spin = 0; spin < SPIN_CAP; spin++) {
      if (__all(fl == tag)) break;
      if (spin) __builtin_amdgcn_s_sleep(HSLEEP);
      if (SPSL && spare) __builtin_amdgcn_s_sleep(SPSL);
      asm volatile("buffer_load_dword %0, %1, %2, 0 offen sc1\n\ts_waitcnt vmcnt(0)" : "=&v"(fl) : "v"(fo), "s"(sWS) : "memory");
    }
    *(volatile LAS u32*)(sb + GO) = 1;
  } else {
    lds_wait<GSL>(sb + GO, 1);
  }
  __builtin_amdgcn_s_setprio(G2PRIO);
  if (WBAL && w >= 2) __builtin_amdgcn_s_setprio(G2PRIO + 1);
  ST(10);
  SB();

  u32x4 hfr[56];
  auto load_h = [&](int i) {
    const int st = step(i);
    hfr[i] = __builtin_amdgcn_raw_buffer_load_b128(sWS, (i < 28 ? l16 : l16b) + (st & 3) * 1024, hb[st >> 2], 16);
  };
#pragma unroll
  for (int i = 0; i < HEAD; i++) load_h(i);
  SB();
  f32x16 oc[2][2] = {};
  auto send = [&](int t, int j) {
    const f32x16& s = oc[t][j >> 2];
    const int r = 4 * (j & 3);
    *(LAS f32x4*)((LAS char*)(xw ^ (j << 5)) + (1 - t) * SLOT + j * 1024) = f32x4{s[r], s[r + 1], s[r + 2], s[r + 3]};
  };
  auto signal = [&](u32 n) { asm volatile("" ::: "memory"); *(volatile LAS u32*)rdw = n; };
  u32x4 wl[56][2];
  auto fetch_w = [&](int i) { if (i < 56 && step(i) >= 12) { wl[i][0] = w2frag(step(i), 0); wl[i][1] = w2frag(step(i), 1); } };
  sfor<0, 56>([&](auto ic) {
    constexpr int i = decltype(ic)::v, t = i / 28, st = step(i);
    constexpr int D2 = 53 - TAILN;
    if (i < D2 || !TAILN) fetch_w(i + 1); else { fetch_w(2 * i - D2 + 1); fetch_w(2 * i - D2 + 2); }
    SB();
    u32x4 a0, a1;
    if (st < 12) { a0 = w2r[st < 12 ? st : 0][0]; a1 = w2r[st < 12 ? st : 0][1]; } else { a0 = wl[i][0]; a1 = wl[i][1]; }
    oc[t][0] = MF32(a0, hfr[i], oc[t][0]);
    if (i + HEAD < 56) load_h(i + HEAD);
    SB();
    if (i < 56 - TAILN) oc[t][1] = MF32(a1, hfr[i], oc[t][1]);
    if (i == ATAT) signal(1);
    if (i >= 31 && i < 39) send(0, i - 31);
    if (i == 38) signal(2);
    SB();
    if (i == 27) ST(18);
  });
#pragma unroll
  for (int i = 0; i < TAILN; i++) {
    oc[1][1] = MF32(wl[56 - TAILN + i][1], hfr[56 - TAILN + i], oc[1][1]);
    SB();
    if (i >= 1 && i < 3) { send(1, 2 * i - 2); send(1, 2 * i - 1); }
    SB();
  }
  ST(12);
#pragma unroll
  for (int j = TAILN >= 3 ? 4 : 0; j < 8; j++) send(1, j);
  BAR();
  STAMP_OUT();
}

extern "C" void kernel_launch(void* const* d_in, const int* in_sizes, int n_in, void* d_out, int out_size,
                              void* d_ws, size_t ws_size, hipStream_t stream) {
  Args a;
  a.x = Srd{d_in[0], L * D * 2, 0x00020000}; a.w1 = Srd{d_in[8], D * F * 2, 0x00020000}; a.w2 = Srd{d_in[10], D * F * 2, 0x00020000};
  a.ws = (char*)d_ws + WSO; a.out = (u16*)d_out;
  a.lar = (const u16*)d_in[1]; a.aim = (const u16*)d_in[2]; a.cre = (const u16*)d_in[3]; a.cim = (const u16*)d_in[4];
  a.ldt = (const u16*)d_in[5]; a.dsk = (const u16*)d_in[6]; a.b1 = (const u16*)d_in[9]; a.b2 = (const u16*)d_in[11];
  a.gw = (const u16*)d_in[12];
  fused_mix<<<256, 512, 0, stream>>>(a);
}
```
